```python
import torch

def _softmax(x, axis=-1):
    return torch.softmax(x, dim=axis)
def _gelu(x):
    return torch.nn.functional.gelu(x, approximate="tanh")
def _silu(x):
    return torch.nn.functional.silu(x)
def _sigmoid(x):
    return torch.sigmoid(x)
def _layernorm(x, axis=-1, eps=1e-5):
    m = x.mean(dim=axis, keepdim=True)
    v = x.var(dim=axis, keepdim=True, unbiased=False)
    return (x - m) / torch.sqrt(v + eps)
def _scatter_add(tgt, idx, src, axis=0):
    return tgt.clone().index_add_(axis, idx, src)
def _segment_sum(src, seg, n):
    out = torch.zeros((n,) + src.shape[1:], dtype=src.dtype)
    return out.index_add_(0, seg, src)
def _pad(x, pw):
    flat = tuple(v for pair in reversed(pw) for v in pair)
    return torch.nn.functional.pad(x, flat)

kernel_name = 'sequential_preLN__sliding_local_dilated__geglu_ffn__prompt'

def setup_inputs():

    _w = (lambda a: (lambda t: t.to(torch.bfloat16) if t.is_floating_point() else t)(torch.as_tensor(a)))
    def _A_setup():
        B, S, D = 1, 2048, 576
        return {
            "x": _w((lambda *s: torch.randn(*s, dtype=torch.bfloat16) * 0.05)(B, S, D)),
            "wqkv": _w((lambda *s: torch.randn(*s, dtype=torch.bfloat16) * 0.05)(D, 3 * D)),
            "wo": _w((lambda *s: torch.randn(*s, dtype=torch.bfloat16) * 0.05)(D, D)),
        }
    _A_in = _A_setup()

    def _B_setup():
        B, S, D, F = 1, 2048, 576, 4608
        return {
            "x": _w((lambda *s: torch.randn(*s, dtype=torch.bfloat16) * 0.05)(B, S, D)),
            "w_gate": _w((lambda *s: torch.randn(*s, dtype=torch.bfloat16) * 0.05)(D, F)),
            "w_up": _w((lambda *s: torch.randn(*s, dtype=torch.bfloat16) * 0.05)(D, F)),
            "w_down": _w((lambda *s: torch.randn(*s, dtype=torch.bfloat16) * 0.05)(F, D)),
        }
    _B_in = _B_setup()

    return {
        "A_x": _A_in["x"],
        "A_wqkv": _A_in["wqkv"],
        "A_wo": _A_in["wo"],
        "B_x": _B_in["x"],
        "B_w_gate": _B_in["w_gate"],
        "B_w_up": _B_in["w_up"],
        "B_w_down": _B_in["w_down"],
    }

def reference(A_x, A_wqkv, A_wo, B_x, B_w_gate, B_w_up, B_w_down):
    def _A_ref(x, wqkv, wo):
        B, S, D = x.shape
        H = 32; Dh = D // H
        W, dil = 8, 2
        qkv = (lambda x, *d: x.permute(*d))((x @ wqkv).reshape(B, S, 3, H, Dh), 2, 0, 3, 1, 4)
        q, k, v = qkv[0], qkv[1], qkv[2]
        scale = Dh ** -0.5
        pos = torch.arange(S)
        off = torch.arange(W) * dil
        idx = pos[:, None] - off[None, :]
        mask = idx >= 0
        idx_c = torch.where(mask, idx, 0).reshape(-1)
        kg = k[:, :, idx_c, :].reshape(B, H, S, W, Dh)
        vg = v[:, :, idx_c, :].reshape(B, H, S, W, Dh)
        logits = torch.einsum("bhsd,bhswd->bhsw", q, kg) * scale
        logits = torch.where(mask[None, None, :, :], logits, -1e9)
        attn = _softmax(logits, axis=-1)
        o = torch.einsum("bhsw,bhswd->bhsd", attn, vg)
        return (lambda x, *d: x.permute(*d))(o, 0, 2, 1, 3).reshape(B, S, D) @ wo

    def _B_ref(x, w_gate, w_up, w_down):
        return (_gelu(x @ w_gate) * (x @ w_up)) @ w_down

    x = A_x
    h = x + _A_ref(_layernorm(x, axis=-1), A_wqkv, A_wo)
    h2 = h + _B_ref(_layernorm(h, axis=-1), B_w_gate, B_w_up, B_w_down)
    return h2
```

```cpp
#include <hip/hip_runtime.h>
#include <hip/hip_bf16.h>
typedef __hip_bfloat16 bf16;
#define S_ 2048
#define D_ 576
#define F_ 4608
__device__ __forceinline__ float b2f(bf16 x){return __bfloat162float(x);}
__device__ __forceinline__ bf16 f2b(float x){return __float2bfloat16(x);}
__global__ void ln_k(const bf16* x, bf16* y){
  int r=blockIdx.x; __shared__ float red[256];
  float s=0; for(int i=threadIdx.x;i<D_;i+=256) s+=b2f(x[r*D_+i]);
  red[threadIdx.x]=s; __syncthreads();
  for(int o=128;o>0;o>>=1){ if(threadIdx.x<o) red[threadIdx.x]+=red[threadIdx.x+o]; __syncthreads();}
  float m=red[0]/D_; __syncthreads();
  float v=0; for(int i=threadIdx.x;i<D_;i+=256){float d=b2f(x[r*D_+i])-m; v+=d*d;}
  red[threadIdx.x]=v; __syncthreads();
  for(int o=128;o>0;o>>=1){ if(threadIdx.x<o) red[threadIdx.x]+=red[threadIdx.x+o]; __syncthreads();}
  float rs=rsqrtf(red[0]/D_+1e-5f);
  for(int i=threadIdx.x;i<D_;i+=256) y[r*D_+i]=f2b((b2f(x[r*D_+i])-m)*rs);
}
__global__ void gemm_k(const bf16* A,const bf16* B,bf16* C,const bf16* res,int M,int N,int K){
  __shared__ float As[16][17], Bs[16][17];
  int tx=threadIdx.x&15, ty=threadIdx.x>>4;
  int row=blockIdx.y*16+ty, col=blockIdx.x*16+tx; float acc=0;
  for(int k0=0;k0<K;k0+=16){
    As[ty][tx]=b2f(A[row*K+k0+tx]); Bs[ty][tx]=b2f(B[(k0+ty)*N+col]); __syncthreads();
    for(int k=0;k<16;k++) acc+=As[ty][k]*Bs[k][tx]; __syncthreads();
  }
  if(res) acc=b2f(f2b(acc))+b2f(res[row*N+col]);
  C[row*N+col]=f2b(acc);
}
__global__ void attn_k(const bf16* qkv, bf16* o){
  int idx=blockIdx.x*blockDim.x+threadIdx.x; if(idx>=S_*32) return;
  int s=idx/32,h=idx%32; float q[18]; for(int d=0;d<18;d++) q[d]=b2f(qkv[s*1728+h*18+d]);
  float lg[8]; float mx=-1e30f; float scale=rsqrtf(18.f);
  for(int w=0;w<8;w++){int p=s-2*w; float a=0; if(p>=0){for(int d=0;d<18;d++) a+=q[d]*b2f(qkv[p*1728+576+h*18+d]); a*=scale;} else a=-1e9f; lg[w]=a; mx=fmaxf(mx,a);}
  float sum=0; for(int w=0;w<8;w++){lg[w]=__expf(lg[w]-mx); sum+=lg[w];}
  for(int d=0;d<18;d++){float a=0; for(int w=0;w<8;w++){int p=s-2*w; if(p<0)p=0; a+=lg[w]/sum*b2f(qkv[p*1728+1152+h*18+d]);} o[s*D_+h*18+d]=f2b(a);}
}
__global__ void geglu_k(const bf16* g,const bf16* u,bf16* a,int n){
  int i=blockIdx.x*blockDim.x+threadIdx.x; if(i>=n) return;
  float x=b2f(g[i]); float t=0.5f*x*(1.f+tanhf(0.7978845608f*(x+0.044715f*x*x*x)));
  a[i]=f2b(b2f(f2b(t))*b2f(u[i]));
}
extern "C" void kernel_launch(void* const* d_in, const int* in_sizes, int n_in, void* d_out, int out_size, void* d_ws, size_t ws_size, hipStream_t stream) {
  const bf16* x=(const bf16*)d_in[0]; const bf16* wqkv=(const bf16*)d_in[1]; const bf16* wo=(const bf16*)d_in[2];
  const bf16* wg=(const bf16*)d_in[4]; const bf16* wu=(const bf16*)d_in[5]; const bf16* wd=(const bf16*)d_in[6];
  bf16* ws=(bf16*)d_ws; bf16* xn=ws; bf16* qkv=xn+S_*D_; bf16* o=qkv+S_*1728; bf16* h=o+S_*D_; bf16* hn=h+S_*D_;
  bf16* g=hn+S_*D_; bf16* u=g+S_*F_; bf16* a=u+S_*F_;
  ln_k<<<S_,256,0,stream>>>(x,xn);
  gemm_k<<<dim3(1728/16,S_/16),256,0,stream>>>(xn,wqkv,qkv,nullptr,S_,1728,D_);
  attn_k<<<S_*32/256,256,0,stream>>>(qkv,o);
  gemm_k<<<dim3(D_/16,S_/16),256,0,stream>>>(o,wo,h,x,S_,D_,D_);
  ln_k<<<S_,256,0,stream>>>(h,hn);
  gemm_k<<<dim3(F_/16,S_/16),256,0,stream>>>(hn,wg,g,nullptr,S_,F_,D_);
  gemm_k<<<dim3(F_/16,S_/16),256,0,stream>>>(hn,wu,u,nullptr,S_,F_,D_);
  geglu_k<<<S_*F_/256,256,0,stream>>>(g,u,a,S_*F_);
  gemm_k<<<dim3(D_/16,S_/16),256,0,stream>>>(a,wd,(bf16*)d_out,h,S_,D_,F_);
}
```

```cpp
#include <hip/hip_runtime.h>
#include <hip/hip_bf16.h>
#include <stdint.h>
#include <type_traits>

typedef __bf16 bf16_t;
typedef __attribute__((ext_vector_type(8))) __bf16 bf16x8;
typedef __attribute__((ext_vector_type(2))) __bf16 bf16x2;
typedef __attribute__((ext_vector_type(4))) short s16x4;
typedef __attribute__((ext_vector_type(8))) short s16x8;
typedef __attribute__((ext_vector_type(2))) float f32x2;
typedef __attribute__((ext_vector_type(4))) float f32x4;
typedef __attribute__((ext_vector_type(16))) float f32x16;
typedef __attribute__((ext_vector_type(4))) int i32x4;
typedef __attribute__((ext_vector_type(4))) unsigned u32x4;
typedef __attribute__((ext_vector_type(2))) unsigned u32x2;
#define LDS_AS __attribute__((address_space(3)))
#define DI __device__ __forceinline__

constexpr int T_ = 2048, D_ = 576, F_ = 4608;

constexpr size_t WS_O = 0;
constexpr size_t WS_HC = 2359296;
constexpr size_t WS_ACT = 4718592;
constexpr size_t WS_PART = 23592960;
constexpr size_t WS_WF = 33030144;
constexpr size_t WS_MEANX = 39059456;
constexpr size_t WS_H = 39067648;
constexpr size_t WS_WBAR = 39075840;
constexpr size_t WS_SSQ = 39124992;
constexpr unsigned WF2_OFF = 0;
constexpr unsigned WFO_OFF = 5308416;
constexpr unsigned WF_TOTAL = 5971968 + 18432;
constexpr size_t WS_CNT = 39190528;
constexpr size_t WS_DBG = 80u << 20;

template <int I, int N, class F>
DI void static_for(F&& f) {
  if constexpr (I < N) {
    f(std::integral_constant<int, I>{});
    static_for<I + 1, N>(f);
  }
}

DI i32x4 make_rsrc(const void* p, unsigned bytes) {
  uint64_t a = (uint64_t)p;
  i32x4 r;
  r[0] = (int)(unsigned)a;
  r[1] = (int)((unsigned)(a >> 32) & 0xffffu);
  r[2] = (int)bytes;
  r[3] = 0x00020000;
  return r;
}
DI void glds16(i32x4 rsrc, unsigned voff, unsigned soff, unsigned lds_addr) {
  asm volatile("s_mov_b32 m0, %0\n\ts_nop 0\n\tbuffer_load_dwordx4 %1, %2, %3 offen lds"
               :: "s"(lds_addr), "v"(voff), "s"(rsrc), "s"(soff) : "memory");
}
template <int N> DI void wait_vm() { asm volatile("s_waitcnt vmcnt(%0)" :: "n"(N) : "memory"); }
template <int N> DI void wait_vm_lgkm0_barrier() {
  asm volatile("s_waitcnt vmcnt(%0) lgkmcnt(0)\n\ts_barrier" :: "n"(N) : "memory");
}
template <int N> DI void wait_vm_barrier() {
  asm volatile("s_waitcnt vmcnt(%0)\n\ts_barrier" :: "n"(N) : "memory");
}
DI float fmul_s(float a, float b) { float r; asm("v_mul_f32_e32 %0, %1, %2" : "=v"(r) : "v"(a), "v"(b)); return r; }
DI float ffma_s(float a, float b, float c) { float r; asm("v_fma_f32 %0, %1, %2, %3" : "=v"(r) : "v"(a), "v"(b), "v"(c)); return r; }
typedef __attribute__((address_space(1))) unsigned gu32;
#define GLDS3(rsrc, voff, s0, s1, lbase, limm) do { unsigned t_; \
  asm volatile("s_add_u32 m0, %5, %6\n\ts_add_u32 %0, %3, %4\n\tbuffer_load_dwordx4 %1, %2, %0 offen sc1 lds" \
               : "=&s"(t_) : "v"(voff), "s"(rsrc), "s"(s0), "s"(s1), "s"(lbase), "i"(limm) : "memory", "scc"); } while (0)
DI void glds16m(i32x4 rsrc, unsigned voff, unsigned soff, unsigned lds_addr, unsigned mlo, unsigned mhi) {
  asm volatile("s_mov_b32 m0, %0\n\ts_mov_b32 exec_lo, %4\n\ts_mov_b32 exec_hi, %5\n\tbuffer_load_dwordx4 %1, %2, %3 offen lds\n\ts_mov_b64 exec, -1"
               :: "s"(lds_addr), "v"(voff), "s"(rsrc), "s"(soff), "s"(mlo), "s"(mhi) : "memory");
}
DI void barrier_lgkm0() { asm volatile("s_waitcnt lgkmcnt(0)\n\ts_barrier" ::: "memory"); }
DI s16x4 tr_read(unsigned a) { return __builtin_amdgcn_ds_read_tr16_b64_v4i16((LDS_AS s16x4*)(size_t)a); }
DI bf16x8 tr_frag(unsigned a0, unsigned a1) {
  s16x4 lo = tr_read(a0), hi = tr_read(a1);
  s16x8 v = __builtin_shufflevector(lo, hi, 0, 1, 2, 3, 4, 5, 6, 7);
  return __builtin_bit_cast(bf16x8, v);
}
DI bf16x8 lds_frag(unsigned a) { return *(LDS_AS bf16x8*)(size_t)a; }
DI unsigned pack_bf16(float a, float b) {
  f32x2 f = {a, b};
  bf16x2 c = __builtin_convertvector(f, bf16x2);
  return __builtin_bit_cast(unsigned, c);
}
DI float bf_lo(unsigned u) { return __uint_as_float(u << 16); }
DI float bf_hi(unsigned u) { return __uint_as_float(u & 0xffff0000u); }
DI float round_bf16(float a) { return bf_lo(pack_bf16(a, 0.f)); }
#define SCHED_FENCE() __builtin_amdgcn_sched_barrier(0)
DI float dot2(unsigned a, unsigned b, float c) {
  return __builtin_amdgcn_fdot2_f32_bf16(__builtin_bit_cast(bf16x2, a), __builtin_bit_cast(bf16x2, b), c, false);
}

constexpr int K1_WROW = 768;
constexpr int K1_WB = 32 * K1_WROW;
constexpr int K1_NW = 4;
constexpr int K1_XB = 10240;
constexpr int K1_NX = 4;
constexpr int K1_XOFF = K1_NW * K1_WB;
constexpr int K1_POFF = K1_XOFF + K1_NX * K1_XB;
constexpr int K1_SOFF = K1_POFF + 4096;
constexpr int K1_BOFF = K1_SOFF + 2048;
constexpr int K1_LDS = K1_BOFF + 2048 + 256;
constexpr int K1_LA = 4;
constexpr int K1_NUNITS = 162;

DI float xsum16(float v) { auto r = __builtin_amdgcn_permlane16_swap(__float_as_uint(v), __float_as_uint(v), false, false); return __uint_as_float(r[0]) + __uint_as_float(r[1]); }
DI float xsum32(float v) { auto r = __builtin_amdgcn_permlane32_swap(__float_as_uint(v), __float_as_uint(v), false, false); return __uint_as_float(r[0]) + __uint_as_float(r[1]); }
DI float xmax16(float v) { auto r = __builtin_amdgcn_permlane16_swap(__float_as_uint(v), __float_as_uint(v), false, false); return fmaxf(__uint_as_float(r[0]), __uint_as_float(r[1])); }
DI float xmax32(float v) { auto r = __builtin_amdgcn_permlane32_swap(__float_as_uint(v), __float_as_uint(v), false, false); return fmaxf(__uint_as_float(r[0]), __uint_as_float(r[1])); }
DI void gload16(u32x4& d, const void* p) { asm volatile("global_load_dwordx4 %0, %1, off" : "=v"(d) : "v"(p) : "memory"); }
DI void pin4(u32x4& a, u32x4& b, u32x4& c, u32x4& d) { asm volatile("" : "+v"(a), "+v"(b), "+v"(c), "+v"(d)); }
DI void mfma16_acc(f32x4& c, const bf16x8& a, const bf16x8& b) {
  asm volatile("v_mfma_f32_16x16x32_bf16 %0, %1, %2, %0" : "+v"(c) : "v"(a), "v"(b));
}
DI void mfma16_0(f32x4& c, const bf16x8& a, const bf16x8& b) {
  asm volatile("v_mfma_f32_16x16x32_bf16 %0, %1, %2, 0" : "=&v"(c) : "v"(a), "v"(b));
}
template <bool FIRST> DI void mfma16_x(f32x4& c, const bf16x8& a, const bf16x8& b) { if constexpr (FIRST) mfma16_0(c, a, b); else mfma16_acc(c, a, b); }
DI void pinf(float& a) { asm volatile("" : "+v"(a)); }
DI void touch4(i32x4 rsrc, unsigned voff, unsigned soff, unsigned lds_addr) {
  asm volatile("s_mov_b32 m0, %0\n\ts_nop 0\n\tbuffer_load_dword %1, %2, %3 offen lds"
               :: "s"(lds_addr), "v"(voff), "s"(rsrc), "s"(soff) : "memory");
}
DI void gldsg16(const void* p, unsigned lds_addr) {
  asm volatile("s_mov_b32 m0, %0\n\ts_nop 0\n\tglobal_load_lds_dwordx4 %1, off" :: "s"(lds_addr), "v"(p) : "memory");
}
DI void touchg(const void* p, unsigned lds_addr) {
  asm volatile("s_mov_b32 m0, %0\n\ts_nop 0\n\tglobal_load_lds_dword %1, off" :: "s"(lds_addr), "v"(p) : "memory");
}
template <int N> DI void code_touch(int part, int lane, unsigned lds_addr) {
  unsigned long long pc; unsigned len;
  asm volatile("s_getpc_b64 %0\n1:\n\ts_mov_b32 %1, ffn_code_end_lbl-1b" : "=s"(pc), "=s"(len));
  const unsigned off = (unsigned)(part * N + lane) * 128u;
  if (lane < N && off < len && len < (1u << 20)) touchg((const char*)(pc & ~3ull) + off, lds_addr);
}
#define MFMA16(a, b, c) __builtin_amdgcn_mfma_f32_16x16x32_bf16((a), (b), (c), 0, 0, 0)
DI int operm(int kp) {
  const int b = kp / 72, e = kp - b * 72;
  const int hh = e < 64 ? (e >> 4) : ((e - 64) >> 1);
  const int d = e < 64 ? (e & 15) + 2 * (hh & 1) : ((hh & 1) ? 0 : 16) + (e & 1);
  return b * 72 + hh * 18 + d;
}

__global__ __launch_bounds__(768, 3) void k_qkv_attn(const bf16_t* __restrict__ x, const bf16_t* __restrict__ wqkv,
                                                     const bf16_t* __restrict__ wo, const bf16_t* __restrict__ wg,
                                                     const bf16_t* __restrict__ wu, float* __restrict__ part,
                                                     bf16_t* __restrict__ o_frag, float* __restrict__ meanx,
                                                     bf16_t* __restrict__ wbar, bf16_t* __restrict__ wf_all,
                                                     unsigned* __restrict__ cnt, const bf16_t* __restrict__ wdn) {
  __shared__ __attribute__((aligned(1024))) char smem[K1_LDS];
  const unsigned lds0 = (unsigned)(size_t)(LDS_AS char*)smem;
  const int tid = threadIdx.x, lane = tid & 63;
  const int wv = __builtin_amdgcn_readfirstlane(tid >> 6);
  const int w = wv - 4;
  const int bid = blockIdx.x;
  const int t16 = lane & 15, g = lane >> 4;
  const int xcd = bid & 7, idx = bid >> 3;
  const int hg = (xcd & 1) * 4 + (idx & 3), rt = (xcd >> 1) * 8 + (idx >> 2);

  if (__builtin_expect(wv < 4, 1)) {
    const int lw = wv;
    __builtin_amdgcn_s_setprio(0);
    const i32x4 rx = make_rsrc(x, T_ * D_ * 2);
    const i32x4 rw = make_rsrc(wqkv, D_ * 3 * D_ * 2);
    unsigned vx[2], vx4[2];
    const unsigned vw = (4 * lw + g) * (3 * D_ * 2) + ((hg * 144) & ~127) + ((t16 ^ ((g | (((lw >> 1) & 1) << 2)) << 1)) * 16);
#pragma unroll
    for (int p = 0; p < 2; ++p) {
      {
        const int r = (5 * p + lw) * 8 + (lane >> 3);
        int tok = rt * 64 - 16 + r; tok = tok < 0 ? 0 : tok;
        vx[p] = tok * (D_ * 2) + (((lane & 7) ^ ((r >> 1) & 7)) * 16);
      }
      {
        const int r = (5 * p + 4) * 8 + (lane >> 3);
        vx4[p] = (rt * 64 - 16 + r) * (D_ * 2) + (((lane & 7) ^ ((r >> 1) & 7)) * 16);
      }
    }
    unsigned mW4, mX4, mX0;
    asm volatile("s_cmp_lt_u32 %3, 2\n\ts_cselect_b32 %0, -1, 0\n\ts_cmp_eq_u32 %3, 2\n\ts_cselect_b32 %1, -1, 0\n\t"
                 "s_cmp_eq_u32 %3, 0\n\ts_cselect_b32 %2, 0xffffff00, -1"
                 : "=&s"(mW4), "=&s"(mX4), "=&s"(mX0) : "s"(lw) : "scc");
    const unsigned dW = lds0 + lw * 1024, dX = lds0 + K1_XOFF + lw * 1024;
    const int rot = ((idx >> 2) + 2 * (idx & 3)) % 9;
    unsigned un[9];
#pragma unroll
    for (int U = 0; U < 9; ++U) { const int v = U + rot; un[U] = v >= 9 ? v - 9 : v; }
    auto issueW = [&](auto S) {
      constexpr int s = decltype(S)::value;
      const unsigned so = (un[s / 2] * 2 + (s & 1)) * (32 * 3 * D_ * 2);
#pragma unroll
      for (int i = 0; i < 6; ++i) glds16(rw, vw, so + (i >> 1) * (D_ * 2) + (i & 1) * 16 * (3 * D_ * 2), dW + (s % K1_NW) * K1_WB + i * 4096);
    };
    auto issueX = [&](auto H) {
      constexpr int hh = decltype(H)::value, U = hh / 2, p = hh % 2;
      glds16m(rx, vx[p], un[U] * 128, dX + (U % K1_NX) * K1_XB + 5 * p * 1024, p == 0 ? mX0 : ~0u, ~0u);
      glds16m(rx, vx4[p], un[U] * 128, lds0 + K1_XOFF + (U % K1_NX) * K1_XB + (5 * p + 4) * 1024, mX4, mX4);
    };
    const int pw = bid * 4 + lw;
    auto step = [&](auto S) {
      constexpr int s = decltype(S)::value;
      if constexpr (s < 18) issueW(S);
      if constexpr (s + 1 < 18) issueX(std::integral_constant<int, s + 1>{});
    };
    issueX(std::integral_constant<int, 0>{});
    step(std::integral_constant<int, 0>{});
    step(std::integral_constant<int, 1>{});
    wait_vm_barrier<7>();
    step(std::integral_constant<int, 2>{});
    if constexpr (K1_LA > 3) step(std::integral_constant<int, 3>{});
    static_for<0, 17>([&](auto S) {
      constexpr int s = decltype(S)::value;
      constexpr int last = (s + K1_LA - 1 < 17) ? s + K1_LA - 1 : 17;
      constexpr int nn = 7 * (last - s - 1) - ((last == 17 && last - s - 1 > 0) ? 1 : 0) + ((s > 0 && s + 1 <= 0 + K1_LA) ? 1 : 0);
      wait_vm_barrier<nn>();
      step(std::integral_constant<int, s + K1_LA>{});
      if constexpr (s == 0) {
        int ln = lane; asm volatile("" : "+v"(ln));
        const int pu = bid < 162 ? bid : 0;
        gldsg16(wo + (size_t)operm(32 * (pu / 9) + 8 * lw + (ln >> 3)) * D_ + 64 * (pu % 9) + 8 * (ln & 7), lds0 + K1_POFF + lw * 1024);
      }
      if constexpr (s == 16) {
        const __amdgpu_buffer_rsrc_t rpart = __builtin_amdgcn_make_buffer_rsrc((void*)part, 0, 1024 * 9216, 0x00020000);
        const unsigned sSent = (lw == (bid & 3)) ? 0x40000000u : (unsigned)((bid * 4 + lw) * 9216);
#pragma unroll
        for (int k = 0; k < 9; ++k)
          __builtin_amdgcn_raw_buffer_store_b128((u32x4){~0u, ~0u, ~0u, ~0u}, rpart, lane * 16, sSent + k * 1024, 16);
      }
      if constexpr (s == 16) {
        int ln = lane; asm volatile("" : "+v"(ln));
        const int e = lw * 64 + ln;
        const int li = idx * 162 + (e < 162 ? e : 0);
        const bf16_t* tp = ((xcd & 1) ? wu : wg) + (size_t)(li / 18) * F_ + ((xcd >> 1) * 18 + li % 18) * 64;
        if (lw < 3) touchg(tp, lds0 + K1_LDS - 256);
      }
    });
    const u32x4* rowp = (const u32x4*)(wo + (size_t)operm(pw < 576 ? pw : 0) * D_);
    const u32x4 wra = rowp[lane];
    u32x4 wrb = rowp[64 + (lane & 7)];
    asm volatile("s_barrier" ::: "memory");
    if (pw < 576) {
      if (lane >= 8) wrb = (u32x4){0, 0, 0, 0};
      float wsum = 0.f;
#pragma unroll
      for (int i = 0; i < 4; ++i) { wsum = dot2(wra[i], 0x3f803f80u, wsum); wsum = dot2(wrb[i], 0x3f803f80u, wsum); }
#pragma unroll
      for (int o = 8; o > 0; o >>= 1) wsum += __shfl_xor(wsum, o);
      wsum = xsum32(xsum16(wsum));
      if (lane == 0) {
        const float m = wsum * (1.0f / D_);
        const unsigned hi = pack_bf16(m, 0.f) & 0xffffu;
        const unsigned lo = pack_bf16(m - bf_lo(hi), 0.f) & 0xffffu;
        unsigned short* dst = (unsigned short*)wf_all + (WFO_OFF + (36 * 18 + (pw >> 5)) * 1024 + ((pw >> 3) & 3) * 256) / 2 + (pw & 7);
        dst[0] = (unsigned short)hi;
        dst[8] = (unsigned short)lo;
      }
    }
    if (lw == 3 && lane == 0) cnt[bid * 64] = 0u;
    if (lw == 2) ((unsigned*)wbar)[bid * 64 + lane] = ~0u;
    return;
  }

  if (w == 7) code_touch<20>(idx, lane, lds0 + K1_LDS - 256);
  const int hd = w & 3, th = w >> 2;
  if (w == 0 && lane < K1_NX * 8) {
    u32x4 ones = {0x3f803f80u, 0x3f803f80u, 0x3f803f80u, 0x3f803f80u};
    *(LDS_AS u32x4*)(size_t)(lds0 + K1_XOFF + (lane >> 3) * K1_XB + (lane & 7) * 16) = ones;
  }
  const int q4 = t16 >> 2, p4 = t16 & 3;
  const __amdgpu_buffer_rsrc_t rout = __builtin_amdgcn_make_buffer_rsrc((void*)wf_all, 0, WF_TOTAL, 0x00020000);
  unsigned long long ctp;
  bool ctok;
  {
    const int li = bid * 486 + w * 64 + lane;
    const int mi = li / 41472, lo = li - mi * 41472;
    unsigned long long base = (unsigned long long)wg;
    if (mi == 1) base = (unsigned long long)wu;
    if (mi == 2) base = (unsigned long long)wdn;
    ctp = base + (unsigned long long)lo * 128ull;
    ctok = w * 64 + lane < 486 && mi < 3;
  }
  auto prep_unit = [&]() {
    const unsigned ra = lds0 + K1_POFF + (8 * g + q4) * 128 + p4 * 8 + hd * 32;
    const bf16x8 fr = tr_frag(ra, ra + 4 * 128);
    const unsigned ob = (bid < 162 && th == 0) ? (unsigned)(WFO_OFF + ((4 * (bid % 9) + hd) * 18 + bid / 9) * 1024) : 0x40000000u;
    __builtin_amdgcn_raw_buffer_store_b128(__builtin_bit_cast(u32x4, fr), rout, lane * 16, ob, 16);
  };

  unsigned wa[4], wb[4];
  {
    const int hb = ((hg * 144) & 127) + hd * 36;
    const int mainb = hb + (hd & 1) * 4, leftb = (hd & 1) ? hb - 4 : hb + 32;
    const int f5 = (q4 | ((g & 1) << 2)) << 5;
#pragma unroll
    for (int j = 0; j < 4; ++j) {
      const int sec = (j < 3) ? j : (p4 < 3 ? p4 : 2);
      int byte = (j < 3) ? mainb + p4 * 8 : leftb;
      byte = byte > 248 ? 248 : byte;
      wa[j] = lds0 + (8 * g + q4) * 256 + sec * 8192 + (byte ^ f5);
      wb[j] = wa[j] + 2 * K1_WB;
    }
  }
  unsigned xa[2], sa[2];
  xa[0] = lds0 + K1_XOFF + th * 4096 + (t16 >> 3) * 1024 + (t16 & 7) * 128 + ((g ^ ((t16 >> 1) & 7)) * 16);
  xa[1] = xa[0] ^ 64;
  const int stile = th == 0 ? (hd < 3 ? hd : 0) : (hd < 2 ? hd + 1 : 1);
  const bool swr = th == 0 ? hd < 3 : hd < 2;
  sa[0] = xa[0] + stile * 2048; sa[1] = xa[1] + stile * 2048;
  if (w == 7) {
    f32x4 b0, b1;
#pragma unroll
    for (int r = 0; r < 4; ++r) {
      const int d1 = t16 - (4 * g + r), d0 = d1 + 16;
      b0[r] = ((d0 & 1) == 0 && d0 <= 14) ? 0.f : -3.0e38f;
      b1[r] = ((d1 & 1) == 0 && d1 >= 0 && d1 <= 14) ? 0.f : -3.0e38f;
    }
    *(LDS_AS f32x4*)(size_t)(lds0 + K1_BOFF + lane * 16) = b0;
    *(LDS_AS f32x4*)(size_t)(lds0 + K1_BOFF + 1024 + lane * 16) = b1;
  }
  asm volatile("" : "+v"(wa[0]), "+v"(wa[1]), "+v"(wa[2]), "+v"(wa[3]), "+v"(wb[0]), "+v"(wb[1]), "+v"(wb[2]), "+v"(wb[3]),
               "+v"(xa[0]), "+v"(xa[1]), "+v"(sa[0]), "+v"(sa[1]));

  f32x4 aq[3], ak[3], al[3], av[3];
  float s1 = 0.f, s2 = 0.f;
  bf16x8 xf[2][3], wf[2][4], sf[2];
  barrier_lgkm0();
  s16x4 wh[2][4][2];
  auto rd1 = [&](auto S, auto R) {
    constexpr int s = decltype(S)::value, r = decltype(R)::value;
    constexpr int ws = s % K1_NW;
    if constexpr (r < 8) {
      constexpr int j = r / 2;
      const unsigned a = (ws < 2 ? wa[j] + ws * K1_WB : wb[j] + (ws - 2) * K1_WB);
      wh[s & 1][j][r & 1] = tr_read(a + (r & 1) * 1024);
    } else if constexpr (r < 11) {
      xf[s & 1][r - 8] = lds_frag(xa[s & 1] + ((s / 2) % K1_NX) * K1_XB + (r - 8) * 2048);
    } else {
      sf[s & 1] = lds_frag(sa[s & 1] + ((s / 2) % K1_NX) * K1_XB);
    }
  };
  static_for<0, 12>([&](auto R) { rd1(std::integral_constant<int, 0>{}, R); });
  static_for<0, 18>([&](auto S) {
    constexpr int s = decltype(S)::value;
#pragma unroll
    for (int j = 0; j < 4; ++j)
      wf[s & 1][j] = __builtin_bit_cast(bf16x8, __builtin_shufflevector(wh[s & 1][j][0], wh[s & 1][j][1], 0, 1, 2, 3, 4, 5, 6, 7));
    if constexpr (s + 1 < 18) barrier_lgkm0();
    const u32x4 xs = __builtin_bit_cast(u32x4, sf[s & 1]);
    static_for<0, 12>([&](auto M) {
      constexpr int m = decltype(M)::value, i = m / 4, k = m % 4;
      if constexpr (k == 0) mfma16_x<s == 0>(aq[i], wf[s & 1][0], xf[s & 1][i]);
      if constexpr (k == 1) mfma16_x<s == 0>(ak[i], wf[s & 1][1], xf[s & 1][i]);
      if constexpr (k == 2) mfma16_x<s == 0>(al[i], wf[s & 1][3], xf[s & 1][i]);
      if constexpr (k == 3) mfma16_x<s == 0>(av[i], xf[s & 1][i], wf[s & 1][2]);
      SCHED_FENCE();
      if constexpr (s + 1 < 18) {
        constexpr int sn = (s + 1 < 18 ? s + 1 : 0);
        if constexpr (m < 4) { rd1(std::integral_constant<int, sn>{}, std::integral_constant<int, 2 * m>{}); rd1(std::integral_constant<int, sn>{}, std::integral_constant<int, 2 * m + 1>{}); }
        else if constexpr (m < 8) rd1(std::integral_constant<int, sn>{}, std::integral_constant<int, (m < 8 ? m + 4 : 0)>{});
      }
      if constexpr (m >= 8) {
        constexpr int e = m - 8;
        s1 = dot2(xs[e], 0x3f803f80u, s1); pinf(s1); s2 = dot2(xs[e], xs[e], s2); pinf(s2);
      }
      SCHED_FENCE();
    });
    if constexpr (s == 8) prep_unit();
    if constexpr (s == 99) { if (ctok) touchg((const void*)ctp, lds0 + K1_LDS - 256); }
  });
  asm volatile("s_nop 15" : "+v"(aq[0]), "+v"(aq[1]), "+v"(aq[2]), "+v"(ak[0]), "+v"(ak[1]), "+v"(ak[2]), "+v"(al[0]), "+v"(al[1]), "+v"(al[2]),
               "+v"(av[0]), "+v"(av[1]), "+v"(av[2]));
  const unsigned smu = lds0 + K1_SOFF, srs = smu + 320, scs = smu + 640 + hd * 320;
  const unsigned svl = lds0 + 3 * K1_WB + w * 512;
  {
    s1 = xsum32(xsum16(s1)); s2 = xsum32(xsum16(s2));
    const float mu = s1 * (1.0f / D_);
    const float var = fmaxf(s2 * (1.0f / D_) - mu * mu, 0.f);
    const float rs = __builtin_amdgcn_rsqf(var + 1e-5f);
    const int gt = th * 2 + stile;
    if (swr && g == 0) {
      *(LDS_AS float*)(size_t)(smu + (gt * 16 + t16) * 4) = mu;
      *(LDS_AS float*)(size_t)(srs + (gt * 16 + t16) * 4) = rs;
      if (hg == 0 && gt >= 1) meanx[rt * 64 + (gt - 1) * 16 + t16] = mu;
    }
    if (th == 0) {
      if (t16 == 0) {
        *(LDS_AS f32x4*)(size_t)(scs + g * 16) = aq[0];
        *(LDS_AS f32x4*)(size_t)(scs + 64 + g * 16) = ak[0];
        *(LDS_AS f32x4*)(size_t)(scs + 128 + g * 16) = al[0];
      }
      if (g == 0) *(LDS_AS float*)(size_t)(scs + 192 + t16 * 4) = av[0][0];
    }
    if (g == 2) {
#pragma unroll
      for (int i = 0; i < 3; ++i) {
        *(LDS_AS float*)(size_t)(svl + (i * 16 + t16) * 4) = (hd & 1) ? al[i][2] : al[i][0];
        *(LDS_AS float*)(size_t)(svl + 192 + (i * 16 + t16) * 4) = (hd & 1) ? al[i][3] : al[i][1];
      }
    }
  }
  barrier_lgkm0();
  {
    const f32x4 cq = *(LDS_AS f32x4*)(size_t)(scs + g * 16), ck = *(LDS_AS f32x4*)(size_t)(scs + 64 + g * 16),
                cl = *(LDS_AS f32x4*)(size_t)(scs + 128 + g * 16);
    const float cvv = *(LDS_AS float*)(size_t)(scs + 192 + t16 * 4), cvl = *(LDS_AS float*)(size_t)(scs + 128 + 32 + ((hd & 1) * 2 + (t16 & 1)) * 4);
    f32x4 avl[3];
#pragma unroll
    for (int i = 0; i < 3; ++i) avl[i] = *(LDS_AS f32x4*)(size_t)(svl + (t16 & 1) * 192 + (i * 16 + 4 * g) * 4);
    const f32x4 bias0 = *(LDS_AS f32x4*)(size_t)(lds0 + K1_BOFF + lane * 16), bias1 = *(LDS_AS f32x4*)(size_t)(lds0 + K1_BOFF + 1024 + lane * 16);
    const bool odd = hd & 1;
    const float cl0 = odd ? cl[2] : cl[0], cl1 = odd ? cl[3] : cl[1];
    float mu1[3], rs1[3];
    f32x4 mu4[3], rs4[3];
#pragma unroll
    for (int i = 0; i < 3; ++i) {
      const unsigned o = (th * 2 + i) * 64;
      mu1[i] = *(LDS_AS float*)(size_t)(smu + o + t16 * 4); rs1[i] = *(LDS_AS float*)(size_t)(srs + o + t16 * 4);
      mu4[i] = *(LDS_AS f32x4*)(size_t)(smu + o + g * 16); rs4[i] = *(LDS_AS f32x4*)(size_t)(srs + o + g * 16);
    }
    u32x4 kop[3], qop[3];
    u32x2 vp[3], vlp[3];
#pragma unroll
    for (int i = 0; i < 3; ++i) {
      const float nm = -mu1[i];
      const unsigned lp = pack_bf16(fmaf(nm, cl0, odd ? al[i][2] : al[i][0]), fmaf(nm, cl1, odd ? al[i][3] : al[i][1]));
      kop[i][0] = pack_bf16(fmaf(nm, ck[0], ak[i][0]), fmaf(nm, ck[1], ak[i][1]));
      kop[i][1] = pack_bf16(fmaf(nm, ck[2], ak[i][2]), fmaf(nm, ck[3], ak[i][3]));
      kop[i][2] = 0u; kop[i][3] = lp;
      if (i >= 1) {
        qop[i][0] = pack_bf16(fmaf(nm, cq[0], aq[i][0]), fmaf(nm, cq[1], aq[i][1]));
        qop[i][1] = pack_bf16(fmaf(nm, cq[2], aq[i][2]), fmaf(nm, cq[3], aq[i][3]));
        auto sw = __builtin_amdgcn_permlane16_swap(lp, lp, false, false);
        qop[i][2] = 0u; qop[i][3] = (g == 1) ? sw[0] : 0u;
      }
      vp[i][0] = pack_bf16(fmaf(-mu4[i][0], cvv, av[i][0]), fmaf(-mu4[i][1], cvv, av[i][1]));
      vp[i][1] = pack_bf16(fmaf(-mu4[i][2], cvv, av[i][2]), fmaf(-mu4[i][3], cvv, av[i][3]));
      vlp[i][0] = pack_bf16(fmaf(-mu4[i][0], cvl, avl[i][0]), fmaf(-mu4[i][1], cvl, avl[i][1]));
      vlp[i][1] = pack_bf16(fmaf(-mu4[i][2], cvl, avl[i][2]), fmaf(-mu4[i][3], cvl, avl[i][3]));
    }
    const __amdgpu_buffer_rsrc_t ro = __builtin_amdgcn_make_buffer_rsrc((void*)o_frag, 0, T_ * D_ * 2, 0x00020000);
    const float scale = 0.23570226039551584f * 1.4426950408889634f;
    const f32x4 z4 = {0.f, 0.f, 0.f, 0.f};
    const bool first = (rt == 0) && (th == 0);
#pragma unroll
    for (int i = 1; i < 3; ++i) {
      f32x4 S0 = MFMA16(__builtin_bit_cast(bf16x8, kop[i - 1]), __builtin_bit_cast(bf16x8, qop[i]), z4);
      f32x4 S1 = MFMA16(__builtin_bit_cast(bf16x8, kop[i]), __builtin_bit_cast(bf16x8, qop[i]), z4);
      const float c = rs1[i] * scale;
      float l0[4], l1[4];
#pragma unroll
      for (int r = 0; r < 4; ++r) {
        const float b0 = (i == 1 && first) ? -3.0e38f : bias0[r];
        l0[r] = fmaf(S0[r] * rs4[i - 1][r], c, b0);
        l1[r] = fmaf(S1[r] * rs4[i][r], c, bias1[r]);
      }
      float mx = fmaxf(fmaxf(fmaxf(l0[0], l0[1]), fmaxf(l0[2], l0[3])), fmaxf(fmaxf(l1[0], l1[1]), fmaxf(l1[2], l1[3])));
      mx = xmax32(xmax16(mx));
      float sum = 0.f;
#pragma unroll
      for (int r = 0; r < 4; ++r) {
        l0[r] = __builtin_amdgcn_exp2f(l0[r] - mx); l1[r] = __builtin_amdgcn_exp2f(l1[r] - mx);
        sum += l0[r] + l1[r];
      }
      sum = xsum32(xsum16(sum));
      const float inv = __builtin_amdgcn_rcpf(sum);
      u32x4 pop;
      pop[0] = pack_bf16(l0[0] * rs4[i - 1][0], l0[1] * rs4[i - 1][1]);
      pop[1] = pack_bf16(l0[2] * rs4[i - 1][2], l0[3] * rs4[i - 1][3]);
      pop[2] = pack_bf16(l1[0] * rs4[i][0], l1[1] * rs4[i][1]);
      pop[3] = pack_bf16(l1[2] * rs4[i][2], l1[3] * rs4[i][3]);
      const u32x4 vop = {vp[i - 1][0], vp[i - 1][1], vp[i][0], vp[i][1]};
      const u32x4 vlop = {vlp[i - 1][0], vlp[i - 1][1], vlp[i][0], vlp[i][1]};
      const f32x4 O = MFMA16(__builtin_bit_cast(bf16x8, vop), __builtin_bit_cast(bf16x8, pop), z4);
      const f32x4 Ol = MFMA16(__builtin_bit_cast(bf16x8, vlop), __builtin_bit_cast(bf16x8, pop), z4);
      u32x2 ov = {pack_bf16(O[0] * inv, O[1] * inv), pack_bf16(O[2] * inv, O[3] * inv)};
      const unsigned ol = pack_bf16(Ol[0] * inv, Ol[1] * inv);
      const unsigned so = ((rt * 2 + th) * 72 + hg * 9) * 512 + (i - 1) * 256;
      __builtin_amdgcn_raw_buffer_store_b64(ov, ro, (hd * 2 + (g >> 1)) * 512 + t16 * 16 + (g & 1) * 8, so, 16);
      __builtin_amdgcn_raw_buffer_store_b32(ol, ro, (g == 0) ? (unsigned)(8 * 512 + t16 * 16 + hd * 4) : 0x40000000u, so, 16);
    }
  }
}

constexpr int K2_IMGP = 336;
DI void wait_count(const unsigned* p, unsigned want) {
  for (unsigned spins = 0; spins < (1u << 20); ++spins) {
    const unsigned c = __builtin_amdgcn_readfirstlane(__hip_atomic_load((gu32*)p, __ATOMIC_RELAXED, __HIP_MEMORY_SCOPE_AGENT));
    if (c >= want) break;
    __builtin_amdgcn_s_sleep(1);
  }
}
__global__ __launch_bounds__(256, 1) void k_ffn(const bf16_t* __restrict__ x, const bf16_t* __restrict__ wfo,
                                                const bf16_t* __restrict__ o_frag, const float* __restrict__ meanx,
                                                const bf16_t* __restrict__ wbar, bf16_t* hc, float* ssq, float* hmean,
                                                const bf16_t* __restrict__ wg, const bf16_t* __restrict__ wu,
                                                const bf16_t* __restrict__ wd, bf16_t* wf2, bf16_t* act, float* part,
                                                unsigned* cnt, bf16_t* __restrict__ out) {
  __shared__ __attribute__((aligned(1024))) char smem[160 * 1024];
  const unsigned lds0 = (unsigned)(size_t)(LDS_AS char*)smem;
  const int tid = threadIdx.x, lane = tid & 63;
  const int w = __builtin_amdgcn_readfirstlane(tid >> 6);
  const int bid = blockIdx.x;
  const int xcd = bid & 7, idx = bid >> 3;
  const int t16 = lane & 15, g = lane >> 4;
  const unsigned vlin = lane * 16;
  const __amdgpu_buffer_rsrc_t rc = __builtin_amdgcn_make_buffer_rsrc((void*)hc, 0, T_ * D_ * 2, 0x00020000);
  if (w == 3) code_touch<16>(idx, lane, lds0 + 159 * 1024);
  asm volatile("" ::: "a0","a1","a2","a3","a4","a5","a6","a7","a8","a9","a10","a11","a12","a13","a14","a15","a16","a17","a18","a19","a20","a21","a22","a23","a24","a25","a26","a27","a28","a29","a30","a31","a32","a33","a34","a35","a36","a37","a38","a39","a40","a41","a42","a43","a44","a45","a46","a47","a48","a49","a50","a51","a52","a53","a54","a55","a56","a57","a58","a59","a60","a61","a62","a63","a64","a65","a66","a67","a68","a69","a70","a71","a72","a73","a74","a75","a76","a77","a78","a79","a80","a81","a82","a83","a84","a85","a86","a87","a88","a89","a90","a91","a92","a93","a94","a95","a96","a97","a98","a99","a100","a101","a102","a103","a104","a105","a106","a107","a108","a109","a110","a111","a112","a113","a114","a115","a116","a117","a118","a119","a120","a121","a122","a123","a124","a125","a126","a127","a128","a129","a130","a131","a132","a133","a134","a135","a136","a137","a138","a139","a140","a141","a142","a143","a144","a145","a146","a147","a148","a149","a150","a151","a152","a153","a154","a155","a156","a157","a158","a159","a160","a161","a162","a163","a164","a165","a166","a167","a168","a169","a170","a171","a172","a173","a174","a175","a176","a177","a178","a179","a180","a181","a182","a183","a184","a185","a186","a187","a188","a189","a190","a191","a192","a193","a194","a195","a196","a197","a198","a199","a200","a201","a202","a203","a204","a205","a206","a207","a208","a209","a210","a211","a212","a213","a214","a215","a216","a217","a218","a219","a220","a221","a222","a223","a224","a225","a226","a227","a228","a229","a230","a231","a232","a233","a234","a235","a236","a237","a238","a239","a240","a241","a242","a243","a244","a245","a246","a247","a248","a249","a250","a251","a252","a253","a254","a255");
    const int nsa = idx & 7, rta = xcd * 4 + (idx >> 3);
    const int tile0 = (nsa * 9) >> 1, odd = nsa & 1;
    const i32x4 rwo = make_rsrc(wfo, 37 * 18 * 1024);
    const i32x4 rof = make_rsrc(o_frag, T_ * D_ * 2);
    const i32x4 rx = make_rsrc(x, T_ * D_ * 2);
    const i32x4 rmx = make_rsrc(meanx, T_ * 4);
    const unsigned lA = lds0 + 64 * 1024;
    const int jsp = odd ? 0 : 4, rsp = odd ? 0 : 8;
    const unsigned vsp = ((t16 & ~1) == rsp) ? (unsigned)((36 - tile0 - jsp) * 18 * 1024 + (g * 16 + (t16 & 1)) * 16) : vlin;
    const unsigned vo = g * 512 + t16 * 16;
    const unsigned so0 = ((rta * 2 + (w >> 1)) * 72) * 512 + (w & 1) * 256;
    const unsigned vA = (odd && w == 0) ? vsp : vlin, vB = odd ? vlin : vsp;
    const unsigned sA = (tile0 + w) * 18 * 1024, dA = lA + w * 1024;
    const int k1_ = w < 2 ? w + 4 : w;
    const unsigned sB0 = ((tile0 + 4) * 18 + w) * 1024, dB0 = lA + 4096 + w * 5120;
    const unsigned sB1 = ((tile0 + 4) * 18 + k1_) * 1024, dB1 = lA + 4096 + k1_ * 5120;
    u32x2 xv[5];
    unsigned mxv;
    const unsigned vx = t16 * (D_ * 2) + g * 8;
    const unsigned sx = (rta * 64 + w * 16) * (D_ * 2) + tile0 * 32;
    asm volatile("buffer_load_dwordx4 a[0:3], %0, %1, %2 offen" :: "v"(vo), "s"(rof), "s"(so0 + 0) : "memory");
    glds16(rwo, vA, sA + 0, dA + 0);
    asm volatile("buffer_load_dwordx4 a[4:7], %0, %1, %2 offen" :: "v"(vo), "s"(rof), "s"(so0 + 2048) : "memory");
    glds16(rwo, vA, sA + 1024, dA + 5120);
    asm volatile("buffer_load_dwordx4 a[8:11], %0, %1, %2 offen" :: "v"(vo), "s"(rof), "s"(so0 + 4096) : "memory");
    glds16(rwo, vA, sA + 2048, dA + 10240);
    asm volatile("buffer_load_dwordx4 a[12:15], %0, %1, %2 offen" :: "v"(vo), "s"(rof), "s"(so0 + 6144) : "memory");
    glds16(rwo, vA, sA + 3072, dA + 15360);
    asm volatile("buffer_load_dwordx4 a[16:19], %0, %1, %2 offen" :: "v"(vo), "s"(rof), "s"(so0 + 8192) : "memory");
    glds16(rwo, vA, sA + 4096, dA + 20480);
    asm volatile("buffer_load_dwordx4 a[20:23], %0, %1, %2 offen" :: "v"(vo), "s"(rof), "s"(so0 + 10240) : "memory");
    glds16(rwo, vA, sA + 5120, dA + 25600);
    glds16(rwo, vB, sB0 + 0, dB0 + 0);
    glds16(rwo, vB, sB1 + 0, dB1 + 0);
    asm volatile("buffer_load_dwordx4 a[24:27], %0, %1, %2 offen" :: "v"(vo), "s"(rof), "s"(so0 + 12288) : "memory");
    glds16(rwo, vA, sA + 6144, dA + 30720);
    asm volatile("buffer_load_dwordx4 a[28:31], %0, %1, %2 offen" :: "v"(vo), "s"(rof), "s"(so0 + 14336) : "memory");
    glds16(rwo, vA, sA + 7168, dA + 35840);
    asm volatile("buffer_load_dwordx4 a[32:35], %0, %1, %2 offen" :: "v"(vo), "s"(rof), "s"(so0 + 16384) : "memory");
    glds16(rwo, vA, sA + 8192, dA + 40960);
    asm volatile("buffer_load_dwordx4 a[36:39], %0, %1, %2 offen" :: "v"(vo), "s"(rof), "s"(so0 + 18432) : "memory");
    glds16(rwo, vA, sA + 9216, dA + 46080);
    asm volatile("buffer_load_dwordx4 a[40:43], %0, %1, %2 offen" :: "v"(vo), "s"(rof), "s"(so0 + 20480) : "memory");
    glds16(rwo, vA, sA + 10240, dA + 51200);
    asm volatile("buffer_load_dwordx4 a[44:47], %0, %1, %2 offen" :: "v"(vo), "s"(rof), "s"(so0 + 22528) : "memory");
    glds16(rwo, vA, sA + 11264, dA + 56320);
    glds16(rwo, vB, sB0 + 6144, dB0 + 30720);
    glds16(rwo, vB, sB1 + 6144, dB1 + 30720);
    asm volatile("buffer_load_dwordx4 a[48:51], %0, %1, %2 offen" :: "v"(vo), "s"(rof), "s"(so0 + 24576) : "memory");
    glds16(rwo, vA, sA + 12288, dA + 61440);
    asm volatile("buffer_load_dwordx4 a[52:55], %0, %1, %2 offen" :: "v"(vo), "s"(rof), "s"(so0 + 26624) : "memory");
    glds16(rwo, vA, sA + 13312, dA + 66560);
    asm volatile("buffer_load_dwordx4 a[56:59], %0, %1, %2 offen" :: "v"(vo), "s"(rof), "s"(so0 + 28672) : "memory");
    glds16(rwo, vA, sA + 14336, dA + 71680);
    asm volatile("buffer_load_dwordx4 a[60:63], %0, %1, %2 offen" :: "v"(vo), "s"(rof), "s"(so0 + 30720) : "memory");
    glds16(rwo, vA, sA + 15360, dA + 76800);
    asm volatile("buffer_load_dwordx4 a[64:67], %0, %1, %2 offen" :: "v"(vo), "s"(rof), "s"(so0 + 32768) : "memory");
    glds16(rwo, vA, sA + 16384, dA + 81920);
    asm volatile("buffer_load_dwordx4 a[68:71], %0, %1, %2 offen" :: "v"(vo), "s"(rof), "s"(so0 + 34816) : "memory");
    glds16(rwo, vA, sA + 17408, dA + 87040);
    glds16(rwo, vB, sB0 + 12288, dB0 + 61440);
    glds16(rwo, vB, sB1 + 12288, dB1 + 61440);
  SCHED_FENCE();
  const int mt = (xcd & 1) * 4 + (idx >> 3);
  const int ksl = xcd >> 1, mt2 = (xcd & 1) * 8 + (idx >> 2), nq = idx & 3;
  const int grp = mt2 * 4 + nq;
  unsigned* cntB = cnt + (8 + xcd * 4 + (idx >> 3)) * 64;
  unsigned* cntE = cnt + (96 + xcd * 4 + (idx >> 3)) * 64;
  const i32x4 rw2 = make_rsrc(wf2, F_ * D_ * 2);
  const i32x4 ra = make_rsrc(act, T_ * F_ * 2);
  const unsigned va = g * 512 + t16 * 16;
  const unsigned sa0 = ((mt2 * 4 + w) * 576 + ksl * 144) * 512;
  const unsigned dW2 = lds0 + w * 1024;
  unsigned gk4[9], sWt[9], sP2[7];
  gk4[0] = (((w >> 1) ? 2 : 0) * 2 + (w & 1)) * 1024;
  gk4[1] = (((w >> 1) ? 6 : 4) * 2 + (w & 1)) * 1024;
  gk4[2] = (((w >> 1) ? 9 : 8) * 2 + (w & 1)) * 1024;
  gk4[3] = (((w >> 1) ? 13 : 11) * 2 + (w & 1)) * 1024;
  gk4[4] = (((w >> 1) ? 17 : 15) * 2 + (w & 1)) * 1024;
  gk4[5] = (((w >> 1) ? 3 : 1) * 2 + (w & 1)) * 1024;
  gk4[6] = (((w >> 1) ? 7 : 5) * 2 + (w & 1)) * 1024;
  gk4[7] = (((w >> 1) ? 12 : 10) * 2 + (w & 1)) * 1024;
  gk4[8] = (((w >> 1) ? 16 : 14) * 2 + (w & 1)) * 1024;
#pragma unroll
  for (int t = 0; t < 9; ++t) {
    const int j = (t < 6) ? ((2 * ksl + 2 + t) & 7) : (t == 6 ? 8 : 2 * ksl + t - 7);
    sWt[t] = ((nq * 9 + j) * 144 + ksl * 36) * 1024;
    if (t < 6) sP2[t] = (((grp * 4 + (j >> 1)) * 4 + ksl) * 9 + (j & 1) * 4 + w) * 1024;
  }
  sP2[6] = (w == ksl) ? 0x40000000u : (unsigned)((((grp * 4 + w) * 4 + ksl) * 9 + 8) * 1024);
  {
  const int ns = (xcd >> 1) * 8 + (idx & 7);
  const int nq9 = (ns >> 2) * 9, nr = ns & 3;
  const i32x4 rw = make_rsrc(w < 2 ? wg : wu, D_ * F_ * 2);
  const i32x4 rt = make_rsrc(hc, T_ * D_ * 2);
  const i32x4 rs = make_rsrc(ssq, T_ * 8 * 4);
  const i32x4 rd = make_rsrc(wd, F_ * D_ * 2);
  const int lup = w >> 1;
  unsigned vW4, vW2, vW1;
  {
    const int r4 = 8 * (w & 1) + (lane >> 3);
    vW4 = r4 * (F_ * 2) + (((lane & 7) ^ ((((r4 >> 1) & 1) | (lup << 1)) << 1)) * 16);
    vW2 = (16 * (w & 1) + (lane >> 2)) * (F_ * 2) + (((lane & 3) ^ (lup << 1)) * 16);
    vW1 = (32 * (w & 1) + (lane >> 1)) * (F_ * 2) + (lane & 1) * 16;
  }
  const unsigned scA = (nq9 + 2 * nr) * 128, scB = scA + 128, scC = (nq9 + 8) * 128 + nr * 32;
  const int rot = ((idx & 7) + 2 * (idx >> 3)) % 9;
  unsigned gw[9], gt[9];
#pragma unroll
  for (int G = 0; G < 9; ++G) {
    const int v = (G + rot >= 9) ? G + rot - 9 : G + rot;
    gw[G] = v * (64 * F_ * 2);
    gt[G] = (mt * 8 + 2 * w) * (72 * 512) + v * 4096;
  }
  const unsigned ssq0 = (mt * 256 + w * 64) * 32, vsq = (lane & 31) * 32;
  const unsigned dW = lds0 + w * 1024, dX = lds0 + w * 8192;
  f32x4 sqv[2][2];
  float ca0[2], ca1[2], cv[2];
#define G1_ROWCONST(tb) do { \
      const f32x4 u0 = sqv[tb][0], u1 = sqv[tb][1]; \
      float s = ((u0[0] + u0[1]) + (u0[2] + u0[3])) + ((u1[0] + u1[1]) + (u1[2] + u1[3])); \
      float var = s * (1.0f / D_) + 1e-5f; \
      float r = __builtin_amdgcn_rsqf(var); \
      const float c0 = -2.0f * 1.4426950408889634f * 0.7978845608028654f; \
      ca0[tb] = c0 * r; ca1[tb] = c0 * 0.044715f * r * r * r; cv[tb] = var; \
    } while (0)
  {
    const unsigned la = lA + vlin, la2 = la + 46080;
    f32x4 acc[5];
    bf16x8 wf[10];
    wait_vm_barrier<28>();
    wf[0] = lds_frag(la + 0);
    wf[1] = lds_frag(la + 1024);
    wf[2] = lds_frag(la + 2048);
    wf[3] = lds_frag(la + 3072);
    wf[4] = lds_frag(la + 4096);
    asm volatile("v_mfma_f32_16x16x32_bf16 %0, %1, a[0:3], 0" : "=&v"(acc[0]) : "v"(wf[0]));
    wf[5] = lds_frag(la + 5120);
    SCHED_FENCE();
    asm volatile("v_mfma_f32_16x16x32_bf16 %0, %1, a[0:3], 0" : "=&v"(acc[1]) : "v"(wf[1]));
    wf[6] = lds_frag(la + 6144);
    asm volatile("buffer_load_dwordx2 %0, %1, %2, %3 offen offset:0" : "=v"(xv[0]) : "v"(vx), "s"(rx), "s"(sx) : "memory");
    SCHED_FENCE();
    asm volatile("v_mfma_f32_16x16x32_bf16 %0, %1, a[0:3], 0" : "=&v"(acc[2]) : "v"(wf[2]));
    wf[7] = lds_frag(la + 7168);
    SCHED_FENCE();
    asm volatile("v_mfma_f32_16x16x32_bf16 %0, %1, a[0:3], 0" : "=&v"(acc[3]) : "v"(wf[3]));
    wf[8] = lds_frag(la + 8192);
    asm volatile("buffer_load_dwordx2 %0, %1, %2, %3 offen offset:32" : "=v"(xv[1]) : "v"(vx), "s"(rx), "s"(sx) : "memory");
    SCHED_FENCE();
    asm volatile("v_mfma_f32_16x16x32_bf16 %0, %1, a[0:3], 0" : "=&v"(acc[4]) : "v"(wf[4]));
    wf[9] = lds_frag(la + 9216);
    SCHED_FENCE();
    asm volatile("v_mfma_f32_16x16x32_bf16 %0, %1, a[4:7], %0" : "+v"(acc[0]) : "v"(wf[5]));
    wf[0] = lds_frag(la + 10240);
    asm volatile("buffer_load_dwordx2 %0, %1, %2, %3 offen offset:64" : "=v"(xv[2]) : "v"(vx), "s"(rx), "s"(sx) : "memory");
    SCHED_FENCE();
    asm volatile("v_mfma_f32_16x16x32_bf16 %0, %1, a[4:7], %0" : "+v"(acc[1]) : "v"(wf[6]));
    wf[1] = lds_frag(la + 11264);
    SCHED_FENCE();
    asm volatile("v_mfma_f32_16x16x32_bf16 %0, %1, a[4:7], %0" : "+v"(acc[2]) : "v"(wf[7]));
    wf[2] = lds_frag(la + 12288);
    asm volatile("buffer_load_dwordx2 %0, %1, %2, %3 offen offset:96" : "=v"(xv[3]) : "v"(vx), "s"(rx), "s"(sx) : "memory");
    SCHED_FENCE();
    asm volatile("v_mfma_f32_16x16x32_bf16 %0, %1, a[4:7], %0" : "+v"(acc[3]) : "v"(wf[8]));
    wf[3] = lds_frag(la + 13312);
    SCHED_FENCE();
    asm volatile("v_mfma_f32_16x16x32_bf16 %0, %1, a[4:7], %0" : "+v"(acc[4]) : "v"(wf[9]));
    wf[4] = lds_frag(la + 14336);
    asm volatile("buffer_load_dwordx2 %0, %1, %2, %3 offen offset:128" : "=v"(xv[4]) : "v"(vx), "s"(rx), "s"(sx) : "memory");
    SCHED_FENCE();
    asm volatile("v_mfma_f32_16x16x32_bf16 %0, %1, a[8:11], %0" : "+v"(acc[0]) : "v"(wf[0]));
    wf[5] = lds_frag(la + 15360);
    SCHED_FENCE();
    asm volatile("v_mfma_f32_16x16x32_bf16 %0, %1, a[8:11], %0" : "+v"(acc[1]) : "v"(wf[1]));
    wf[6] = lds_frag(la + 16384);
    asm volatile("buffer_load_dword %0, %1, %2, %3 offen" : "=v"(mxv) : "v"(t16 * 4), "s"(rmx), "s"((rta * 64 + w * 16) * 4) : "memory");
    SCHED_FENCE();
    asm volatile("v_mfma_f32_16x16x32_bf16 %0, %1, a[8:11], %0" : "+v"(acc[2]) : "v"(wf[2]));
    wf[7] = lds_frag(la + 17408);
    SCHED_FENCE();
    asm volatile("v_mfma_f32_16x16x32_bf16 %0, %1, a[8:11], %0" : "+v"(acc[3]) : "v"(wf[3]));
    wf[8] = lds_frag(la + 18432);
    glds16(rw, vW4, gw[0] + scA + 0, dW + 0);
    SCHED_FENCE();
    asm volatile("v_mfma_f32_16x16x32_bf16 %0, %1, a[8:11], %0" : "+v"(acc[4]) : "v"(wf[4]));
    wf[9] = lds_frag(la + 19456);
    SCHED_FENCE();
    asm volatile("v_mfma_f32_16x16x32_bf16 %0, %1, a[12:15], %0" : "+v"(acc[0]) : "v"(wf[5]));
    wf[0] = lds_frag(la + 20480);
    glds16(rw, vW4, gw[0] + scA + 147456, dW + 4096);
    SCHED_FENCE();
    asm volatile("v_mfma_f32_16x16x32_bf16 %0, %1, a[12:15], %0" : "+v"(acc[1]) : "v"(wf[6]));
    wf[1] = lds_frag(la + 21504);
    SCHED_FENCE();
    asm volatile("v_mfma_f32_16x16x32_bf16 %0, %1, a[12:15], %0" : "+v"(acc[2]) : "v"(wf[7]));
    wf[2] = lds_frag(la + 22528);
    glds16(rw, vW4, gw[0] + scA + 294912, dW + 8192);
    SCHED_FENCE();
    asm volatile("v_mfma_f32_16x16x32_bf16 %0, %1, a[12:15], %0" : "+v"(acc[3]) : "v"(wf[8]));
    wf[3] = lds_frag(la + 23552);
    SCHED_FENCE();
    asm volatile("v_mfma_f32_16x16x32_bf16 %0, %1, a[12:15], %0" : "+v"(acc[4]) : "v"(wf[9]));
    wf[4] = lds_frag(la + 24576);
    glds16(rw, vW4, gw[0] + scA + 442368, dW + 12288);
    SCHED_FENCE();
    asm volatile("v_mfma_f32_16x16x32_bf16 %0, %1, a[16:19], %0" : "+v"(acc[0]) : "v"(wf[0]));
    wf[5] = lds_frag(la + 25600);
    SCHED_FENCE();
    asm volatile("v_mfma_f32_16x16x32_bf16 %0, %1, a[16:19], %0" : "+v"(acc[1]) : "v"(wf[1]));
    wf[6] = lds_frag(la + 26624);
    glds16(rw, vW4, gw[1] + scA + 0, dW + 16384);
    SCHED_FENCE();
    asm volatile("v_mfma_f32_16x16x32_bf16 %0, %1, a[16:19], %0" : "+v"(acc[2]) : "v"(wf[2]));
    wf[7] = lds_frag(la + 27648);
    SCHED_FENCE();
    asm volatile("v_mfma_f32_16x16x32_bf16 %0, %1, a[16:19], %0" : "+v"(acc[3]) : "v"(wf[3]));
    wf[8] = lds_frag(la + 28672);
    glds16(rw, vW4, gw[1] + scA + 147456, dW + 20480);
    SCHED_FENCE();
    asm volatile("v_mfma_f32_16x16x32_bf16 %0, %1, a[16:19], %0" : "+v"(acc[4]) : "v"(wf[4]));
    wf[9] = lds_frag(la + 29696);
    SCHED_FENCE();
    asm volatile("v_mfma_f32_16x16x32_bf16 %0, %1, a[20:23], %0" : "+v"(acc[0]) : "v"(wf[5]));
    glds16(rw, vW4, gw[1] + scA + 294912, dW + 24576);
    SCHED_FENCE();
    asm volatile("v_mfma_f32_16x16x32_bf16 %0, %1, a[20:23], %0" : "+v"(acc[1]) : "v"(wf[6]));
    SCHED_FENCE();
    asm volatile("v_mfma_f32_16x16x32_bf16 %0, %1, a[20:23], %0" : "+v"(acc[2]) : "v"(wf[7]));
    glds16(rw, vW4, gw[1] + scA + 442368, dW + 28672);
    SCHED_FENCE();
    asm volatile("v_mfma_f32_16x16x32_bf16 %0, %1, a[20:23], %0" : "+v"(acc[3]) : "v"(wf[8]));
    SCHED_FENCE();
    asm volatile("v_mfma_f32_16x16x32_bf16 %0, %1, a[20:23], %0" : "+v"(acc[4]) : "v"(wf[9]));
    glds16(rw, vW4, gw[2] + scA + 0, dW + 32768);
    SCHED_FENCE();
    wait_vm_barrier<29>();
    wf[0] = lds_frag(la + 30720);
    wf[1] = lds_frag(la + 31744);
    wf[2] = lds_frag(la + 32768);
    wf[3] = lds_frag(la + 33792);
    wf[4] = lds_frag(la + 34816);
    asm volatile("v_mfma_f32_16x16x32_bf16 %0, %1, a[24:27], %0" : "+v"(acc[0]) : "v"(wf[0]));
    wf[5] = lds_frag(la + 35840);
    SCHED_FENCE();
    asm volatile("v_mfma_f32_16x16x32_bf16 %0, %1, a[24:27], %0" : "+v"(acc[1]) : "v"(wf[1]));
    wf[6] = lds_frag(la + 36864);
    glds16(rw, vW4, gw[2] + scA + 147456, dW + 36864);
    SCHED_FENCE();
    asm volatile("v_mfma_f32_16x16x32_bf16 %0, %1, a[24:27], %0" : "+v"(acc[2]) : "v"(wf[2]));
    wf[7] = lds_frag(la + 37888);
    SCHED_FENCE();
    asm volatile("v_mfma_f32_16x16x32_bf16 %0, %1, a[24:27], %0" : "+v"(acc[3]) : "v"(wf[3]));
    wf[8] = lds_frag(la + 38912);
    glds16(rw, vW4, gw[2] + scA + 294912, dW + 40960);
    SCHED_FENCE();
    asm volatile("v_mfma_f32_16x16x32_bf16 %0, %1, a[24:27], %0" : "+v"(acc[4]) : "v"(wf[4]));
    wf[9] = lds_frag(la + 39936);
    SCHED_FENCE();
    asm volatile("v_mfma_f32_16x16x32_bf16 %0, %1, a[28:31], %0" : "+v"(acc[0]) : "v"(wf[5]));
    wf[0] = lds_frag(la + 40960);
    glds16(rw, vW4, gw[2] + scA + 442368, dW + 45056);
    SCHED_FENCE();
    asm volatile("v_mfma_f32_16x16x32_bf16 %0, %1, a[28:31], %0" : "+v"(acc[1]) : "v"(wf[6]));
    wf[1] = lds_frag(la + 41984);
    SCHED_FENCE();
    asm volatile("v_mfma_f32_16x16x32_bf16 %0, %1, a[28:31], %0" : "+v"(acc[2]) : "v"(wf[7]));
    wf[2] = lds_frag(la + 43008);
    glds16(rw, vW4, gw[3] + scA + 0, dW + 49152);
    SCHED_FENCE();
    asm volatile("v_mfma_f32_16x16x32_bf16 %0, %1, a[28:31], %0" : "+v"(acc[3]) : "v"(wf[8]));
    wf[3] = lds_frag(la + 44032);
    SCHED_FENCE();
    asm volatile("v_mfma_f32_16x16x32_bf16 %0, %1, a[28:31], %0" : "+v"(acc[4]) : "v"(wf[9]));
    wf[4] = lds_frag(la + 45056);
    glds16(rw, vW4, gw[3] + scA + 147456, dW + 53248);
    SCHED_FENCE();
    asm volatile("v_mfma_f32_16x16x32_bf16 %0, %1, a[32:35], %0" : "+v"(acc[0]) : "v"(wf[0]));
    wf[5] = lds_frag(la2 + 0);
    SCHED_FENCE();
    asm volatile("v_mfma_f32_16x16x32_bf16 %0, %1, a[32:35], %0" : "+v"(acc[1]) : "v"(wf[1]));
    wf[6] = lds_frag(la2 + 1024);
    glds16(rw, vW4, gw[3] + scA + 294912, dW + 57344);
    SCHED_FENCE();
    asm volatile("v_mfma_f32_16x16x32_bf16 %0, %1, a[32:35], %0" : "+v"(acc[2]) : "v"(wf[2]));
    wf[7] = lds_frag(la2 + 2048);
    SCHED_FENCE();
    asm volatile("v_mfma_f32_16x16x32_bf16 %0, %1, a[32:35], %0" : "+v"(acc[3]) : "v"(wf[3]));
    wf[8] = lds_frag(la2 + 3072);
    glds16(rw, vW4, gw[3] + scA + 442368, dW + 61440);
    SCHED_FENCE();
    asm volatile("v_mfma_f32_16x16x32_bf16 %0, %1, a[32:35], %0" : "+v"(acc[4]) : "v"(wf[4]));
    wf[9] = lds_frag(la2 + 4096);
    SCHED_FENCE();
    asm volatile("v_mfma_f32_16x16x32_bf16 %0, %1, a[36:39], %0" : "+v"(acc[0]) : "v"(wf[5]));
    wf[0] = lds_frag(la2 + 5120);
    SCHED_FENCE();
    asm volatile("v_mfma_f32_16x16x32_bf16 %0, %1, a[36:39], %0" : "+v"(acc[1]) : "v"(wf[6]));
    wf[1] = lds_frag(la2 + 6144);
    SCHED_FENCE();
    asm volatile("v_mfma_f32_16x16x32_bf16 %0, %1, a[36:39], %0" : "+v"(acc[2]) : "v"(wf[7]));
    wf[2] = lds_frag(la2 + 7168);
    SCHED_FENCE();
    asm volatile("v_mfma_f32_16x16x32_bf16 %0, %1, a[36:39], %0" : "+v"(acc[3]) : "v"(wf[8]));
    wf[3] = lds_frag(la2 + 8192);
    SCHED_FENCE();
    asm volatile("v_mfma_f32_16x16x32_bf16 %0, %1, a[36:39], %0" : "+v"(acc[4]) : "v"(wf[9]));
    wf[4] = lds_frag(la2 + 9216);
    SCHED_FENCE();
    asm volatile("v_mfma_f32_16x16x32_bf16 %0, %1, a[40:43], %0" : "+v"(acc[0]) : "v"(wf[0]));
    wf[5] = lds_frag(la2 + 10240);
    SCHED_FENCE();
    asm volatile("v_mfma_f32_16x16x32_bf16 %0, %1, a[40:43], %0" : "+v"(acc[1]) : "v"(wf[1]));
    wf[6] = lds_frag(la2 + 11264);
    SCHED_FENCE();
    asm volatile("v_mfma_f32_16x16x32_bf16 %0, %1, a[40:43], %0" : "+v"(acc[2]) : "v"(wf[2]));
    wf[7] = lds_frag(la2 + 12288);
    SCHED_FENCE();
    asm volatile("v_mfma_f32_16x16x32_bf16 %0, %1, a[40:43], %0" : "+v"(acc[3]) : "v"(wf[3]));
    wf[8] = lds_frag(la2 + 13312);
    SCHED_FENCE();
    asm volatile("v_mfma_f32_16x16x32_bf16 %0, %1, a[40:43], %0" : "+v"(acc[4]) : "v"(wf[4]));
    wf[9] = lds_frag(la2 + 14336);
    SCHED_FENCE();
    asm volatile("v_mfma_f32_16x16x32_bf16 %0, %1, a[44:47], %0" : "+v"(acc[0]) : "v"(wf[5]));
    SCHED_FENCE();
    asm volatile("v_mfma_f32_16x16x32_bf16 %0, %1, a[44:47], %0" : "+v"(acc[1]) : "v"(wf[6]));
    SCHED_FENCE();
    asm volatile("v_mfma_f32_16x16x32_bf16 %0, %1, a[44:47], %0" : "+v"(acc[2]) : "v"(wf[7]));
    SCHED_FENCE();
    asm volatile("v_mfma_f32_16x16x32_bf16 %0, %1, a[44:47], %0" : "+v"(acc[3]) : "v"(wf[8]));
    SCHED_FENCE();
    asm volatile("v_mfma_f32_16x16x32_bf16 %0, %1, a[44:47], %0" : "+v"(acc[4]) : "v"(wf[9]));
    SCHED_FENCE();
    wait_vm_barrier<16>();
    wf[0] = lds_frag(la2 + 15360);
    wf[1] = lds_frag(la2 + 16384);
    wf[2] = lds_frag(la2 + 17408);
    wf[3] = lds_frag(la2 + 18432);
    wf[4] = lds_frag(la2 + 19456);
    asm volatile("v_mfma_f32_16x16x32_bf16 %0, %1, a[48:51], %0" : "+v"(acc[0]) : "v"(wf[0]));
    wf[5] = lds_frag(la2 + 20480);
    SCHED_FENCE();
    asm volatile("v_mfma_f32_16x16x32_bf16 %0, %1, a[48:51], %0" : "+v"(acc[1]) : "v"(wf[1]));
    wf[6] = lds_frag(la2 + 21504);
    SCHED_FENCE();
    asm volatile("v_mfma_f32_16x16x32_bf16 %0, %1, a[48:51], %0" : "+v"(acc[2]) : "v"(wf[2]));
    wf[7] = lds_frag(la2 + 22528);
    SCHED_FENCE();
    asm volatile("v_mfma_f32_16x16x32_bf16 %0, %1, a[48:51], %0" : "+v"(acc[3]) : "v"(wf[3]));
    wf[8] = lds_frag(la2 + 23552);
    SCHED_FENCE();
    asm volatile("v_mfma_f32_16x16x32_bf16 %0, %1, a[48:51], %0" : "+v"(acc[4]) : "v"(wf[4]));
    wf[9] = lds_frag(la2 + 24576);
    SCHED_FENCE();
    asm volatile("v_mfma_f32_16x16x32_bf16 %0, %1, a[52:55], %0" : "+v"(acc[0]) : "v"(wf[5]));
    wf[0] = lds_frag(la2 + 25600);
    SCHED_FENCE();
    asm volatile("v_mfma_f32_16x16x32_bf16 %0, %1, a[52:55], %0" : "+v"(acc[1]) : "v"(wf[6]));
    wf[1] = lds_frag(la2 + 26624);
    SCHED_FENCE();
    asm volatile("v_mfma_f32_16x16x32_bf16 %0, %1, a[52:55], %0" : "+v"(acc[2]) : "v"(wf[7]));
    wf[2] = lds_frag(la2 + 27648);
    SCHED_FENCE();
    asm volatile("v_mfma_f32_16x16x32_bf16 %0, %1, a[52:55], %0" : "+v"(acc[3]) : "v"(wf[8]));
    wf[3] = lds_frag(la2 + 28672);
    SCHED_FENCE();
    asm volatile("v_mfma_f32_16x16x32_bf16 %0, %1, a[52:55], %0" : "+v"(acc[4]) : "v"(wf[9]));
    wf[4] = lds_frag(la2 + 29696);
    SCHED_FENCE();
    asm volatile("v_mfma_f32_16x16x32_bf16 %0, %1, a[56:59], %0" : "+v"(acc[0]) : "v"(wf[0]));
    wf[5] = lds_frag(la2 + 30720);
    SCHED_FENCE();
    asm volatile("v_mfma_f32_16x16x32_bf16 %0, %1, a[56:59], %0" : "+v"(acc[1]) : "v"(wf[1]));
    wf[6] = lds_frag(la2 + 31744);
    SCHED_FENCE();
    asm volatile("v_mfma_f32_16x16x32_bf16 %0, %1, a[56:59], %0" : "+v"(acc[2]) : "v"(wf[2]));
    wf[7] = lds_frag(la2 + 32768);
    SCHED_FENCE();
    asm volatile("v_mfma_f32_16x16x32_bf16 %0, %1, a[56:59], %0" : "+v"(acc[3]) : "v"(wf[3]));
    wf[8] = lds_frag(la2 + 33792);
    SCHED_FENCE();
    asm volatile("v_mfma_f32_16x16x32_bf16 %0, %1, a[56:59], %0" : "+v"(acc[4]) : "v"(wf[4]));
    wf[9] = lds_frag(la2 + 34816);
    SCHED_FENCE();
    asm volatile("v_mfma_f32_16x16x32_bf16 %0, %1, a[60:63], %0" : "+v"(acc[0]) : "v"(wf[5]));
    wf[0] = lds_frag(la2 + 35840);
    SCHED_FENCE();
    asm volatile("v_mfma_f32_16x16x32_bf16 %0, %1, a[60:63], %0" : "+v"(acc[1]) : "v"(wf[6]));
    wf[1] = lds_frag(la2 + 36864);
    SCHED_FENCE();
    asm volatile("v_mfma_f32_16x16x32_bf16 %0, %1, a[60:63], %0" : "+v"(acc[2]) : "v"(wf[7]));
    wf[2] = lds_frag(la2 + 37888);
    SCHED_FENCE();
    asm volatile("v_mfma_f32_16x16x32_bf16 %0, %1, a[60:63], %0" : "+v"(acc[3]) : "v"(wf[8]));
    wf[3] = lds_frag(la2 + 38912);
    SCHED_FENCE();
    asm volatile("v_mfma_f32_16x16x32_bf16 %0, %1, a[60:63], %0" : "+v"(acc[4]) : "v"(wf[9]));
    wf[4] = lds_frag(la2 + 39936);
    SCHED_FENCE();
    asm volatile("v_mfma_f32_16x16x32_bf16 %0, %1, a[64:67], %0" : "+v"(acc[0]) : "v"(wf[0]));
    wf[5] = lds_frag(la2 + 40960);
    SCHED_FENCE();
    asm volatile("v_mfma_f32_16x16x32_bf16 %0, %1, a[64:67], %0" : "+v"(acc[1]) : "v"(wf[1]));
    wf[6] = lds_frag(la2 + 41984);
    SCHED_FENCE();
    asm volatile("v_mfma_f32_16x16x32_bf16 %0, %1, a[64:67], %0" : "+v"(acc[2]) : "v"(wf[2]));
    wf[7] = lds_frag(la2 + 43008);
    SCHED_FENCE();
    asm volatile("v_mfma_f32_16x16x32_bf16 %0, %1, a[64:67], %0" : "+v"(acc[3]) : "v"(wf[3]));
    wf[8] = lds_frag(la2 + 44032);
    SCHED_FENCE();
    asm volatile("v_mfma_f32_16x16x32_bf16 %0, %1, a[64:67], %0" : "+v"(acc[4]) : "v"(wf[4]));
    wf[9] = lds_frag(la2 + 45056);
    SCHED_FENCE();
    asm volatile("v_mfma_f32_16x16x32_bf16 %0, %1, a[68:71], %0" : "+v"(acc[0]) : "v"(wf[5]));
    SCHED_FENCE();
    asm volatile("v_mfma_f32_16x16x32_bf16 %0, %1, a[68:71], %0" : "+v"(acc[1]) : "v"(wf[6]));
    SCHED_FENCE();
    asm volatile("v_mfma_f32_16x16x32_bf16 %0, %1, a[68:71], %0" : "+v"(acc[2]) : "v"(wf[7]));
    SCHED_FENCE();
    asm volatile("v_mfma_f32_16x16x32_bf16 %0, %1, a[68:71], %0" : "+v"(acc[3]) : "v"(wf[8]));
    SCHED_FENCE();
    asm volatile("v_mfma_f32_16x16x32_bf16 %0, %1, a[68:71], %0" : "+v"(acc[4]) : "v"(wf[9]));
    SCHED_FENCE();
    asm volatile("s_nop 7" : "+v"(acc[0]), "+v"(acc[1]), "+v"(acc[2]), "+v"(acc[3]), "+v"(acc[4]),
                 "+v"(xv[0]), "+v"(xv[1]), "+v"(xv[2]), "+v"(xv[3]), "+v"(xv[4]), "+v"(mxv));
    float mp = odd ? acc[0][0] + acc[0][1] : acc[4][0] + acc[4][1];
    mp = (g == (odd ? 0 : 2)) ? mp : 0.f;
    const float mean = __uint_as_float(mxv) + xsum32(xsum16(mp));
    float sq = 0.f;
#pragma unroll
    for (int j = 0; j < 5; ++j) {
      const int gi = 4 * j + g - 2 * odd;
      const bool on = gi >= 0 && gi < 18;
      const unsigned a0 = pack_bf16(acc[j][0], acc[j][1]), a1 = pack_bf16(acc[j][2], acc[j][3]);
      const unsigned h0 = pack_bf16(bf_lo(xv[j][0]) + bf_lo(a0), bf_hi(xv[j][0]) + bf_hi(a0));
      const unsigned h1 = pack_bf16(bf_lo(xv[j][1]) + bf_lo(a1), bf_hi(xv[j][1]) + bf_hi(a1));
      const float d0 = bf_lo(h0) - mean, d1 = bf_hi(h0) - mean, d2 = bf_lo(h1) - mean, d3 = bf_hi(h1) - mean;
      const float q = fmaf(d0, d0, d1 * d1) + fmaf(d2, d2, d3 * d3);
      sq += on ? q : 0.f;
      const u32x2 cvv = {pack_bf16(d0, d1), pack_bf16(d2, d3)};
      const unsigned vf = on ? (unsigned)((gi >> 1) * 512 + t16 * 16 + (gi & 1) * 8) : 0x40000000u;
      __builtin_amdgcn_raw_buffer_store_b64(cvv, rc, vf, so0 + nsa * 9 * 512, 16);
    }
    sq = xsum32(xsum16(sq));
    const int tok = rta * 64 + w * 16 + t16;
    if (g == 0) {
      __hip_atomic_store((gu32*)(ssq + tok * 8 + nsa), __float_as_uint(sq), __ATOMIC_RELAXED, __HIP_MEMORY_SCOPE_AGENT);
      if (nsa == 0) __hip_atomic_store((gu32*)(hmean + tok), __float_as_uint(mean), __ATOMIC_RELAXED, __HIP_MEMORY_SCOPE_AGENT);
    }

    wait_vm_barrier<0>();
    if (tid == 0) __hip_atomic_fetch_add((gu32*)(cnt + (64 + rta) * 64), 1u, __ATOMIC_RELAXED, __HIP_MEMORY_SCOPE_AGENT);
  }
  const int q4 = (lane & 15) >> 2, p4 = lane & 3;
  const int isup = g & 1, rrow = 8 * (g >> 1) + q4;
  unsigned lb4[4][3], lb2[2][3], lb1[1][3];
#pragma unroll
  for (int t = 0; t < 4; ++t)
#pragma unroll
    for (int r = 0; r < 3; ++r) {
      lb4[t][r] = lds0 + r * 57344 + isup * 2048 + rrow * 128 + ((t ^ (((q4 >> 1) & 1) | (isup << 1))) * 32) + p4 * 8;
      asm volatile("" : "+v"(lb4[t][r]));
    }
#define G1_BASES2() do { _Pragma("unroll") for (int t = 0; t < 2; ++t) _Pragma("unroll") for (int r = 0; r < 3; ++r) { \
      lb2[t][r] = lds0 + r * 57344 + isup * 2048 + rrow * 64 + ((t ^ isup) * 32) + p4 * 8; asm volatile("" : "+v"(lb2[t][r])); } } while (0)
#define G1_BASES1() do { _Pragma("unroll") for (int r = 0; r < 3; ++r) { \
      lb1[0][r] = lds0 + r * 57344 + isup * 2048 + rrow * 32 + p4 * 8; asm volatile("" : "+v"(lb1[0][r])); } } while (0)
  unsigned lp = dX + 131072 + (16 * (g >> 1) + 4 * (g & 1) + q4) * 128 + p4 * 8;
  asm volatile("" : "+v"(lp));
  const unsigned vP = (lane >> 3) * (D_ * 2) + (lane & 7) * 16;
  unsigned sP[2], oP[2];
#pragma unroll
  for (int u = 0; u < 2; ++u) {
    const int ul = ((xcd & 1) * 32 + idx) * 4 + w + 256 * u;
    const int kap = (xcd >> 1) * 36 + ul / 9, c = ul % 9;
    sP[u] = ul < 324 ? (unsigned)(32 * kap * (D_ * 2) + c * 128) : 0x40000000u;
    oP[u] = ul < 324 ? (unsigned)((4 * c * 144 + kap) * 1024) : 0x40000000u;
  }
  const __amdgpu_buffer_rsrc_t rf2 = __builtin_amdgcn_make_buffer_rsrc((void*)wf2, 0, F_ * D_ * 2, 0x00020000);
  bf16x8 wf[12], tv[2][4], pf;
  f32x16 acc[5][2];
  float e0[8], e1[8];
  unsigned pk[4];
  const __amdgpu_buffer_rsrc_t ract = __builtin_amdgcn_make_buffer_rsrc((void*)act, 0, T_ * F_ * 2, 0x00020000);
  const unsigned sactA = ((mt * 8 + 2 * w) * 576 + (nq9 + 2 * nr) * 8) * 512, sactB = sactA + 8 * 512;
  const unsigned sactC = ((mt * 8 + 2 * w) * 576 + (nq9 + 8) * 8 + nr * 2) * 512;
#define G1_SIGNALE() do { } while (0)
#define G1_SIGNALW() do { if (tid == 0) { __hip_atomic_fetch_add((gu32*)(cnt + (40 + (xcd >> 1)) * 64), 1u, __ATOMIC_RELAXED, __HIP_MEMORY_SCOPE_AGENT); \
      __hip_atomic_fetch_add((gu32*)cntE, 1u, __ATOMIC_RELAXED, __HIP_MEMORY_SCOPE_AGENT); } } while (0)
  unsigned f1a, f1b;
#define G1_LOADF1() do { f1a = __hip_atomic_load((gu32*)(cnt + (40 + (xcd >> 1)) * 64), __ATOMIC_RELAXED, __HIP_MEMORY_SCOPE_AGENT); \
      f1b = __hip_atomic_load((gu32*)cntE, __ATOMIC_RELAXED, __HIP_MEMORY_SCOPE_AGENT); \
      asm volatile("" : "+s"(sWt[0]), "+s"(sWt[1]), "+s"(sWt[2]), "+s"(sWt[3]), "+s"(sWt[4]), "+s"(sWt[5]), "+s"(sWt[6]), \
                   "+s"(gk4[0]), "+s"(gk4[1]), "+s"(gk4[2]) : "s"(rw2), "s"(ra), "s"(sa0), "s"(dW2), "v"(va)); } while (0)
  asm volatile("" : "+s"(gt[0]), "+s"(gt[1]), "+s"(gt[2]), "+s"(gt[3]), "+s"(gt[4]), "+s"(gt[5]), "+s"(gt[6]), "+s"(gt[7]), "+s"(gt[8]),
               "+s"(gw[4]), "+s"(gw[5]), "+s"(gw[6]), "+s"(gw[7]), "+s"(gw[8]), "+s"(sP[0]), "+s"(sP[1]), "+s"(oP[0]), "+s"(oP[1]) : "s"(rt), "s"(rs), "s"(rd), "v"(vW2), "v"(vW1), "v"(vP), "v"(vsq));
  wait_count(cnt + (64 + mt * 4 + w) * 64, 8u);
  asm volatile("buffer_load_dwordx4 a[0:3], %0, %1, %2 offen sc1" :: "v"(vlin), "s"(rt), "s"(gt[0] + 0) : "memory");
  asm volatile("buffer_load_dwordx4 a[4:7], %0, %1, %2 offen sc1" :: "v"(vlin), "s"(rt), "s"(gt[0] + 36864) : "memory");
  asm volatile("buffer_load_dwordx4 a[8:11], %0, %1, %2 offen sc1" :: "v"(vlin), "s"(rt), "s"(gt[0] + 1024) : "memory");
  asm volatile("buffer_load_dwordx4 a[12:15], %0, %1, %2 offen sc1" :: "v"(vlin), "s"(rt), "s"(gt[0] + 37888) : "memory");
  asm volatile("buffer_load_dwordx4 a[16:19], %0, %1, %2 offen sc1" :: "v"(vlin), "s"(rt), "s"(gt[0] + 2048) : "memory");
  asm volatile("buffer_load_dwordx4 a[20:23], %0, %1, %2 offen sc1" :: "v"(vlin), "s"(rt), "s"(gt[0] + 38912) : "memory");
  asm volatile("buffer_load_dwordx4 a[24:27], %0, %1, %2 offen sc1" :: "v"(vlin), "s"(rt), "s"(gt[0] + 3072) : "memory");
  asm volatile("buffer_load_dwordx4 a[28:31], %0, %1, %2 offen sc1" :: "v"(vlin), "s"(rt), "s"(gt[0] + 39936) : "memory");
  asm volatile("buffer_load_dwordx4 a[32:35], %0, %1, %2 offen sc1" :: "v"(vlin), "s"(rt), "s"(gt[1] + 0) : "memory");
  asm volatile("buffer_load_dwordx4 a[36:39], %0, %1, %2 offen sc1" :: "v"(vlin), "s"(rt), "s"(gt[1] + 36864) : "memory");
  asm volatile("buffer_load_dwordx4 a[40:43], %0, %1, %2 offen sc1" :: "v"(vlin), "s"(rt), "s"(gt[1] + 1024) : "memory");
  asm volatile("buffer_load_dwordx4 a[44:47], %0, %1, %2 offen sc1" :: "v"(vlin), "s"(rt), "s"(gt[1] + 37888) : "memory");
  asm volatile("buffer_load_dwordx4 a[48:51], %0, %1, %2 offen sc1" :: "v"(vlin), "s"(rt), "s"(gt[1] + 2048) : "memory");
  asm volatile("buffer_load_dwordx4 a[52:55], %0, %1, %2 offen sc1" :: "v"(vlin), "s"(rt), "s"(gt[1] + 38912) : "memory");
  asm volatile("buffer_load_dwordx4 a[56:59], %0, %1, %2 offen sc1" :: "v"(vlin), "s"(rt), "s"(gt[1] + 3072) : "memory");
  asm volatile("buffer_load_dwordx4 a[60:63], %0, %1, %2 offen sc1" :: "v"(vlin), "s"(rt), "s"(gt[1] + 39936) : "memory");
  asm volatile("buffer_load_dwordx4 a[64:67], %0, %1, %2 offen sc1" :: "v"(vlin), "s"(rt), "s"(gt[2] + 0) : "memory");
  asm volatile("buffer_load_dwordx4 a[68:71], %0, %1, %2 offen sc1" :: "v"(vlin), "s"(rt), "s"(gt[2] + 36864) : "memory");
  asm volatile("buffer_load_dwordx4 a[72:75], %0, %1, %2 offen sc1" :: "v"(vlin), "s"(rt), "s"(gt[2] + 1024) : "memory");
  asm volatile("buffer_load_dwordx4 a[76:79], %0, %1, %2 offen sc1" :: "v"(vlin), "s"(rt), "s"(gt[2] + 37888) : "memory");
  asm volatile("buffer_load_dwordx4 a[80:83], %0, %1, %2 offen sc1" :: "v"(vlin), "s"(rt), "s"(gt[2] + 2048) : "memory");
  asm volatile("buffer_load_dwordx4 a[84:87], %0, %1, %2 offen sc1" :: "v"(vlin), "s"(rt), "s"(gt[2] + 38912) : "memory");
  asm volatile("buffer_load_dwordx4 a[88:91], %0, %1, %2 offen sc1" :: "v"(vlin), "s"(rt), "s"(gt[2] + 3072) : "memory");
  asm volatile("buffer_load_dwordx4 a[92:95], %0, %1, %2 offen sc1" :: "v"(vlin), "s"(rt), "s"(gt[2] + 39936) : "memory");
  wait_vm_barrier<16>();
  wf[0] = tr_frag(lb4[0][0] + 0, lb4[0][0] + 0 + 512);
  wf[1] = tr_frag(lb4[1][0] + 0, lb4[1][0] + 0 + 512);
  wf[2] = tr_frag(lb4[2][0] + 0, lb4[2][0] + 0 + 512);
  SCHED_FENCE();
  asm volatile("v_mfma_f32_32x32x16_bf16 %0, %1, a[0:3], 0" : "=&v"(acc[0][0]) : "v"(wf[0]));
  SCHED_FENCE();
  wf[3] = tr_frag(lb4[3][0] + 0, lb4[3][0] + 0 + 512);
  SCHED_FENCE();
  asm volatile("v_mfma_f32_32x32x16_bf16 %0, %1, a[4:7], 0" : "=&v"(acc[0][1]) : "v"(wf[0]));
  SCHED_FENCE();
  asm volatile("buffer_load_dwordx4 a[96:99], %0, %1, %2 offen sc1" :: "v"(vlin), "s"(rt), "s"(gt[3] + 0) : "memory");
  SCHED_FENCE();
  asm volatile("v_mfma_f32_32x32x16_bf16 %0, %1, a[0:3], 0" : "=&v"(acc[1][0]) : "v"(wf[1]));
  SCHED_FENCE();
  wf[0] = tr_frag(lb4[0][0] + 4096, lb4[0][0] + 4096 + 512);
  SCHED_FENCE();
  asm volatile("v_mfma_f32_32x32x16_bf16 %0, %1, a[4:7], 0" : "=&v"(acc[1][1]) : "v"(wf[1]));
  SCHED_FENCE();
  asm volatile("buffer_load_dwordx4 a[100:103], %0, %1, %2 offen sc1" :: "v"(vlin), "s"(rt), "s"(gt[3] + 36864) : "memory");
  SCHED_FENCE();
  asm volatile("v_mfma_f32_32x32x16_bf16 %0, %1, a[0:3], 0" : "=&v"(acc[2][0]) : "v"(wf[2]));
  SCHED_FENCE();
  wf[1] = tr_frag(lb4[1][0] + 4096, lb4[1][0] + 4096 + 512);
  SCHED_FENCE();
  asm volatile("v_mfma_f32_32x32x16_bf16 %0, %1, a[4:7], 0" : "=&v"(acc[2][1]) : "v"(wf[2]));
  SCHED_FENCE();
  asm volatile("buffer_load_dwordx4 a[104:107], %0, %1, %2 offen sc1" :: "v"(vlin), "s"(rt), "s"(gt[3] + 1024) : "memory");
  SCHED_FENCE();
  asm volatile("v_mfma_f32_32x32x16_bf16 %0, %1, a[0:3], 0" : "=&v"(acc[3][0]) : "v"(wf[3]));
  SCHED_FENCE();
  wf[2] = tr_frag(lb4[2][0] + 4096, lb4[2][0] + 4096 + 512);
  SCHED_FENCE();
  asm volatile("v_mfma_f32_32x32x16_bf16 %0, %1, a[4:7], 0" : "=&v"(acc[3][1]) : "v"(wf[3]));
  SCHED_FENCE();
  asm volatile("buffer_load_dwordx4 a[108:111], %0, %1, %2 offen sc1" :: "v"(vlin), "s"(rt), "s"(gt[3] + 37888) : "memory");
  SCHED_FENCE();
  asm volatile("v_mfma_f32_32x32x16_bf16 %0, %1, a[8:11], %0" : "+v"(acc[0][0]) : "v"(wf[0]));
  SCHED_FENCE();
  wf[3] = tr_frag(lb4[3][0] + 4096, lb4[3][0] + 4096 + 512);
  SCHED_FENCE();
  asm volatile("v_mfma_f32_32x32x16_bf16 %0, %1, a[12:15], %0" : "+v"(acc[0][1]) : "v"(wf[0]));
  SCHED_FENCE();
  asm volatile("buffer_load_dwordx4 a[112:115], %0, %1, %2 offen sc1" :: "v"(vlin), "s"(rt), "s"(gt[3] + 2048) : "memory");
  SCHED_FENCE();
  asm volatile("v_mfma_f32_32x32x16_bf16 %0, %1, a[8:11], %0" : "+v"(acc[1][0]) : "v"(wf[1]));
  SCHED_FENCE();
  wf[0] = tr_frag(lb4[0][0] + 8192, lb4[0][0] + 8192 + 512);
  SCHED_FENCE();
  asm volatile("v_mfma_f32_32x32x16_bf16 %0, %1, a[12:15], %0" : "+v"(acc[1][1]) : "v"(wf[1]));
  SCHED_FENCE();
  asm volatile("buffer_load_dwordx4 a[116:119], %0, %1, %2 offen sc1" :: "v"(vlin), "s"(rt), "s"(gt[3] + 38912) : "memory");
  SCHED_FENCE();
  asm volatile("v_mfma_f32_32x32x16_bf16 %0, %1, a[8:11], %0" : "+v"(acc[2][0]) : "v"(wf[2]));
  SCHED_FENCE();
  wf[1] = tr_frag(lb4[1][0] + 8192, lb4[1][0] + 8192 + 512);
  SCHED_FENCE();
  asm volatile("v_mfma_f32_32x32x16_bf16 %0, %1, a[12:15], %0" : "+v"(acc[2][1]) : "v"(wf[2]));
  SCHED_FENCE();
  asm volatile("buffer_load_dwordx4 a[120:123], %0, %1, %2 offen sc1" :: "v"(vlin), "s"(rt), "s"(gt[3] + 3072) : "memory");
  SCHED_FENCE();
  asm volatile("v_mfma_f32_32x32x16_bf16 %0, %1, a[8:11], %0" : "+v"(acc[3][0]) : "v"(wf[3]));
  SCHED_FENCE();
  wf[2] = tr_frag(lb4[2][0] + 8192, lb4[2][0] + 8192 + 512);
  SCHED_FENCE();
  asm volatile("v_mfma_f32_32x32x16_bf16 %0, %1, a[12:15], %0" : "+v"(acc[3][1]) : "v"(wf[3]));
  SCHED_FENCE();
  asm volatile("buffer_load_dwordx4 a[124:127], %0, %1, %2 offen sc1" :: "v"(vlin), "s"(rt), "s"(gt[3] + 39936) : "memory");
  SCHED_FENCE();
  asm volatile("v_mfma_f32_32x32x16_bf16 %0, %1, a[16:19], %0" : "+v"(acc[0][0]) : "v"(wf[0]));
  SCHED_FENCE();
  wf[3] = tr_frag(lb4[3][0] + 8192, lb4[3][0] + 8192 + 512);
  SCHED_FENCE();
  asm volatile("v_mfma_f32_32x32x16_bf16 %0, %1, a[20:23], %0" : "+v"(acc[0][1]) : "v"(wf[0]));
  SCHED_FENCE();
  asm volatile("buffer_load_dwordx4 a[128:131], %0, %1, %2 offen sc1" :: "v"(vlin), "s"(rt), "s"(gt[4] + 0) : "memory");
  SCHED_FENCE();
  asm volatile("v_mfma_f32_32x32x16_bf16 %0, %1, a[16:19], %0" : "+v"(acc[1][0]) : "v"(wf[1]));
  SCHED_FENCE();
  wf[0] = tr_frag(lb4[0][0] + 12288, lb4[0][0] + 12288 + 512);
  SCHED_FENCE();
  asm volatile("v_mfma_f32_32x32x16_bf16 %0, %1, a[20:23], %0" : "+v"(acc[1][1]) : "v"(wf[1]));
  SCHED_FENCE();
  asm volatile("buffer_load_dwordx4 a[132:135], %0, %1, %2 offen sc1" :: "v"(vlin), "s"(rt), "s"(gt[4] + 36864) : "memory");
  SCHED_FENCE();
  asm volatile("v_mfma_f32_32x32x16_bf16 %0, %1, a[16:19], %0" : "+v"(acc[2][0]) : "v"(wf[2]));
  SCHED_FENCE();
  wf[1] = tr_frag(lb4[1][0] + 12288, lb4[1][0] + 12288 + 512);
  SCHED_FENCE();
  asm volatile("v_mfma_f32_32x32x16_bf16 %0, %1, a[20:23], %0" : "+v"(acc[2][1]) : "v"(wf[2]));
  SCHED_FENCE();
  asm volatile("buffer_load_dwordx4 a[136:139], %0, %1, %2 offen sc1" :: "v"(vlin), "s"(rt), "s"(gt[4] + 1024) : "memory");
  SCHED_FENCE();
  asm volatile("v_mfma_f32_32x32x16_bf16 %0, %1, a[16:19], %0" : "+v"(acc[3][0]) : "v"(wf[3]));
  SCHED_FENCE();
  wf[2] = tr_frag(lb4[2][0] + 12288, lb4[2][0] + 12288 + 512);
  SCHED_FENCE();
  asm volatile("v_mfma_f32_32x32x16_bf16 %0, %1, a[20:23], %0" : "+v"(acc[3][1]) : "v"(wf[3]));
  SCHED_FENCE();
  asm volatile("buffer_load_dwordx4 a[140:143], %0, %1, %2 offen sc1" :: "v"(vlin), "s"(rt), "s"(gt[4] + 37888) : "memory");
  SCHED_FENCE();
  asm volatile("v_mfma_f32_32x32x16_bf16 %0, %1, a[24:27], %0" : "+v"(acc[0][0]) : "v"(wf[0]));
  SCHED_FENCE();
  wf[3] = tr_frag(lb4[3][0] + 12288, lb4[3][0] + 12288 + 512);
  SCHED_FENCE();
  asm volatile("v_mfma_f32_32x32x16_bf16 %0, %1, a[28:31], %0" : "+v"(acc[0][1]) : "v"(wf[0]));
  SCHED_FENCE();
  asm volatile("buffer_load_dwordx4 a[144:147], %0, %1, %2 offen sc1" :: "v"(vlin), "s"(rt), "s"(gt[4] + 2048) : "memory");
  SCHED_FENCE();
  asm volatile("v_mfma_f32_32x32x16_bf16 %0, %1, a[24:27], %0" : "+v"(acc[1][0]) : "v"(wf[1]));
  SCHED_FENCE();
  wait_vm_barrier<21>();
  wf[0] = tr_frag(lb4[0][0] + 16384, lb4[0][0] + 16384 + 512);
  SCHED_FENCE();
  asm volatile("v_mfma_f32_32x32x16_bf16 %0, %1, a[28:31], %0" : "+v"(acc[1][1]) : "v"(wf[1]));
  SCHED_FENCE();
  asm volatile("buffer_load_dwordx4 a[148:151], %0, %1, %2 offen sc1" :: "v"(vlin), "s"(rt), "s"(gt[4] + 38912) : "memory");
  SCHED_FENCE();
  asm volatile("v_mfma_f32_32x32x16_bf16 %0, %1, a[24:27], %0" : "+v"(acc[2][0]) : "v"(wf[2]));
  SCHED_FENCE();
  wf[1] = tr_frag(lb4[1][0] + 16384, lb4[1][0] + 16384 + 512);
  SCHED_FENCE();
  asm volatile("v_mfma_f32_32x32x16_bf16 %0, %1, a[28:31], %0" : "+v"(acc[2][1]) : "v"(wf[2]));
  SCHED_FENCE();
  asm volatile("buffer_load_dwordx4 a[152:155], %0, %1, %2 offen sc1" :: "v"(vlin), "s"(rt), "s"(gt[4] + 3072) : "memory");
  SCHED_FENCE();
  asm volatile("v_mfma_f32_32x32x16_bf16 %0, %1, a[24:27], %0" : "+v"(acc[3][0]) : "v"(wf[3]));
  SCHED_FENCE();
  wf[2] = tr_frag(lb4[2][0] + 16384, lb4[2][0] + 16384 + 512);
  SCHED_FENCE();
  asm volatile("v_mfma_f32_32x32x16_bf16 %0, %1, a[28:31], %0" : "+v"(acc[3][1]) : "v"(wf[3]));
  SCHED_FENCE();
  asm volatile("buffer_load_dwordx4 a[156:159], %0, %1, %2 offen sc1" :: "v"(vlin), "s"(rt), "s"(gt[4] + 39936) : "memory");
  SCHED_FENCE();
  asm volatile("v_mfma_f32_32x32x16_bf16 %0, %1, a[32:35], %0" : "+v"(acc[0][0]) : "v"(wf[0]));
  SCHED_FENCE();
  wf[3] = tr_frag(lb4[3][0] + 16384, lb4[3][0] + 16384 + 512);
  SCHED_FENCE();
  asm volatile("v_mfma_f32_32x32x16_bf16 %0, %1, a[36:39], %0" : "+v"(acc[0][1]) : "v"(wf[0]));
  SCHED_FENCE();
  asm volatile("buffer_load_dwordx4 a[160:163], %0, %1, %2 offen sc1" :: "v"(vlin), "s"(rt), "s"(gt[5] + 0) : "memory");
  SCHED_FENCE();
  asm volatile("v_mfma_f32_32x32x16_bf16 %0, %1, a[32:35], %0" : "+v"(acc[1][0]) : "v"(wf[1]));
  SCHED_FENCE();
  wf[0] = tr_frag(lb4[0][0] + 20480, lb4[0][0] + 20480 + 512);
  SCHED_FENCE();
  asm volatile("v_mfma_f32_32x32x16_bf16 %0, %1, a[36:39], %0" : "+v"(acc[1][1]) : "v"(wf[1]));
  SCHED_FENCE();
  asm volatile("buffer_load_dwordx4 a[164:167], %0, %1, %2 offen sc1" :: "v"(vlin), "s"(rt), "s"(gt[5] + 36864) : "memory");
  SCHED_FENCE();
  asm volatile("v_mfma_f32_32x32x16_bf16 %0, %1, a[32:35], %0" : "+v"(acc[2][0]) : "v"(wf[2]));
  SCHED_FENCE();
  wf[1] = tr_frag(lb4[1][0] + 20480, lb4[1][0] + 20480 + 512);
  SCHED_FENCE();
  asm volatile("v_mfma_f32_32x32x16_bf16 %0, %1, a[36:39], %0" : "+v"(acc[2][1]) : "v"(wf[2]));
  SCHED_FENCE();
  asm volatile("buffer_load_dwordx4 a[168:171], %0, %1, %2 offen sc1" :: "v"(vlin), "s"(rt), "s"(gt[5] + 1024) : "memory");
  SCHED_FENCE();
  asm volatile("v_mfma_f32_32x32x16_bf16 %0, %1, a[32:35], %0" : "+v"(acc[3][0]) : "v"(wf[3]));
  SCHED_FENCE();
  wf[2] = tr_frag(lb4[2][0] + 20480, lb4[2][0] + 20480 + 512);
  SCHED_FENCE();
  asm volatile("v_mfma_f32_32x32x16_bf16 %0, %1, a[36:39], %0" : "+v"(acc[3][1]) : "v"(wf[3]));
  SCHED_FENCE();
  asm volatile("buffer_load_dwordx4 a[172:175], %0, %1, %2 offen sc1" :: "v"(vlin), "s"(rt), "s"(gt[5] + 37888) : "memory");
  SCHED_FENCE();
  asm volatile("v_mfma_f32_32x32x16_bf16 %0, %1, a[40:43], %0" : "+v"(acc[0][0]) : "v"(wf[0]));
  SCHED_FENCE();
  wf[3] = tr_frag(lb4[3][0] + 20480, lb4[3][0] + 20480 + 512);
  SCHED_FENCE();
  asm volatile("v_mfma_f32_32x32x16_bf16 %0, %1, a[44:47], %0" : "+v"(acc[0][1]) : "v"(wf[0]));
  SCHED_FENCE();
  asm volatile("buffer_load_dwordx4 a[176:179], %0, %1, %2 offen sc1" :: "v"(vlin), "s"(rt), "s"(gt[5] + 2048) : "memory");
  SCHED_FENCE();
  asm volatile("v_mfma_f32_32x32x16_bf16 %0, %1, a[40:43], %0" : "+v"(acc[1][0]) : "v"(wf[1]));
  SCHED_FENCE();
  wf[0] = tr_frag(lb4[0][0] + 24576, lb4[0][0] + 24576 + 512);
  SCHED_FENCE();
  asm volatile("v_mfma_f32_32x32x16_bf16 %0, %1, a[44:47], %0" : "+v"(acc[1][1]) : "v"(wf[1]));
  SCHED_FENCE();
  asm volatile("buffer_load_dwordx4 a[180:183], %0, %1, %2 offen sc1" :: "v"(vlin), "s"(rt), "s"(gt[5] + 38912) : "memory");
  SCHED_FENCE();
  asm volatile("v_mfma_f32_32x32x16_bf16 %0, %1, a[40:43], %0" : "+v"(acc[2][0]) : "v"(wf[2]));
  SCHED_FENCE();
  wf[1] = tr_frag(lb4[1][0] + 24576, lb4[1][0] + 24576 + 512);
  SCHED_FENCE();
  asm volatile("v_mfma_f32_32x32x16_bf16 %0, %1, a[44:47], %0" : "+v"(acc[2][1]) : "v"(wf[2]));
  SCHED_FENCE();
  asm volatile("buffer_load_dwordx4 a[184:187], %0, %1, %2 offen sc1" :: "v"(vlin), "s"(rt), "s"(gt[5] + 3072) : "memory");
  SCHED_FENCE();
  asm volatile("v_mfma_f32_32x32x16_bf16 %0, %1, a[40:43], %0" : "+v"(acc[3][0]) : "v"(wf[3]));
  SCHED_FENCE();
  wf[2] = tr_frag(lb4[2][0] + 24576, lb4[2][0] + 24576 + 512);
  SCHED_FENCE();
  asm volatile("v_mfma_f32_32x32x16_bf16 %0, %1, a[44:47], %0" : "+v"(acc[3][1]) : "v"(wf[3]));
  SCHED_FENCE();
  asm volatile("buffer_load_dwordx4 a[188:191], %0, %1, %2 offen sc1" :: "v"(vlin), "s"(rt), "s"(gt[5] + 39936) : "memory");
  SCHED_FENCE();
  asm volatile("v_mfma_f32_32x32x16_bf16 %0, %1, a[48:51], %0" : "+v"(acc[0][0]) : "v"(wf[0]));
  SCHED_FENCE();
  wf[3] = tr_frag(lb4[3][0] + 24576, lb4[3][0] + 24576 + 512);
  SCHED_FENCE();
  asm volatile("v_mfma_f32_32x32x16_bf16 %0, %1, a[52:55], %0" : "+v"(acc[0][1]) : "v"(wf[0]));
  SCHED_FENCE();
  asm volatile("buffer_load_dwordx4 %0, %1, %2, %3 offen offset:0 sc1" : "=v"(sqv[0][0]) : "v"(vsq), "s"(rs), "s"(ssq0) : "memory");
  SCHED_FENCE();
  asm volatile("v_mfma_f32_32x32x16_bf16 %0, %1, a[48:51], %0" : "+v"(acc[1][0]) : "v"(wf[1]));
  SCHED_FENCE();
  wf[0] = tr_frag(lb4[0][0] + 28672, lb4[0][0] + 28672 + 512);
  SCHED_FENCE();
  asm volatile("v_mfma_f32_32x32x16_bf16 %0, %1, a[52:55], %0" : "+v"(acc[1][1]) : "v"(wf[1]));
  SCHED_FENCE();
  asm volatile("buffer_load_dwordx4 %0, %1, %2, %3 offen offset:16 sc1" : "=v"(sqv[0][1]) : "v"(vsq), "s"(rs), "s"(ssq0) : "memory");
  SCHED_FENCE();
  asm volatile("v_mfma_f32_32x32x16_bf16 %0, %1, a[48:51], %0" : "+v"(acc[2][0]) : "v"(wf[2]));
  SCHED_FENCE();
  wf[1] = tr_frag(lb4[1][0] + 28672, lb4[1][0] + 28672 + 512);
  SCHED_FENCE();
  asm volatile("v_mfma_f32_32x32x16_bf16 %0, %1, a[52:55], %0" : "+v"(acc[2][1]) : "v"(wf[2]));
  SCHED_FENCE();
  asm volatile("buffer_load_dwordx4 %0, %1, %2, %3 offen offset:1024 sc1" : "=v"(sqv[1][0]) : "v"(vsq), "s"(rs), "s"(ssq0) : "memory");
  SCHED_FENCE();
  asm volatile("v_mfma_f32_32x32x16_bf16 %0, %1, a[48:51], %0" : "+v"(acc[3][0]) : "v"(wf[3]));
  SCHED_FENCE();
  wf[2] = tr_frag(lb4[2][0] + 28672, lb4[2][0] + 28672 + 512);
  SCHED_FENCE();
  asm volatile("v_mfma_f32_32x32x16_bf16 %0, %1, a[52:55], %0" : "+v"(acc[3][1]) : "v"(wf[3]));
  SCHED_FENCE();
  asm volatile("buffer_load_dwordx4 %0, %1, %2, %3 offen offset:1040 sc1" : "=v"(sqv[1][1]) : "v"(vsq), "s"(rs), "s"(ssq0) : "memory");
  SCHED_FENCE();
  asm volatile("v_mfma_f32_32x32x16_bf16 %0, %1, a[56:59], %0" : "+v"(acc[0][0]) : "v"(wf[0]));
  SCHED_FENCE();
  wf[3] = tr_frag(lb4[3][0] + 28672, lb4[3][0] + 28672 + 512);
  SCHED_FENCE();
  asm volatile("v_mfma_f32_32x32x16_bf16 %0, %1, a[60:63], %0" : "+v"(acc[0][1]) : "v"(wf[0]));
  SCHED_FENCE();
  asm volatile("buffer_load_dwordx4 a[192:195], %0, %1, %2 offen sc1" :: "v"(vlin), "s"(rt), "s"(gt[6] + 0) : "memory");
  SCHED_FENCE();
  asm volatile("v_mfma_f32_32x32x16_bf16 %0, %1, a[56:59], %0" : "+v"(acc[1][0]) : "v"(wf[1]));
  SCHED_FENCE();
  wait_vm_barrier<29>();
  wf[0] = tr_frag(lb4[0][0] + 32768, lb4[0][0] + 32768 + 512);
  SCHED_FENCE();
  asm volatile("v_mfma_f32_32x32x16_bf16 %0, %1, a[60:63], %0" : "+v"(acc[1][1]) : "v"(wf[1]));
  SCHED_FENCE();
  asm volatile("buffer_load_dwordx4 a[196:199], %0, %1, %2 offen sc1" :: "v"(vlin), "s"(rt), "s"(gt[6] + 36864) : "memory");
  SCHED_FENCE();
  asm volatile("v_mfma_f32_32x32x16_bf16 %0, %1, a[56:59], %0" : "+v"(acc[2][0]) : "v"(wf[2]));
  SCHED_FENCE();
  wf[1] = tr_frag(lb4[1][0] + 32768, lb4[1][0] + 32768 + 512);
  SCHED_FENCE();
  asm volatile("v_mfma_f32_32x32x16_bf16 %0, %1, a[60:63], %0" : "+v"(acc[2][1]) : "v"(wf[2]));
  SCHED_FENCE();
  asm volatile("buffer_load_dwordx4 a[200:203], %0, %1, %2 offen sc1" :: "v"(vlin), "s"(rt), "s"(gt[6] + 1024) : "memory");
  SCHED_FENCE();
  asm volatile("v_mfma_f32_32x32x16_bf16 %0, %1, a[56:59], %0" : "+v"(acc[3][0]) : "v"(wf[3]));
  SCHED_FENCE();
  wf[2] = tr_frag(lb4[2][0] + 32768, lb4[2][0] + 32768 + 512);
  SCHED_FENCE();
  asm volatile("v_mfma_f32_32x32x16_bf16 %0, %1, a[60:63], %0" : "+v"(acc[3][1]) : "v"(wf[3]));
  SCHED_FENCE();
  asm volatile("buffer_load_dwordx4 a[204:207], %0, %1, %2 offen sc1" :: "v"(vlin), "s"(rt), "s"(gt[6] + 37888) : "memory");
  SCHED_FENCE();
  asm volatile("v_mfma_f32_32x32x16_bf16 %0, %1, a[64:67], %0" : "+v"(acc[0][0]) : "v"(wf[0]));
  SCHED_FENCE();
  wf[3] = tr_frag(lb4[3][0] + 32768, lb4[3][0] + 32768 + 512);
  SCHED_FENCE();
  asm volatile("v_mfma_f32_32x32x16_bf16 %0, %1, a[68:71], %0" : "+v"(acc[0][1]) : "v"(wf[0]));
  SCHED_FENCE();
  asm volatile("buffer_load_dwordx4 a[208:211], %0, %1, %2 offen sc1" :: "v"(vlin), "s"(rt), "s"(gt[6] + 2048) : "memory");
  SCHED_FENCE();
  asm volatile("v_mfma_f32_32x32x16_bf16 %0, %1, a[64:67], %0" : "+v"(acc[1][0]) : "v"(wf[1]));
  SCHED_FENCE();
  wf[0] = tr_frag(lb4[0][0] + 36864, lb4[0][0] + 36864 + 512);
  SCHED_FENCE();
  asm volatile("v_mfma_f32_32x32x16_bf16 %0, %1, a[68:71], %0" : "+v"(acc[1][1]) : "v"(wf[1]));
  SCHED_FENCE();
  asm volatile("buffer_load_dwordx4 a[212:215], %0, %1, %2 offen sc1" :: "v"(vlin), "s"(rt), "s"(gt[6] + 38912) : "memory");
  SCHED_FENCE();
  asm volatile("v_mfma_f32_32x32x16_bf16 %0, %1, a[64:67], %0" : "+v"(acc[2][0]) : "v"(wf[2]));
  SCHED_FENCE();
  wf[1] = tr_frag(lb4[1][0] + 36864, lb4[1][0] + 36864 + 512);
  SCHED_FENCE();
  asm volatile("v_mfma_f32_32x32x16_bf16 %0, %1, a[68:71], %0" : "+v"(acc[2][1]) : "v"(wf[2]));
  SCHED_FENCE();
  asm volatile("buffer_load_dwordx4 a[216:219], %0, %1, %2 offen sc1" :: "v"(vlin), "s"(rt), "s"(gt[6] + 3072) : "memory");
  SCHED_FENCE();
  asm volatile("v_mfma_f32_32x32x16_bf16 %0, %1, a[64:67], %0" : "+v"(acc[3][0]) : "v"(wf[3]));
  SCHED_FENCE();
  wf[2] = tr_frag(lb4[2][0] + 36864, lb4[2][0] + 36864 + 512);
  SCHED_FENCE();
  asm volatile("v_mfma_f32_32x32x16_bf16 %0, %1, a[68:71], %0" : "+v"(acc[3][1]) : "v"(wf[3]));
  SCHED_FENCE();
  asm volatile("buffer_load_dwordx4 a[220:223], %0, %1, %2 offen sc1" :: "v"(vlin), "s"(rt), "s"(gt[6] + 39936) : "memory");
  SCHED_FENCE();
  asm volatile("v_mfma_f32_32x32x16_bf16 %0, %1, a[72:75], %0" : "+v"(acc[0][0]) : "v"(wf[0]));
  SCHED_FENCE();
  wf[3] = tr_frag(lb4[3][0] + 36864, lb4[3][0] + 36864 + 512);
  SCHED_FENCE();
  asm volatile("v_mfma_f32_32x32x16_bf16 %0, %1, a[76:79], %0" : "+v"(acc[0][1]) : "v"(wf[0]));
  SCHED_FENCE();
  glds16(rw, vW4, gw[4] + scA + 0, dW + 65536);
  SCHED_FENCE();
  asm volatile("v_mfma_f32_32x32x16_bf16 %0, %1, a[72:75], %0" : "+v"(acc[1][0]) : "v"(wf[1]));
  SCHED_FENCE();
  wf[0] = tr_frag(lb4[0][0] + 40960, lb4[0][0] + 40960 + 512);
  SCHED_FENCE();
  asm volatile("v_mfma_f32_32x32x16_bf16 %0, %1, a[76:79], %0" : "+v"(acc[1][1]) : "v"(wf[1]));
  SCHED_FENCE();
  glds16(rw, vW4, gw[4] + scA + 147456, dW + 69632);
  SCHED_FENCE();
  asm volatile("v_mfma_f32_32x32x16_bf16 %0, %1, a[72:75], %0" : "+v"(acc[2][0]) : "v"(wf[2]));
  SCHED_FENCE();
  wf[1] = tr_frag(lb4[1][0] + 40960, lb4[1][0] + 40960 + 512);
  SCHED_FENCE();
  asm volatile("v_mfma_f32_32x32x16_bf16 %0, %1, a[76:79], %0" : "+v"(acc[2][1]) : "v"(wf[2]));
  SCHED_FENCE();
  glds16(rw, vW4, gw[4] + scA + 294912, dW + 73728);
  SCHED_FENCE();
  asm volatile("v_mfma_f32_32x32x16_bf16 %0, %1, a[72:75], %0" : "+v"(acc[3][0]) : "v"(wf[3]));
  SCHED_FENCE();
  wf[2] = tr_frag(lb4[2][0] + 40960, lb4[2][0] + 40960 + 512);
  SCHED_FENCE();
  asm volatile("v_mfma_f32_32x32x16_bf16 %0, %1, a[76:79], %0" : "+v"(acc[3][1]) : "v"(wf[3]));
  SCHED_FENCE();
  glds16(rw, vW4, gw[4] + scA + 442368, dW + 77824);
  SCHED_FENCE();
  asm volatile("v_mfma_f32_32x32x16_bf16 %0, %1, a[80:83], %0" : "+v"(acc[0][0]) : "v"(wf[0]));
  SCHED_FENCE();
  wf[3] = tr_frag(lb4[3][0] + 40960, lb4[3][0] + 40960 + 512);
  SCHED_FENCE();
  asm volatile("v_mfma_f32_32x32x16_bf16 %0, %1, a[84:87], %0" : "+v"(acc[0][1]) : "v"(wf[0]));
  SCHED_FENCE();
  asm volatile("buffer_load_dwordx4 a[224:227], %0, %1, %2 offen sc1" :: "v"(vlin), "s"(rt), "s"(gt[7] + 0) : "memory");
  SCHED_FENCE();
  asm volatile("v_mfma_f32_32x32x16_bf16 %0, %1, a[80:83], %0" : "+v"(acc[1][0]) : "v"(wf[1]));
  SCHED_FENCE();
  wf[0] = tr_frag(lb4[0][0] + 45056, lb4[0][0] + 45056 + 512);
  SCHED_FENCE();
  asm volatile("v_mfma_f32_32x32x16_bf16 %0, %1, a[84:87], %0" : "+v"(acc[1][1]) : "v"(wf[1]));
  SCHED_FENCE();
  asm volatile("buffer_load_dwordx4 a[228:231], %0, %1, %2 offen sc1" :: "v"(vlin), "s"(rt), "s"(gt[7] + 36864) : "memory");
  SCHED_FENCE();
  asm volatile("v_mfma_f32_32x32x16_bf16 %0, %1, a[80:83], %0" : "+v"(acc[2][0]) : "v"(wf[2]));
  SCHED_FENCE();
  wf[1] = tr_frag(lb4[1][0] + 45056, lb4[1][0] + 45056 + 512);
  SCHED_FENCE();
  asm volatile("v_mfma_f32_32x32x16_bf16 %0, %1, a[84:87], %0" : "+v"(acc[2][1]) : "v"(wf[2]));
  SCHED_FENCE();
  asm volatile("buffer_load_dwordx4 a[232:235], %0, %1, %2 offen sc1" :: "v"(vlin), "s"(rt), "s"(gt[7] + 1024) : "memory");
  SCHED_FENCE();
  asm volatile("v_mfma_f32_32x32x16_bf16 %0, %1, a[80:83], %0" : "+v"(acc[3][0]) : "v"(wf[3]));
  SCHED_FENCE();
  wf[2] = tr_frag(lb4[2][0] + 45056, lb4[2][0] + 45056 + 512);
  SCHED_FENCE();
  asm volatile("v_mfma_f32_32x32x16_bf16 %0, %1, a[84:87], %0" : "+v"(acc[3][1]) : "v"(wf[3]));
  SCHED_FENCE();
  asm volatile("buffer_load_dwordx4 a[236:239], %0, %1, %2 offen sc1" :: "v"(vlin), "s"(rt), "s"(gt[7] + 37888) : "memory");
  SCHED_FENCE();
  asm volatile("v_mfma_f32_32x32x16_bf16 %0, %1, a[88:91], %0" : "+v"(acc[0][0]) : "v"(wf[0]));
  SCHED_FENCE();
  wf[3] = tr_frag(lb4[3][0] + 45056, lb4[3][0] + 45056 + 512);
  SCHED_FENCE();
  asm volatile("v_mfma_f32_32x32x16_bf16 %0, %1, a[92:95], %0" : "+v"(acc[0][1]) : "v"(wf[0]));
  SCHED_FENCE();
  asm volatile("buffer_load_dwordx4 a[240:243], %0, %1, %2 offen sc1" :: "v"(vlin), "s"(rt), "s"(gt[7] + 2048) : "memory");
  SCHED_FENCE();
  asm volatile("v_mfma_f32_32x32x16_bf16 %0, %1, a[88:91], %0" : "+v"(acc[1][0]) : "v"(wf[1]));
  SCHED_FENCE();
  wait_vm_barrier<37>();
  wf[0] = tr_frag(lb4[0][0] + 49152, lb4[0][0] + 49152 + 512);
  SCHED_FENCE();
  asm volatile("v_mfma_f32_32x32x16_bf16 %0, %1, a[92:95], %0" : "+v"(acc[1][1]) : "v"(wf[1]));
  SCHED_FENCE();
  asm volatile("buffer_load_dwordx4 a[244:247], %0, %1, %2 offen sc1" :: "v"(vlin), "s"(rt), "s"(gt[7] + 38912) : "memory");
  SCHED_FENCE();
  asm volatile("v_mfma_f32_32x32x16_bf16 %0, %1, a[88:91], %0" : "+v"(acc[2][0]) : "v"(wf[2]));
  SCHED_FENCE();
  wf[1] = tr_frag(lb4[1][0] + 49152, lb4[1][0] + 49152 + 512);
  SCHED_FENCE();
  asm volatile("v_mfma_f32_32x32x16_bf16 %0, %1, a[92:95], %0" : "+v"(acc[2][1]) : "v"(wf[2]));
  SCHED_FENCE();
  asm volatile("buffer_load_dwordx4 a[248:251], %0, %1, %2 offen sc1" :: "v"(vlin), "s"(rt), "s"(gt[7] + 3072) : "memory");
  SCHED_FENCE();
  asm volatile("v_mfma_f32_32x32x16_bf16 %0, %1, a[88:91], %0" : "+v"(acc[3][0]) : "v"(wf[3]));
  SCHED_FENCE();
  wf[2] = tr_frag(lb4[2][0] + 49152, lb4[2][0] + 49152 + 512);
  SCHED_FENCE();
  asm volatile("v_mfma_f32_32x32x16_bf16 %0, %1, a[92:95], %0" : "+v"(acc[3][1]) : "v"(wf[3]));
  SCHED_FENCE();
  asm volatile("buffer_load_dwordx4 a[252:255], %0, %1, %2 offen sc1" :: "v"(vlin), "s"(rt), "s"(gt[7] + 39936) : "memory");
  SCHED_FENCE();
  asm volatile("v_mfma_f32_32x32x16_bf16 %0, %1, a[96:99], %0" : "+v"(acc[0][0]) : "v"(wf[0]));
  SCHED_FENCE();
  wf[3] = tr_frag(lb4[3][0] + 49152, lb4[3][0] + 49152 + 512);
  SCHED_FENCE();
  asm volatile("v_mfma_f32_32x32x16_bf16 %0, %1, a[100:103], %0" : "+v"(acc[0][1]) : "v"(wf[0]));
  SCHED_FENCE();
  glds16(rw, vW4, gw[5] + scA + 0, dW + 81920);
  SCHED_FENCE();
  asm volatile("v_mfma_f32_32x32x16_bf16 %0, %1, a[96:99], %0" : "+v"(acc[1][0]) : "v"(wf[1]));
  SCHED_FENCE();
  wf[0] = tr_frag(lb4[0][0] + 53248, lb4[0][0] + 53248 + 512);
  SCHED_FENCE();
  asm volatile("v_mfma_f32_32x32x16_bf16 %0, %1, a[100:103], %0" : "+v"(acc[1][1]) : "v"(wf[1]));
  SCHED_FENCE();
  glds16(rw, vW4, gw[5] + scA + 147456, dW + 86016);
  SCHED_FENCE();
  asm volatile("v_mfma_f32_32x32x16_bf16 %0, %1, a[96:99], %0" : "+v"(acc[2][0]) : "v"(wf[2]));
  SCHED_FENCE();
  wf[1] = tr_frag(lb4[1][0] + 53248, lb4[1][0] + 53248 + 512);
  SCHED_FENCE();
  asm volatile("v_mfma_f32_32x32x16_bf16 %0, %1, a[100:103], %0" : "+v"(acc[2][1]) : "v"(wf[2]));
  SCHED_FENCE();
  glds16(rw, vW4, gw[5] + scA + 294912, dW + 90112);
  SCHED_FENCE();
  asm volatile("v_mfma_f32_32x32x16_bf16 %0, %1, a[96:99], %0" : "+v"(acc[3][0]) : "v"(wf[3]));
  SCHED_FENCE();
  wf[2] = tr_frag(lb4[2][0] + 53248, lb4[2][0] + 53248 + 512);
  SCHED_FENCE();
  asm volatile("v_mfma_f32_32x32x16_bf16 %0, %1, a[100:103], %0" : "+v"(acc[3][1]) : "v"(wf[3]));
  SCHED_FENCE();
  glds16(rw, vW4, gw[5] + scA + 442368, dW + 94208);
  SCHED_FENCE();
  asm volatile("v_mfma_f32_32x32x16_bf16 %0, %1, a[104:107], %0" : "+v"(acc[0][0]) : "v"(wf[0]));
  SCHED_FENCE();
  wf[3] = tr_frag(lb4[3][0] + 53248, lb4[3][0] + 53248 + 512);
  SCHED_FENCE();
  asm volatile("v_mfma_f32_32x32x16_bf16 %0, %1, a[108:111], %0" : "+v"(acc[0][1]) : "v"(wf[0]));
  SCHED_FENCE();
  asm volatile("buffer_load_dwordx4 %0, %1, %2, %3 offen sc1" : "=v"(tv[0][0]) : "v"(vlin), "s"(rt), "s"(gt[8] + 0) : "memory");
  SCHED_FENCE();
  asm volatile("v_mfma_f32_32x32x16_bf16 %0, %1, a[104:107], %0" : "+v"(acc[1][0]) : "v"(wf[1]));
  SCHED_FENCE();
  wf[0] = tr_frag(lb4[0][1] + 0, lb4[0][1] + 0 + 512);
  SCHED_FENCE();
  asm volatile("v_mfma_f32_32x32x16_bf16 %0, %1, a[108:111], %0" : "+v"(acc[1][1]) : "v"(wf[1]));
  SCHED_FENCE();
  asm volatile("buffer_load_dwordx4 %0, %1, %2, %3 offen sc1" : "=v"(tv[1][0]) : "v"(vlin), "s"(rt), "s"(gt[8] + 36864) : "memory");
  SCHED_FENCE();
  asm volatile("v_mfma_f32_32x32x16_bf16 %0, %1, a[104:107], %0" : "+v"(acc[2][0]) : "v"(wf[2]));
  SCHED_FENCE();
  wf[1] = tr_frag(lb4[1][1] + 0, lb4[1][1] + 0 + 512);
  SCHED_FENCE();
  asm volatile("v_mfma_f32_32x32x16_bf16 %0, %1, a[108:111], %0" : "+v"(acc[2][1]) : "v"(wf[2]));
  SCHED_FENCE();
  asm volatile("buffer_load_dwordx4 %0, %1, %2, %3 offen sc1" : "=v"(tv[0][1]) : "v"(vlin), "s"(rt), "s"(gt[8] + 1024) : "memory");
  SCHED_FENCE();
  asm volatile("v_mfma_f32_32x32x16_bf16 %0, %1, a[104:107], %0" : "+v"(acc[3][0]) : "v"(wf[3]));
  SCHED_FENCE();
  wf[2] = tr_frag(lb4[2][1] + 0, lb4[2][1] + 0 + 512);
  SCHED_FENCE();
  asm volatile("v_mfma_f32_32x32x16_bf16 %0, %1, a[108:111], %0" : "+v"(acc[3][1]) : "v"(wf[3]));
  SCHED_FENCE();
  asm volatile("buffer_load_dwordx4 %0, %1, %2, %3 offen sc1" : "=v"(tv[1][1]) : "v"(vlin), "s"(rt), "s"(gt[8] + 37888) : "memory");
  SCHED_FENCE();
  asm volatile("v_mfma_f32_32x32x16_bf16 %0, %1, a[112:115], %0" : "+v"(acc[0][0]) : "v"(wf[0]));
  SCHED_FENCE();
  wf[3] = tr_frag(lb4[3][1] + 0, lb4[3][1] + 0 + 512);
  SCHED_FENCE();
  asm volatile("v_mfma_f32_32x32x16_bf16 %0, %1, a[116:119], %0" : "+v"(acc[0][1]) : "v"(wf[0]));
  SCHED_FENCE();
  asm volatile("buffer_load_dwordx4 %0, %1, %2, %3 offen sc1" : "=v"(tv[0][2]) : "v"(vlin), "s"(rt), "s"(gt[8] + 2048) : "memory");
  SCHED_FENCE();
  asm volatile("v_mfma_f32_32x32x16_bf16 %0, %1, a[112:115], %0" : "+v"(acc[1][0]) : "v"(wf[1]));
  SCHED_FENCE();
  wf[0] = tr_frag(lb4[0][1] + 4096, lb4[0][1] + 4096 + 512);
  SCHED_FENCE();
  asm volatile("v_mfma_f32_32x32x16_bf16 %0, %1, a[116:119], %0" : "+v"(acc[1][1]) : "v"(wf[1]));
  SCHED_FENCE();
  asm volatile("buffer_load_dwordx4 %0, %1, %2, %3 offen sc1" : "=v"(tv[1][2]) : "v"(vlin), "s"(rt), "s"(gt[8] + 38912) : "memory");
  SCHED_FENCE();
  asm volatile("v_mfma_f32_32x32x16_bf16 %0, %1, a[112:115], %0" : "+v"(acc[2][0]) : "v"(wf[2]));
  SCHED_FENCE();
  wf[1] = tr_frag(lb4[1][1] + 4096, lb4[1][1] + 4096 + 512);
  SCHED_FENCE();
  asm volatile("v_mfma_f32_32x32x16_bf16 %0, %1, a[116:119], %0" : "+v"(acc[2][1]) : "v"(wf[2]));
  SCHED_FENCE();
  asm volatile("buffer_load_dwordx4 %0, %1, %2, %3 offen sc1" : "=v"(tv[0][3]) : "v"(vlin), "s"(rt), "s"(gt[8] + 3072) : "memory");
  SCHED_FENCE();
  asm volatile("v_mfma_f32_32x32x16_bf16 %0, %1, a[112:115], %0" : "+v"(acc[3][0]) : "v"(wf[3]));
  SCHED_FENCE();
  wf[2] = tr_frag(lb4[2][1] + 4096, lb4[2][1] + 4096 + 512);
  SCHED_FENCE();
  asm volatile("v_mfma_f32_32x32x16_bf16 %0, %1, a[116:119], %0" : "+v"(acc[3][1]) : "v"(wf[3]));
  SCHED_FENCE();
  asm volatile("buffer_load_dwordx4 %0, %1, %2, %3 offen sc1" : "=v"(tv[1][3]) : "v"(vlin), "s"(rt), "s"(gt[8] + 39936) : "memory");
  SCHED_FENCE();
  asm volatile("v_mfma_f32_32x32x16_bf16 %0, %1, a[120:123], %0" : "+v"(acc[0][0]) : "v"(wf[0]));
  SCHED_FENCE();
  wf[3] = tr_frag(lb4[3][1] + 4096, lb4[3][1] + 4096 + 512);
  SCHED_FENCE();
  asm volatile("v_mfma_f32_32x32x16_bf16 %0, %1, a[124:127], %0" : "+v"(acc[0][1]) : "v"(wf[0]));
  SCHED_FENCE();
  glds16(rw, vW4, gw[6] + scA + 0, dW + 0);
  SCHED_FENCE();
  asm volatile("v_mfma_f32_32x32x16_bf16 %0, %1, a[120:123], %0" : "+v"(acc[1][0]) : "v"(wf[1]));
  SCHED_FENCE();
  wait_vm_barrier<21>();
  wf[0] = tr_frag(lb4[0][1] + 8192, lb4[0][1] + 8192 + 512);
  SCHED_FENCE();
  asm volatile("v_mfma_f32_32x32x16_bf16 %0, %1, a[124:127], %0" : "+v"(acc[1][1]) : "v"(wf[1]));
  SCHED_FENCE();
  glds16(rw, vW4, gw[6] + scA + 147456, dW + 4096);
  SCHED_FENCE();
  asm volatile("v_mfma_f32_32x32x16_bf16 %0, %1, a[120:123], %0" : "+v"(acc[2][0]) : "v"(wf[2]));
  SCHED_FENCE();
  wf[1] = tr_frag(lb4[1][1] + 8192, lb4[1][1] + 8192 + 512);
  SCHED_FENCE();
  asm volatile("v_mfma_f32_32x32x16_bf16 %0, %1, a[124:127], %0" : "+v"(acc[2][1]) : "v"(wf[2]));
  SCHED_FENCE();
  glds16(rw, vW4, gw[6] + scA + 294912, dW + 8192);
  SCHED_FENCE();
  asm volatile("v_mfma_f32_32x32x16_bf16 %0, %1, a[120:123], %0" : "+v"(acc[3][0]) : "v"(wf[3]));
  SCHED_FENCE();
  wf[2] = tr_frag(lb4[2][1] + 8192, lb4[2][1] + 8192 + 512);
  SCHED_FENCE();
  asm volatile("v_mfma_f32_32x32x16_bf16 %0, %1, a[124:127], %0" : "+v"(acc[3][1]) : "v"(wf[3]));
  SCHED_FENCE();
  glds16(rw, vW4, gw[6] + scA + 442368, dW + 12288);
  SCHED_FENCE();
  asm volatile("v_mfma_f32_32x32x16_bf16 %0, %1, a[128:131], %0" : "+v"(acc[0][0]) : "v"(wf[0]));
  SCHED_FENCE();
  wf[3] = tr_frag(lb4[3][1] + 8192, lb4[3][1] + 8192 + 512);
  SCHED_FENCE();
  asm volatile("v_mfma_f32_32x32x16_bf16 %0, %1, a[132:135], %0" : "+v"(acc[0][1]) : "v"(wf[0]));
  SCHED_FENCE();
  glds16(rw, vW4, gw[7] + scA + 0, dW + 16384);
  SCHED_FENCE();
  asm volatile("v_mfma_f32_32x32x16_bf16 %0, %1, a[128:131], %0" : "+v"(acc[1][0]) : "v"(wf[1]));
  SCHED_FENCE();
  wf[0] = tr_frag(lb4[0][1] + 12288, lb4[0][1] + 12288 + 512);
  SCHED_FENCE();
  asm volatile("v_mfma_f32_32x32x16_bf16 %0, %1, a[132:135], %0" : "+v"(acc[1][1]) : "v"(wf[1]));
  SCHED_FENCE();
  glds16(rw, vW4, gw[7] + scA + 147456, dW + 20480);
  SCHED_FENCE();
  asm volatile("v_mfma_f32_32x32x16_bf16 %0, %1, a[128:131], %0" : "+v"(acc[2][0]) : "v"(wf[2]));
  SCHED_FENCE();
  wf[1] = tr_frag(lb4[1][1] + 12288, lb4[1][1] + 12288 + 512);
  SCHED_FENCE();
  asm volatile("v_mfma_f32_32x32x16_bf16 %0, %1, a[132:135], %0" : "+v"(acc[2][1]) : "v"(wf[2]));
  SCHED_FENCE();
  glds16(rw, vW4, gw[7] + scA + 294912, dW + 24576);
  SCHED_FENCE();
  asm volatile("v_mfma_f32_32x32x16_bf16 %0, %1, a[128:131], %0" : "+v"(acc[3][0]) : "v"(wf[3]));
  SCHED_FENCE();
  wf[2] = tr_frag(lb4[2][1] + 12288, lb4[2][1] + 12288 + 512);
  SCHED_FENCE();
  asm volatile("v_mfma_f32_32x32x16_bf16 %0, %1, a[132:135], %0" : "+v"(acc[3][1]) : "v"(wf[3]));
  SCHED_FENCE();
  glds16(rw, vW4, gw[7] + scA + 442368, dW + 28672);
  SCHED_FENCE();
  asm volatile("v_mfma_f32_32x32x16_bf16 %0, %1, a[136:139], %0" : "+v"(acc[0][0]) : "v"(wf[0]));
  SCHED_FENCE();
  wf[3] = tr_frag(lb4[3][1] + 12288, lb4[3][1] + 12288 + 512);
  SCHED_FENCE();
  asm volatile("v_mfma_f32_32x32x16_bf16 %0, %1, a[140:143], %0" : "+v"(acc[0][1]) : "v"(wf[0]));
  SCHED_FENCE();
  glds16(rw, vW4, gw[8] + scA + 0, dW + 32768);
  SCHED_FENCE();
  asm volatile("v_mfma_f32_32x32x16_bf16 %0, %1, a[136:139], %0" : "+v"(acc[1][0]) : "v"(wf[1]));
  SCHED_FENCE();
  wf[0] = tr_frag(lb4[0][1] + 16384, lb4[0][1] + 16384 + 512);
  SCHED_FENCE();
  asm volatile("v_mfma_f32_32x32x16_bf16 %0, %1, a[140:143], %0" : "+v"(acc[1][1]) : "v"(wf[1]));
  SCHED_FENCE();
  glds16(rw, vW4, gw[8] + scA + 147456, dW + 36864);
  SCHED_FENCE();
  asm volatile("v_mfma_f32_32x32x16_bf16 %0, %1, a[136:139], %0" : "+v"(acc[2][0]) : "v"(wf[2]));
  SCHED_FENCE();
  wf[1] = tr_frag(lb4[1][1] + 16384, lb4[1][1] + 16384 + 512);
  SCHED_FENCE();
  asm volatile("v_mfma_f32_32x32x16_bf16 %0, %1, a[140:143], %0" : "+v"(acc[2][1]) : "v"(wf[2]));
  SCHED_FENCE();
  glds16(rw, vW4, gw[8] + scA + 294912, dW + 40960);
  SCHED_FENCE();
  asm volatile("v_mfma_f32_32x32x16_bf16 %0, %1, a[136:139], %0" : "+v"(acc[3][0]) : "v"(wf[3]));
  SCHED_FENCE();
  wf[2] = tr_frag(lb4[2][1] + 16384, lb4[2][1] + 16384 + 512);
  SCHED_FENCE();
  asm volatile("v_mfma_f32_32x32x16_bf16 %0, %1, a[140:143], %0" : "+v"(acc[3][1]) : "v"(wf[3]));
  SCHED_FENCE();
  glds16(rw, vW4, gw[8] + scA + 442368, dW + 45056);
  SCHED_FENCE();
  asm volatile("v_mfma_f32_32x32x16_bf16 %0, %1, a[144:147], %0" : "+v"(acc[0][0]) : "v"(wf[0]));
  SCHED_FENCE();
  wf[3] = tr_frag(lb4[3][1] + 16384, lb4[3][1] + 16384 + 512);
  SCHED_FENCE();
  asm volatile("v_mfma_f32_32x32x16_bf16 %0, %1, a[148:151], %0" : "+v"(acc[0][1]) : "v"(wf[0]));
  SCHED_FENCE();
  glds16(rw, vW1, gw[0] + scC + 0, dW + 98304);
  SCHED_FENCE();
  asm volatile("v_mfma_f32_32x32x16_bf16 %0, %1, a[144:147], %0" : "+v"(acc[1][0]) : "v"(wf[1]));
  SCHED_FENCE();
  wf[0] = tr_frag(lb4[0][1] + 20480, lb4[0][1] + 20480 + 512);
  SCHED_FENCE();
  asm volatile("v_mfma_f32_32x32x16_bf16 %0, %1, a[148:151], %0" : "+v"(acc[1][1]) : "v"(wf[1]));
  SCHED_FENCE();
  asm volatile("v_mfma_f32_32x32x16_bf16 %0, %1, a[144:147], %0" : "+v"(acc[2][0]) : "v"(wf[2]));
  SCHED_FENCE();
  wf[1] = tr_frag(lb4[1][1] + 20480, lb4[1][1] + 20480 + 512);
  SCHED_FENCE();
  asm volatile("v_mfma_f32_32x32x16_bf16 %0, %1, a[148:151], %0" : "+v"(acc[2][1]) : "v"(wf[2]));
  SCHED_FENCE();
  asm volatile("v_mfma_f32_32x32x16_bf16 %0, %1, a[144:147], %0" : "+v"(acc[3][0]) : "v"(wf[3]));
  SCHED_FENCE();
  wf[2] = tr_frag(lb4[2][1] + 20480, lb4[2][1] + 20480 + 512);
  SCHED_FENCE();
  asm volatile("v_mfma_f32_32x32x16_bf16 %0, %1, a[148:151], %0" : "+v"(acc[3][1]) : "v"(wf[3]));
  SCHED_FENCE();
  asm volatile("v_mfma_f32_32x32x16_bf16 %0, %1, a[152:155], %0" : "+v"(acc[0][0]) : "v"(wf[0]));
  SCHED_FENCE();
  wf[3] = tr_frag(lb4[3][1] + 20480, lb4[3][1] + 20480 + 512);
  SCHED_FENCE();
  asm volatile("v_mfma_f32_32x32x16_bf16 %0, %1, a[156:159], %0" : "+v"(acc[0][1]) : "v"(wf[0]));
  SCHED_FENCE();
  asm volatile("v_mfma_f32_32x32x16_bf16 %0, %1, a[152:155], %0" : "+v"(acc[1][0]) : "v"(wf[1]));
  SCHED_FENCE();
  wait_vm_barrier<21>();
  wf[0] = tr_frag(lb4[0][1] + 24576, lb4[0][1] + 24576 + 512);
  SCHED_FENCE();
  asm volatile("v_mfma_f32_32x32x16_bf16 %0, %1, a[156:159], %0" : "+v"(acc[1][1]) : "v"(wf[1]));
  SCHED_FENCE();
  glds16(rw, vW1, gw[1] + scC + 0, dW + 102400);
  SCHED_FENCE();
  asm volatile("v_mfma_f32_32x32x16_bf16 %0, %1, a[152:155], %0" : "+v"(acc[2][0]) : "v"(wf[2]));
  SCHED_FENCE();
  wf[1] = tr_frag(lb4[1][1] + 24576, lb4[1][1] + 24576 + 512);
  SCHED_FENCE();
  asm volatile("v_mfma_f32_32x32x16_bf16 %0, %1, a[156:159], %0" : "+v"(acc[2][1]) : "v"(wf[2]));
  SCHED_FENCE();
  asm volatile("v_mfma_f32_32x32x16_bf16 %0, %1, a[152:155], %0" : "+v"(acc[3][0]) : "v"(wf[3]));
  SCHED_FENCE();
  wf[2] = tr_frag(lb4[2][1] + 24576, lb4[2][1] + 24576 + 512);
  SCHED_FENCE();
  asm volatile("v_mfma_f32_32x32x16_bf16 %0, %1, a[156:159], %0" : "+v"(acc[3][1]) : "v"(wf[3]));
  SCHED_FENCE();
  asm volatile("v_mfma_f32_32x32x16_bf16 %0, %1, a[160:163], %0" : "+v"(acc[0][0]) : "v"(wf[0]));
  SCHED_FENCE();
  wf[3] = tr_frag(lb4[3][1] + 24576, lb4[3][1] + 24576 + 512);
  SCHED_FENCE();
  asm volatile("v_mfma_f32_32x32x16_bf16 %0, %1, a[164:167], %0" : "+v"(acc[0][1]) : "v"(wf[0]));
  SCHED_FENCE();
  asm volatile("v_mfma_f32_32x32x16_bf16 %0, %1, a[160:163], %0" : "+v"(acc[1][0]) : "v"(wf[1]));
  SCHED_FENCE();
  wf[0] = tr_frag(lb4[0][1] + 28672, lb4[0][1] + 28672 + 512);
  SCHED_FENCE();
  asm volatile("v_mfma_f32_32x32x16_bf16 %0, %1, a[164:167], %0" : "+v"(acc[1][1]) : "v"(wf[1]));
  SCHED_FENCE();
  asm volatile("v_mfma_f32_32x32x16_bf16 %0, %1, a[160:163], %0" : "+v"(acc[2][0]) : "v"(wf[2]));
  SCHED_FENCE();
  wf[1] = tr_frag(lb4[1][1] + 28672, lb4[1][1] + 28672 + 512);
  SCHED_FENCE();
  asm volatile("v_mfma_f32_32x32x16_bf16 %0, %1, a[164:167], %0" : "+v"(acc[2][1]) : "v"(wf[2]));
  SCHED_FENCE();
  glds16(rw, vW1, gw[2] + scC + 0, dW + 106496);
  SCHED_FENCE();
  asm volatile("v_mfma_f32_32x32x16_bf16 %0, %1, a[160:163], %0" : "+v"(acc[3][0]) : "v"(wf[3]));
  SCHED_FENCE();
  wf[2] = tr_frag(lb4[2][1] + 28672, lb4[2][1] + 28672 + 512);
  SCHED_FENCE();
  asm volatile("v_mfma_f32_32x32x16_bf16 %0, %1, a[164:167], %0" : "+v"(acc[3][1]) : "v"(wf[3]));
  SCHED_FENCE();
  asm volatile("v_mfma_f32_32x32x16_bf16 %0, %1, a[168:171], %0" : "+v"(acc[0][0]) : "v"(wf[0]));
  SCHED_FENCE();
  wf[3] = tr_frag(lb4[3][1] + 28672, lb4[3][1] + 28672 + 512);
  SCHED_FENCE();
  asm volatile("v_mfma_f32_32x32x16_bf16 %0, %1, a[172:175], %0" : "+v"(acc[0][1]) : "v"(wf[0]));
  SCHED_FENCE();
  asm volatile("v_mfma_f32_32x32x16_bf16 %0, %1, a[168:171], %0" : "+v"(acc[1][0]) : "v"(wf[1]));
  SCHED_FENCE();
  wf[0] = tr_frag(lb4[0][1] + 32768, lb4[0][1] + 32768 + 512);
  SCHED_FENCE();
  asm volatile("v_mfma_f32_32x32x16_bf16 %0, %1, a[172:175], %0" : "+v"(acc[1][1]) : "v"(wf[1]));
  SCHED_FENCE();
  asm volatile("v_mfma_f32_32x32x16_bf16 %0, %1, a[168:171], %0" : "+v"(acc[2][0]) : "v"(wf[2]));
  SCHED_FENCE();
  wf[1] = tr_frag(lb4[1][1] + 32768, lb4[1][1] + 32768 + 512);
  SCHED_FENCE();
  asm volatile("v_mfma_f32_32x32x16_bf16 %0, %1, a[172:175], %0" : "+v"(acc[2][1]) : "v"(wf[2]));
  SCHED_FENCE();
  asm volatile("v_mfma_f32_32x32x16_bf16 %0, %1, a[168:171], %0" : "+v"(acc[3][0]) : "v"(wf[3]));
  SCHED_FENCE();
  wf[2] = tr_frag(lb4[2][1] + 32768, lb4[2][1] + 32768 + 512);
  SCHED_FENCE();
  asm volatile("v_mfma_f32_32x32x16_bf16 %0, %1, a[172:175], %0" : "+v"(acc[3][1]) : "v"(wf[3]));
  SCHED_FENCE();
  glds16(rw, vW1, gw[3] + scC + 0, dW + 110592);
  SCHED_FENCE();
  asm volatile("v_mfma_f32_32x32x16_bf16 %0, %1, a[176:179], %0" : "+v"(acc[0][0]) : "v"(wf[0]));
  SCHED_FENCE();
  wf[3] = tr_frag(lb4[3][1] + 32768, lb4[3][1] + 32768 + 512);
  SCHED_FENCE();
  asm volatile("v_mfma_f32_32x32x16_bf16 %0, %1, a[180:183], %0" : "+v"(acc[0][1]) : "v"(wf[0]));
  SCHED_FENCE();
  asm volatile("v_mfma_f32_32x32x16_bf16 %0, %1, a[176:179], %0" : "+v"(acc[1][0]) : "v"(wf[1]));
  SCHED_FENCE();
  wf[0] = tr_frag(lb4[0][1] + 36864, lb4[0][1] + 36864 + 512);
  SCHED_FENCE();
  asm volatile("v_mfma_f32_32x32x16_bf16 %0, %1, a[180:183], %0" : "+v"(acc[1][1]) : "v"(wf[1]));
  SCHED_FENCE();
  asm volatile("v_mfma_f32_32x32x16_bf16 %0, %1, a[176:179], %0" : "+v"(acc[2][0]) : "v"(wf[2]));
  SCHED_FENCE();
  wf[1] = tr_frag(lb4[1][1] + 36864, lb4[1][1] + 36864 + 512);
  SCHED_FENCE();
  asm volatile("v_mfma_f32_32x32x16_bf16 %0, %1, a[180:183], %0" : "+v"(acc[2][1]) : "v"(wf[2]));
  SCHED_FENCE();
  asm volatile("v_mfma_f32_32x32x16_bf16 %0, %1, a[176:179], %0" : "+v"(acc[3][0]) : "v"(wf[3]));
  SCHED_FENCE();
  wf[2] = tr_frag(lb4[2][1] + 36864, lb4[2][1] + 36864 + 512);
  SCHED_FENCE();
  asm volatile("v_mfma_f32_32x32x16_bf16 %0, %1, a[180:183], %0" : "+v"(acc[3][1]) : "v"(wf[3]));
  SCHED_FENCE();
  asm volatile("v_mfma_f32_32x32x16_bf16 %0, %1, a[184:187], %0" : "+v"(acc[0][0]) : "v"(wf[0]));
  SCHED_FENCE();
  wf[3] = tr_frag(lb4[3][1] + 36864, lb4[3][1] + 36864 + 512);
  SCHED_FENCE();
  asm volatile("v_mfma_f32_32x32x16_bf16 %0, %1, a[188:191], %0" : "+v"(acc[0][1]) : "v"(wf[0]));
  SCHED_FENCE();
  glds16(rw, vW1, gw[4] + scC + 0, dW + 114688);
  SCHED_FENCE();
  asm volatile("v_mfma_f32_32x32x16_bf16 %0, %1, a[184:187], %0" : "+v"(acc[1][0]) : "v"(wf[1]));
  SCHED_FENCE();
  wait_vm_barrier<13>();
  wf[0] = tr_frag(lb4[0][0] + 0, lb4[0][0] + 0 + 512);
  SCHED_FENCE();
  asm volatile("v_mfma_f32_32x32x16_bf16 %0, %1, a[188:191], %0" : "+v"(acc[1][1]) : "v"(wf[1]));
  SCHED_FENCE();
  asm volatile("v_mfma_f32_32x32x16_bf16 %0, %1, a[184:187], %0" : "+v"(acc[2][0]) : "v"(wf[2]));
  SCHED_FENCE();
  wf[1] = tr_frag(lb4[1][0] + 0, lb4[1][0] + 0 + 512);
  SCHED_FENCE();
  asm volatile("v_mfma_f32_32x32x16_bf16 %0, %1, a[188:191], %0" : "+v"(acc[2][1]) : "v"(wf[2]));
  SCHED_FENCE();
  asm volatile("v_mfma_f32_32x32x16_bf16 %0, %1, a[184:187], %0" : "+v"(acc[3][0]) : "v"(wf[3]));
  SCHED_FENCE();
  wf[2] = tr_frag(lb4[2][0] + 0, lb4[2][0] + 0 + 512);
  SCHED_FENCE();
  asm volatile("v_mfma_f32_32x32x16_bf16 %0, %1, a[188:191], %0" : "+v"(acc[3][1]) : "v"(wf[3]));
  SCHED_FENCE();
  asm volatile("v_mfma_f32_32x32x16_bf16 %0, %1, a[192:195], %0" : "+v"(acc[0][0]) : "v"(wf[0]));
  SCHED_FENCE();
  wf[3] = tr_frag(lb4[3][0] + 0, lb4[3][0] + 0 + 512);
  SCHED_FENCE();
  asm volatile("v_mfma_f32_32x32x16_bf16 %0, %1, a[196:199], %0" : "+v"(acc[0][1]) : "v"(wf[0]));
  SCHED_FENCE();
  asm volatile("v_mfma_f32_32x32x16_bf16 %0, %1, a[192:195], %0" : "+v"(acc[1][0]) : "v"(wf[1]));
  SCHED_FENCE();
  wf[0] = tr_frag(lb4[0][0] + 4096, lb4[0][0] + 4096 + 512);
  SCHED_FENCE();
  asm volatile("v_mfma_f32_32x32x16_bf16 %0, %1, a[196:199], %0" : "+v"(acc[1][1]) : "v"(wf[1]));
  SCHED_FENCE();
  glds16(rw, vW1, gw[5] + scC + 0, dW + 118784);
  SCHED_FENCE();
  asm volatile("v_mfma_f32_32x32x16_bf16 %0, %1, a[192:195], %0" : "+v"(acc[2][0]) : "v"(wf[2]));
  SCHED_FENCE();
  wf[1] = tr_frag(lb4[1][0] + 4096, lb4[1][0] + 4096 + 512);
  SCHED_FENCE();
  asm volatile("v_mfma_f32_32x32x16_bf16 %0, %1, a[196:199], %0" : "+v"(acc[2][1]) : "v"(wf[2]));
  SCHED_FENCE();
  asm volatile("v_mfma_f32_32x32x16_bf16 %0, %1, a[192:195], %0" : "+v"(acc[3][0]) : "v"(wf[3]));
  SCHED_FENCE();
  wf[2] = tr_frag(lb4[2][0] + 4096, lb4[2][0] + 4096 + 512);
  SCHED_FENCE();
  asm volatile("v_mfma_f32_32x32x16_bf16 %0, %1, a[196:199], %0" : "+v"(acc[3][1]) : "v"(wf[3]));
  SCHED_FENCE();
  asm volatile("v_mfma_f32_32x32x16_bf16 %0, %1, a[200:203], %0" : "+v"(acc[0][0]) : "v"(wf[0]));
  SCHED_FENCE();
  wf[3] = tr_frag(lb4[3][0] + 4096, lb4[3][0] + 4096 + 512);
  SCHED_FENCE();
  asm volatile("v_mfma_f32_32x32x16_bf16 %0, %1, a[204:207], %0" : "+v"(acc[0][1]) : "v"(wf[0]));
  SCHED_FENCE();
  asm volatile("v_mfma_f32_32x32x16_bf16 %0, %1, a[200:203], %0" : "+v"(acc[1][0]) : "v"(wf[1]));
  SCHED_FENCE();
  wf[0] = tr_frag(lb4[0][0] + 8192, lb4[0][0] + 8192 + 512);
  SCHED_FENCE();
  asm volatile("v_mfma_f32_32x32x16_bf16 %0, %1, a[204:207], %0" : "+v"(acc[1][1]) : "v"(wf[1]));
  SCHED_FENCE();
  asm volatile("v_mfma_f32_32x32x16_bf16 %0, %1, a[200:203], %0" : "+v"(acc[2][0]) : "v"(wf[2]));
  SCHED_FENCE();
  wf[1] = tr_frag(lb4[1][0] + 8192, lb4[1][0] + 8192 + 512);
  SCHED_FENCE();
  asm volatile("v_mfma_f32_32x32x16_bf16 %0, %1, a[204:207], %0" : "+v"(acc[2][1]) : "v"(wf[2]));
  SCHED_FENCE();
  glds16(rw, vW1, gw[6] + scC + 0, dW + 122880);
  SCHED_FENCE();
  asm volatile("v_mfma_f32_32x32x16_bf16 %0, %1, a[200:203], %0" : "+v"(acc[3][0]) : "v"(wf[3]));
  SCHED_FENCE();
  wf[2] = tr_frag(lb4[2][0] + 8192, lb4[2][0] + 8192 + 512);
  SCHED_FENCE();
  asm volatile("v_mfma_f32_32x32x16_bf16 %0, %1, a[204:207], %0" : "+v"(acc[3][1]) : "v"(wf[3]));
  SCHED_FENCE();
  asm volatile("v_mfma_f32_32x32x16_bf16 %0, %1, a[208:211], %0" : "+v"(acc[0][0]) : "v"(wf[0]));
  SCHED_FENCE();
  wf[3] = tr_frag(lb4[3][0] + 8192, lb4[3][0] + 8192 + 512);
  SCHED_FENCE();
  asm volatile("v_mfma_f32_32x32x16_bf16 %0, %1, a[212:215], %0" : "+v"(acc[0][1]) : "v"(wf[0]));
  SCHED_FENCE();
  asm volatile("v_mfma_f32_32x32x16_bf16 %0, %1, a[208:211], %0" : "+v"(acc[1][0]) : "v"(wf[1]));
  SCHED_FENCE();
  wf[0] = tr_frag(lb4[0][0] + 12288, lb4[0][0] + 12288 + 512);
  SCHED_FENCE();
  asm volatile("v_mfma_f32_32x32x16_bf16 %0, %1, a[212:215], %0" : "+v"(acc[1][1]) : "v"(wf[1]));
  SCHED_FENCE();
  asm volatile("v_mfma_f32_32x32x16_bf16 %0, %1, a[208:211], %0" : "+v"(acc[2][0]) : "v"(wf[2]));
  SCHED_FENCE();
  wf[1] = tr_frag(lb4[1][0] + 12288, lb4[1][0] + 12288 + 512);
  SCHED_FENCE();
  asm volatile("v_mfma_f32_32x32x16_bf16 %0, %1, a[212:215], %0" : "+v"(acc[2][1]) : "v"(wf[2]));
  SCHED_FENCE();
  asm volatile("v_mfma_f32_32x32x16_bf16 %0, %1, a[208:211], %0" : "+v"(acc[3][0]) : "v"(wf[3]));
  SCHED_FENCE();
  wf[2] = tr_frag(lb4[2][0] + 12288, lb4[2][0] + 12288 + 512);
  SCHED_FENCE();
  asm volatile("v_mfma_f32_32x32x16_bf16 %0, %1, a[212:215], %0" : "+v"(acc[3][1]) : "v"(wf[3]));
  SCHED_FENCE();
  glds16(rw, vW1, gw[7] + scC + 0, dW + 126976);
  SCHED_FENCE();
  asm volatile("v_mfma_f32_32x32x16_bf16 %0, %1, a[216:219], %0" : "+v"(acc[0][0]) : "v"(wf[0]));
  SCHED_FENCE();
  wf[3] = tr_frag(lb4[3][0] + 12288, lb4[3][0] + 12288 + 512);
  SCHED_FENCE();
  asm volatile("v_mfma_f32_32x32x16_bf16 %0, %1, a[220:223], %0" : "+v"(acc[0][1]) : "v"(wf[0]));
  SCHED_FENCE();
  asm volatile("v_mfma_f32_32x32x16_bf16 %0, %1, a[216:219], %0" : "+v"(acc[1][0]) : "v"(wf[1]));
  SCHED_FENCE();
  wait_vm_barrier<12>();
  wf[0] = tr_frag(lb4[0][0] + 16384, lb4[0][0] + 16384 + 512);
  SCHED_FENCE();
  asm volatile("v_mfma_f32_32x32x16_bf16 %0, %1, a[220:223], %0" : "+v"(acc[1][1]) : "v"(wf[1]));
  SCHED_FENCE();
  asm volatile("v_mfma_f32_32x32x16_bf16 %0, %1, a[216:219], %0" : "+v"(acc[2][0]) : "v"(wf[2]));
  SCHED_FENCE();
  wf[1] = tr_frag(lb4[1][0] + 16384, lb4[1][0] + 16384 + 512);
  SCHED_FENCE();
  asm volatile("v_mfma_f32_32x32x16_bf16 %0, %1, a[220:223], %0" : "+v"(acc[2][1]) : "v"(wf[2]));
  SCHED_FENCE();
  asm volatile("v_mfma_f32_32x32x16_bf16 %0, %1, a[216:219], %0" : "+v"(acc[3][0]) : "v"(wf[3]));
  SCHED_FENCE();
  wf[2] = tr_frag(lb4[2][0] + 16384, lb4[2][0] + 16384 + 512);
  SCHED_FENCE();
  asm volatile("v_mfma_f32_32x32x16_bf16 %0, %1, a[220:223], %0" : "+v"(acc[3][1]) : "v"(wf[3]));
  SCHED_FENCE();
  asm volatile("v_mfma_f32_32x32x16_bf16 %0, %1, a[224:227], %0" : "+v"(acc[0][0]) : "v"(wf[0]));
  SCHED_FENCE();
  wf[3] = tr_frag(lb4[3][0] + 16384, lb4[3][0] + 16384 + 512);
  SCHED_FENCE();
  asm volatile("v_mfma_f32_32x32x16_bf16 %0, %1, a[228:231], %0" : "+v"(acc[0][1]) : "v"(wf[0]));
  SCHED_FENCE();
  glds16(rd, vP, sP[0] + 0, dX + 131072);
  SCHED_FENCE();
  asm volatile("v_mfma_f32_32x32x16_bf16 %0, %1, a[224:227], %0" : "+v"(acc[1][0]) : "v"(wf[1]));
  SCHED_FENCE();
  wf[0] = tr_frag(lb4[0][0] + 20480, lb4[0][0] + 20480 + 512);
  SCHED_FENCE();
  asm volatile("v_mfma_f32_32x32x16_bf16 %0, %1, a[228:231], %0" : "+v"(acc[1][1]) : "v"(wf[1]));
  SCHED_FENCE();
  glds16(rd, vP, sP[0] + 9216, dX + 132096);
  SCHED_FENCE();
  asm volatile("v_mfma_f32_32x32x16_bf16 %0, %1, a[224:227], %0" : "+v"(acc[2][0]) : "v"(wf[2]));
  SCHED_FENCE();
  wf[1] = tr_frag(lb4[1][0] + 20480, lb4[1][0] + 20480 + 512);
  SCHED_FENCE();
  asm volatile("v_mfma_f32_32x32x16_bf16 %0, %1, a[228:231], %0" : "+v"(acc[2][1]) : "v"(wf[2]));
  SCHED_FENCE();
  glds16(rd, vP, sP[0] + 18432, dX + 133120);
  SCHED_FENCE();
  asm volatile("v_mfma_f32_32x32x16_bf16 %0, %1, a[224:227], %0" : "+v"(acc[3][0]) : "v"(wf[3]));
  SCHED_FENCE();
  wf[2] = tr_frag(lb4[2][0] + 20480, lb4[2][0] + 20480 + 512);
  SCHED_FENCE();
  asm volatile("v_mfma_f32_32x32x16_bf16 %0, %1, a[228:231], %0" : "+v"(acc[3][1]) : "v"(wf[3]));
  SCHED_FENCE();
  glds16(rd, vP, sP[0] + 27648, dX + 134144);
  SCHED_FENCE();
  asm volatile("v_mfma_f32_32x32x16_bf16 %0, %1, a[232:235], %0" : "+v"(acc[0][0]) : "v"(wf[0]));
  SCHED_FENCE();
  wf[3] = tr_frag(lb4[3][0] + 20480, lb4[3][0] + 20480 + 512);
  SCHED_FENCE();
  asm volatile("v_mfma_f32_32x32x16_bf16 %0, %1, a[236:239], %0" : "+v"(acc[0][1]) : "v"(wf[0]));
  SCHED_FENCE();
  glds16(rd, vP, sP[1] + 0, dX + 135168);
  SCHED_FENCE();
  asm volatile("v_mfma_f32_32x32x16_bf16 %0, %1, a[232:235], %0" : "+v"(acc[1][0]) : "v"(wf[1]));
  SCHED_FENCE();
  wf[0] = tr_frag(lb4[0][0] + 24576, lb4[0][0] + 24576 + 512);
  SCHED_FENCE();
  asm volatile("v_mfma_f32_32x32x16_bf16 %0, %1, a[236:239], %0" : "+v"(acc[1][1]) : "v"(wf[1]));
  SCHED_FENCE();
  glds16(rd, vP, sP[1] + 9216, dX + 136192);
  SCHED_FENCE();
  asm volatile("v_mfma_f32_32x32x16_bf16 %0, %1, a[232:235], %0" : "+v"(acc[2][0]) : "v"(wf[2]));
  SCHED_FENCE();
  wf[1] = tr_frag(lb4[1][0] + 24576, lb4[1][0] + 24576 + 512);
  SCHED_FENCE();
  asm volatile("v_mfma_f32_32x32x16_bf16 %0, %1, a[236:239], %0" : "+v"(acc[2][1]) : "v"(wf[2]));
  SCHED_FENCE();
  glds16(rd, vP, sP[1] + 18432, dX + 137216);
  SCHED_FENCE();
  asm volatile("v_mfma_f32_32x32x16_bf16 %0, %1, a[232:235], %0" : "+v"(acc[3][0]) : "v"(wf[3]));
  SCHED_FENCE();
  wf[2] = tr_frag(lb4[2][0] + 24576, lb4[2][0] + 24576 + 512);
  SCHED_FENCE();
  asm volatile("v_mfma_f32_32x32x16_bf16 %0, %1, a[236:239], %0" : "+v"(acc[3][1]) : "v"(wf[3]));
  SCHED_FENCE();
  glds16(rd, vP, sP[1] + 27648, dX + 138240);
  SCHED_FENCE();
  asm volatile("v_mfma_f32_32x32x16_bf16 %0, %1, a[240:243], %0" : "+v"(acc[0][0]) : "v"(wf[0]));
  SCHED_FENCE();
  wf[3] = tr_frag(lb4[3][0] + 24576, lb4[3][0] + 24576 + 512);
  SCHED_FENCE();
  asm volatile("v_mfma_f32_32x32x16_bf16 %0, %1, a[244:247], %0" : "+v"(acc[0][1]) : "v"(wf[0]));
  SCHED_FENCE();
  glds16(rw, vW1, gw[8] + scC + 0, dW + 49152);
  SCHED_FENCE();
  asm volatile("v_mfma_f32_32x32x16_bf16 %0, %1, a[240:243], %0" : "+v"(acc[1][0]) : "v"(wf[1]));
  SCHED_FENCE();
  wf[0] = tr_frag(lb4[0][0] + 28672, lb4[0][0] + 28672 + 512);
  SCHED_FENCE();
  asm volatile("v_mfma_f32_32x32x16_bf16 %0, %1, a[244:247], %0" : "+v"(acc[1][1]) : "v"(wf[1]));
  SCHED_FENCE();
  asm volatile("v_mfma_f32_32x32x16_bf16 %0, %1, a[240:243], %0" : "+v"(acc[2][0]) : "v"(wf[2]));
  SCHED_FENCE();
  wf[1] = tr_frag(lb4[1][0] + 28672, lb4[1][0] + 28672 + 512);
  SCHED_FENCE();
  asm volatile("v_mfma_f32_32x32x16_bf16 %0, %1, a[244:247], %0" : "+v"(acc[2][1]) : "v"(wf[2]));
  SCHED_FENCE();
  asm volatile("v_mfma_f32_32x32x16_bf16 %0, %1, a[240:243], %0" : "+v"(acc[3][0]) : "v"(wf[3]));
  SCHED_FENCE();
  wf[2] = tr_frag(lb4[2][0] + 28672, lb4[2][0] + 28672 + 512);
  SCHED_FENCE();
  asm volatile("v_mfma_f32_32x32x16_bf16 %0, %1, a[244:247], %0" : "+v"(acc[3][1]) : "v"(wf[3]));
  SCHED_FENCE();
  asm volatile("v_mfma_f32_32x32x16_bf16 %0, %1, a[248:251], %0" : "+v"(acc[0][0]) : "v"(wf[0]));
  SCHED_FENCE();
  wf[3] = tr_frag(lb4[3][0] + 28672, lb4[3][0] + 28672 + 512);
  SCHED_FENCE();
  asm volatile("v_mfma_f32_32x32x16_bf16 %0, %1, a[252:255], %0" : "+v"(acc[0][1]) : "v"(wf[0]));
  SCHED_FENCE();
  asm volatile("v_mfma_f32_32x32x16_bf16 %0, %1, a[248:251], %0" : "+v"(acc[1][0]) : "v"(wf[1]));
  SCHED_FENCE();
  wait_vm_barrier<17>();
  asm volatile("" : "+v"(sqv[0][0]), "+v"(sqv[0][1]), "+v"(sqv[1][0]), "+v"(sqv[1][1]));
  asm volatile("" : "+v"(tv[0][0]), "+v"(tv[0][1]), "+v"(tv[0][2]), "+v"(tv[0][3]), "+v"(tv[1][0]), "+v"(tv[1][1]), "+v"(tv[1][2]), "+v"(tv[1][3]));
  wf[0] = tr_frag(lb4[0][0] + 32768, lb4[0][0] + 32768 + 512);
  SCHED_FENCE();
  asm volatile("v_mfma_f32_32x32x16_bf16 %0, %1, a[252:255], %0" : "+v"(acc[1][1]) : "v"(wf[1]));
  SCHED_FENCE();
  glds16(rw, vW2, gw[0] + scB + 0, dW + 53248);
  SCHED_FENCE();
  asm volatile("v_mfma_f32_32x32x16_bf16 %0, %1, a[248:251], %0" : "+v"(acc[2][0]) : "v"(wf[2]));
  SCHED_FENCE();
  wf[1] = tr_frag(lb4[1][0] + 32768, lb4[1][0] + 32768 + 512);
  SCHED_FENCE();
  asm volatile("v_mfma_f32_32x32x16_bf16 %0, %1, a[252:255], %0" : "+v"(acc[2][1]) : "v"(wf[2]));
  SCHED_FENCE();
  asm volatile("v_mfma_f32_32x32x16_bf16 %0, %1, a[248:251], %0" : "+v"(acc[3][0]) : "v"(wf[3]));
  SCHED_FENCE();
  wf[2] = tr_frag(lb4[2][0] + 32768, lb4[2][0] + 32768 + 512);
  SCHED_FENCE();
  asm volatile("v_mfma_f32_32x32x16_bf16 %0, %1, a[252:255], %0" : "+v"(acc[3][1]) : "v"(wf[3]));
  SCHED_FENCE();
  asm volatile("v_mfma_f32_32x32x16_bf16 %0, %1, %2, %0" : "+v"(acc[0][0]) : "v"(wf[0]), "v"(tv[0][0]));
  SCHED_FENCE();
  wf[3] = tr_frag(lb4[3][0] + 32768, lb4[3][0] + 32768 + 512);
  SCHED_FENCE();
  asm volatile("v_mfma_f32_32x32x16_bf16 %0, %1, %2, %0" : "+v"(acc[0][1]) : "v"(wf[0]), "v"(tv[1][0]));
  SCHED_FENCE();
  glds16(rw, vW2, gw[0] + scB + 294912, dW + 57344);
  SCHED_FENCE();
  asm volatile("v_mfma_f32_32x32x16_bf16 %0, %1, %2, %0" : "+v"(acc[1][0]) : "v"(wf[1]), "v"(tv[0][0]));
  SCHED_FENCE();
  wf[0] = tr_frag(lb4[0][0] + 36864, lb4[0][0] + 36864 + 512);
  SCHED_FENCE();
  asm volatile("v_mfma_f32_32x32x16_bf16 %0, %1, %2, %0" : "+v"(acc[1][1]) : "v"(wf[1]), "v"(tv[1][0]));
  SCHED_FENCE();
  asm volatile("v_mfma_f32_32x32x16_bf16 %0, %1, %2, %0" : "+v"(acc[2][0]) : "v"(wf[2]), "v"(tv[0][0]));
  SCHED_FENCE();
  wf[1] = tr_frag(lb4[1][0] + 36864, lb4[1][0] + 36864 + 512);
  SCHED_FENCE();
  asm volatile("v_mfma_f32_32x32x16_bf16 %0, %1, %2, %0" : "+v"(acc[2][1]) : "v"(wf[2]), "v"(tv[1][0]));
  SCHED_FENCE();
  asm volatile("v_mfma_f32_32x32x16_bf16 %0, %1, %2, %0" : "+v"(acc[3][0]) : "v"(wf[3]), "v"(tv[0][0]));
  SCHED_FENCE();
  wf[2] = tr_frag(lb4[2][0] + 36864, lb4[2][0] + 36864 + 512);
  SCHED_FENCE();
  asm volatile("v_mfma_f32_32x32x16_bf16 %0, %1, %2, %0" : "+v"(acc[3][1]) : "v"(wf[3]), "v"(tv[1][0]));
  SCHED_FENCE();
  glds16(rw, vW2, gw[1] + scB + 0, dW + 61440);
  SCHED_FENCE();
  asm volatile("v_mfma_f32_32x32x16_bf16 %0, %1, %2, %0" : "+v"(acc[0][0]) : "v"(wf[0]), "v"(tv[0][1]));
  SCHED_FENCE();
  wf[3] = tr_frag(lb4[3][0] + 36864, lb4[3][0] + 36864 + 512);
  G1_ROWCONST(0);
  SCHED_FENCE();
  asm volatile("v_mfma_f32_32x32x16_bf16 %0, %1, %2, %0" : "+v"(acc[0][1]) : "v"(wf[0]), "v"(tv[1][1]));
  SCHED_FENCE();
  asm volatile("v_mfma_f32_32x32x16_bf16 %0, %1, %2, %0" : "+v"(acc[1][0]) : "v"(wf[1]), "v"(tv[0][1]));
  SCHED_FENCE();
  wf[0] = tr_frag(lb4[0][0] + 40960, lb4[0][0] + 40960 + 512);
  SCHED_FENCE();
  asm volatile("v_mfma_f32_32x32x16_bf16 %0, %1, %2, %0" : "+v"(acc[1][1]) : "v"(wf[1]), "v"(tv[1][1]));
  SCHED_FENCE();
  asm volatile("v_mfma_f32_32x32x16_bf16 %0, %1, %2, %0" : "+v"(acc[2][0]) : "v"(wf[2]), "v"(tv[0][1]));
  SCHED_FENCE();
  wf[1] = tr_frag(lb4[1][0] + 40960, lb4[1][0] + 40960 + 512);
  SCHED_FENCE();
  asm volatile("v_mfma_f32_32x32x16_bf16 %0, %1, %2, %0" : "+v"(acc[2][1]) : "v"(wf[2]), "v"(tv[1][1]));
  SCHED_FENCE();
  glds16(rw, vW2, gw[1] + scB + 294912, dW + 65536);
  SCHED_FENCE();
  asm volatile("v_mfma_f32_32x32x16_bf16 %0, %1, %2, %0" : "+v"(acc[3][0]) : "v"(wf[3]), "v"(tv[0][1]));
  SCHED_FENCE();
  wf[2] = tr_frag(lb4[2][0] + 40960, lb4[2][0] + 40960 + 512);
  G1_BASES1();
  SCHED_FENCE();
  asm volatile("v_mfma_f32_32x32x16_bf16 %0, %1, %2, %0" : "+v"(acc[3][1]) : "v"(wf[3]), "v"(tv[1][1]));
  SCHED_FENCE();
  asm volatile("v_mfma_f32_32x32x16_bf16 %0, %1, %2, %0" : "+v"(acc[0][0]) : "v"(wf[0]), "v"(tv[0][2]));
  SCHED_FENCE();
  wf[3] = tr_frag(lb4[3][0] + 40960, lb4[3][0] + 40960 + 512);
  G1_ROWCONST(1);
  SCHED_FENCE();
  asm volatile("v_mfma_f32_32x32x16_bf16 %0, %1, %2, %0" : "+v"(acc[0][1]) : "v"(wf[0]), "v"(tv[1][2]));
  SCHED_FENCE();
  asm volatile("v_mfma_f32_32x32x16_bf16 %0, %1, %2, %0" : "+v"(acc[1][0]) : "v"(wf[1]), "v"(tv[0][2]));
  SCHED_FENCE();
  wf[0] = tr_frag(lb4[0][0] + 45056, lb4[0][0] + 45056 + 512);
  SCHED_FENCE();
  asm volatile("v_mfma_f32_32x32x16_bf16 %0, %1, %2, %0" : "+v"(acc[1][1]) : "v"(wf[1]), "v"(tv[1][2]));
  SCHED_FENCE();
  glds16(rw, vW2, gw[2] + scB + 0, dW + 69632);
  SCHED_FENCE();
  asm volatile("v_mfma_f32_32x32x16_bf16 %0, %1, %2, %0" : "+v"(acc[2][0]) : "v"(wf[2]), "v"(tv[0][2]));
  SCHED_FENCE();
  wf[1] = tr_frag(lb4[1][0] + 45056, lb4[1][0] + 45056 + 512);
  SCHED_FENCE();
  asm volatile("v_mfma_f32_32x32x16_bf16 %0, %1, %2, %0" : "+v"(acc[2][1]) : "v"(wf[2]), "v"(tv[1][2]));
  SCHED_FENCE();
  asm volatile("v_mfma_f32_32x32x16_bf16 %0, %1, %2, %0" : "+v"(acc[3][0]) : "v"(wf[3]), "v"(tv[0][2]));
  SCHED_FENCE();
  wf[2] = tr_frag(lb4[2][0] + 45056, lb4[2][0] + 45056 + 512);
  SCHED_FENCE();
  asm volatile("v_mfma_f32_32x32x16_bf16 %0, %1, %2, %0" : "+v"(acc[3][1]) : "v"(wf[3]), "v"(tv[1][2]));
  SCHED_FENCE();
  asm volatile("v_mfma_f32_32x32x16_bf16 %0, %1, %2, %0" : "+v"(acc[0][0]) : "v"(wf[0]), "v"(tv[0][3]));
  SCHED_FENCE();
  wf[3] = tr_frag(lb4[3][0] + 45056, lb4[3][0] + 45056 + 512);
  SCHED_FENCE();
  asm volatile("v_mfma_f32_32x32x16_bf16 %0, %1, %2, %0" : "+v"(acc[0][1]) : "v"(wf[0]), "v"(tv[1][3]));
  SCHED_FENCE();
  glds16(rw, vW2, gw[2] + scB + 294912, dW + 73728);
  SCHED_FENCE();
  asm volatile("v_mfma_f32_32x32x16_bf16 %0, %1, %2, %0" : "+v"(acc[1][0]) : "v"(wf[1]), "v"(tv[0][3]));
  SCHED_FENCE();
  wait_vm_barrier<20>();
  wf[0] = tr_frag(lb1[0][1] + 40960, lb1[0][1] + 40960 + 128);
  SCHED_FENCE();
  asm volatile("v_mfma_f32_32x32x16_bf16 %0, %1, %2, %0" : "+v"(acc[1][1]) : "v"(wf[1]), "v"(tv[1][3]));
  SCHED_FENCE();
  asm volatile("v_mfma_f32_32x32x16_bf16 %0, %1, %2, %0" : "+v"(acc[2][0]) : "v"(wf[2]), "v"(tv[0][3]));
  SCHED_FENCE();
  wf[1] = tr_frag(lb1[0][1] + 41472, lb1[0][1] + 41472 + 128);
  SCHED_FENCE();
  asm volatile("v_mfma_f32_32x32x16_bf16 %0, %1, %2, %0" : "+v"(acc[2][1]) : "v"(wf[2]), "v"(tv[1][3]));
  SCHED_FENCE();
  asm volatile("v_mfma_f32_32x32x16_bf16 %0, %1, %2, %0" : "+v"(acc[3][0]) : "v"(wf[3]), "v"(tv[0][3]));
  SCHED_FENCE();
  wf[2] = tr_frag(lb1[0][1] + 41984, lb1[0][1] + 41984 + 128);
  SCHED_FENCE();
  asm volatile("v_mfma_f32_32x32x16_bf16 %0, %1, %2, %0" : "+v"(acc[3][1]) : "v"(wf[3]), "v"(tv[1][3]));
  SCHED_FENCE();
  glds16(rw, vW2, gw[3] + scB + 0, dW + 77824);
  SCHED_FENCE();
  asm volatile("v_mfma_f32_32x32x16_bf16 %0, %1, a[0:3], 0" : "=&v"(acc[4][0]) : "v"(wf[0]));
  SCHED_FENCE();
  wf[3] = tr_frag(lb1[0][1] + 42496, lb1[0][1] + 42496 + 128);
  SCHED_FENCE();
  asm volatile("v_mfma_f32_32x32x16_bf16 %0, %1, a[4:7], 0" : "=&v"(acc[4][1]) : "v"(wf[0]));
  SCHED_FENCE();
  asm volatile("v_mfma_f32_32x32x16_bf16 %0, %1, a[8:11], %0" : "+v"(acc[4][0]) : "v"(wf[1]));
  SCHED_FENCE();
  wf[0] = tr_frag(lb1[0][1] + 45056, lb1[0][1] + 45056 + 128);
  SCHED_FENCE();
  asm volatile("v_mfma_f32_32x32x16_bf16 %0, %1, a[12:15], %0" : "+v"(acc[4][1]) : "v"(wf[1]));
  SCHED_FENCE();
  asm volatile("v_mfma_f32_32x32x16_bf16 %0, %1, a[16:19], %0" : "+v"(acc[4][0]) : "v"(wf[2]));
  SCHED_FENCE();
  wf[1] = tr_frag(lb1[0][1] + 45568, lb1[0][1] + 45568 + 128);
  SCHED_FENCE();
  asm volatile("v_mfma_f32_32x32x16_bf16 %0, %1, a[20:23], %0" : "+v"(acc[4][1]) : "v"(wf[2]));
  SCHED_FENCE();
  glds16(rw, vW2, gw[3] + scB + 294912, dW + 81920);
  SCHED_FENCE();
  asm volatile("v_mfma_f32_32x32x16_bf16 %0, %1, a[24:27], %0" : "+v"(acc[4][0]) : "v"(wf[3]));
  SCHED_FENCE();
  wf[2] = tr_frag(lb1[0][1] + 46080, lb1[0][1] + 46080 + 128);
  e0[0] = fmul_s(acc[0][0][0], acc[0][0][0]);
  e0[1] = fmul_s(acc[0][0][1], acc[0][0][1]);
  e0[2] = fmul_s(acc[0][0][2], acc[0][0][2]);
  e0[3] = fmul_s(acc[0][0][3], acc[0][0][3]);
  SCHED_FENCE();
  asm volatile("v_mfma_f32_32x32x16_bf16 %0, %1, a[28:31], %0" : "+v"(acc[4][1]) : "v"(wf[3]));
  SCHED_FENCE();
  e0[0] = ffma_s(e0[0], ca1[0], ca0[0]);
  e0[1] = ffma_s(e0[1], ca1[0], ca0[0]);
  e0[2] = ffma_s(e0[2], ca1[0], ca0[0]);
  e0[3] = ffma_s(e0[3], ca1[0], ca0[0]);
  SCHED_FENCE();
  asm volatile("v_mfma_f32_32x32x16_bf16 %0, %1, a[32:35], %0" : "+v"(acc[4][0]) : "v"(wf[0]));
  SCHED_FENCE();
  wf[3] = tr_frag(lb1[0][1] + 46592, lb1[0][1] + 46592 + 128);
  e0[0] = fmul_s(e0[0], acc[0][0][0]);
  e0[1] = fmul_s(e0[1], acc[0][0][1]);
  e0[2] = fmul_s(e0[2], acc[0][0][2]);
  e0[3] = fmul_s(e0[3], acc[0][0][3]);
  SCHED_FENCE();
  asm volatile("v_mfma_f32_32x32x16_bf16 %0, %1, a[36:39], %0" : "+v"(acc[4][1]) : "v"(wf[0]));
  SCHED_FENCE();
  e0[0] = __builtin_amdgcn_exp2f(e0[0]);
  e0[1] = __builtin_amdgcn_exp2f(e0[1]);
  e0[2] = __builtin_amdgcn_exp2f(e0[2]);
  e0[3] = __builtin_amdgcn_exp2f(e0[3]);
  SCHED_FENCE();
  asm volatile("v_mfma_f32_32x32x16_bf16 %0, %1, a[40:43], %0" : "+v"(acc[4][0]) : "v"(wf[1]));
  SCHED_FENCE();
  wf[0] = tr_frag(lb1[0][1] + 49152, lb1[0][1] + 49152 + 128);
  e1[0] = fmul_s(acc[0][0][0], acc[0][0][8]);
  e1[1] = fmul_s(acc[0][0][1], acc[0][0][9]);
  e1[2] = fmul_s(acc[0][0][2], acc[0][0][10]);
  e1[3] = fmul_s(acc[0][0][3], acc[0][0][11]);
  SCHED_FENCE();
  asm volatile("v_mfma_f32_32x32x16_bf16 %0, %1, a[44:47], %0" : "+v"(acc[4][1]) : "v"(wf[1]));
  SCHED_FENCE();
  glds16(rw, vW2, gw[4] + scB + 0, dW + 86016);
  e0[0] = ffma_s(e0[0], cv[0], cv[0]);
  e0[1] = ffma_s(e0[1], cv[0], cv[0]);
  e0[2] = ffma_s(e0[2], cv[0], cv[0]);
  e0[3] = ffma_s(e0[3], cv[0], cv[0]);
  e0[0] = __builtin_amdgcn_rcpf(e0[0]);
  SCHED_FENCE();
  asm volatile("v_mfma_f32_32x32x16_bf16 %0, %1, a[48:51], %0" : "+v"(acc[4][0]) : "v"(wf[2]));
  SCHED_FENCE();
  wf[1] = tr_frag(lb1[0][1] + 49664, lb1[0][1] + 49664 + 128);
  e0[1] = __builtin_amdgcn_rcpf(e0[1]);
  e0[2] = __builtin_amdgcn_rcpf(e0[2]);
  e0[3] = __builtin_amdgcn_rcpf(e0[3]);
  e1[0] = fmul_s(e1[0], e0[0]);
  SCHED_FENCE();
  asm volatile("v_mfma_f32_32x32x16_bf16 %0, %1, a[52:55], %0" : "+v"(acc[4][1]) : "v"(wf[2]));
  SCHED_FENCE();
  e1[1] = fmul_s(e1[1], e0[1]);
  e1[2] = fmul_s(e1[2], e0[2]);
  e1[3] = fmul_s(e1[3], e0[3]);
  pk[0] = pack_bf16(e1[0], e1[1]);
  SCHED_FENCE();
  asm volatile("v_mfma_f32_32x32x16_bf16 %0, %1, a[56:59], %0" : "+v"(acc[4][0]) : "v"(wf[3]));
  SCHED_FENCE();
  wf[2] = tr_frag(lb1[0][1] + 50176, lb1[0][1] + 50176 + 128);
  pk[1] = pack_bf16(e1[2], e1[3]);
  e0[0] = fmul_s(acc[0][0][4], acc[0][0][4]);
  e0[1] = fmul_s(acc[0][0][5], acc[0][0][5]);
  e0[2] = fmul_s(acc[0][0][6], acc[0][0][6]);
  SCHED_FENCE();
  asm volatile("v_mfma_f32_32x32x16_bf16 %0, %1, a[60:63], %0" : "+v"(acc[4][1]) : "v"(wf[3]));
  SCHED_FENCE();
  e0[3] = fmul_s(acc[0][0][7], acc[0][0][7]);
  e0[0] = ffma_s(e0[0], ca1[0], ca0[0]);
  e0[1] = ffma_s(e0[1], ca1[0], ca0[0]);
  e0[2] = ffma_s(e0[2], ca1[0], ca0[0]);
  SCHED_FENCE();
  asm volatile("v_mfma_f32_32x32x16_bf16 %0, %1, a[64:67], %0" : "+v"(acc[4][0]) : "v"(wf[0]));
  SCHED_FENCE();
  wf[3] = tr_frag(lb1[0][1] + 50688, lb1[0][1] + 50688 + 128);
  e0[3] = ffma_s(e0[3], ca1[0], ca0[0]);
  e0[0] = fmul_s(e0[0], acc[0][0][4]);
  e0[1] = fmul_s(e0[1], acc[0][0][5]);
  e0[2] = fmul_s(e0[2], acc[0][0][6]);
  e0[3] = fmul_s(e0[3], acc[0][0][7]);
  SCHED_FENCE();
  asm volatile("v_mfma_f32_32x32x16_bf16 %0, %1, a[68:71], %0" : "+v"(acc[4][1]) : "v"(wf[0]));
  SCHED_FENCE();
  glds16(rw, vW2, gw[4] + scB + 294912, dW + 90112);
  e0[0] = __builtin_amdgcn_exp2f(e0[0]);
  e0[1] = __builtin_amdgcn_exp2f(e0[1]);
  e0[2] = __builtin_amdgcn_exp2f(e0[2]);
  e0[3] = __builtin_amdgcn_exp2f(e0[3]);
  SCHED_FENCE();
  asm volatile("v_mfma_f32_32x32x16_bf16 %0, %1, a[72:75], %0" : "+v"(acc[4][0]) : "v"(wf[1]));
  SCHED_FENCE();
  wait_vm_barrier<21>();
  wf[0] = tr_frag(lb1[0][1] + 53248, lb1[0][1] + 53248 + 128);
  e1[0] = fmul_s(acc[0][0][4], acc[0][0][12]);
  e1[1] = fmul_s(acc[0][0][5], acc[0][0][13]);
  e1[2] = fmul_s(acc[0][0][6], acc[0][0][14]);
  e1[3] = fmul_s(acc[0][0][7], acc[0][0][15]);
  SCHED_FENCE();
  asm volatile("v_mfma_f32_32x32x16_bf16 %0, %1, a[76:79], %0" : "+v"(acc[4][1]) : "v"(wf[1]));
  SCHED_FENCE();
  e0[0] = ffma_s(e0[0], cv[0], cv[0]);
  e0[1] = ffma_s(e0[1], cv[0], cv[0]);
  e0[2] = ffma_s(e0[2], cv[0], cv[0]);
  e0[3] = ffma_s(e0[3], cv[0], cv[0]);
  SCHED_FENCE();
  asm volatile("v_mfma_f32_32x32x16_bf16 %0, %1, a[80:83], %0" : "+v"(acc[4][0]) : "v"(wf[2]));
  SCHED_FENCE();
  wf[1] = tr_frag(lb1[0][1] + 53760, lb1[0][1] + 53760 + 128);
  e0[0] = __builtin_amdgcn_rcpf(e0[0]);
  e0[1] = __builtin_amdgcn_rcpf(e0[1]);
  e0[2] = __builtin_amdgcn_rcpf(e0[2]);
  e0[3] = __builtin_amdgcn_rcpf(e0[3]);
  SCHED_FENCE();
  asm volatile("v_mfma_f32_32x32x16_bf16 %0, %1, a[84:87], %0" : "+v"(acc[4][1]) : "v"(wf[2]));
  SCHED_FENCE();
  e1[0] = fmul_s(e1[0], e0[0]);
  e1[1] = fmul_s(e1[1], e0[1]);
  e1[2] = fmul_s(e1[2], e0[2]);
  e1[3] = fmul_s(e1[3], e0[3]);
  SCHED_FENCE();
  asm volatile("v_mfma_f32_32x32x16_bf16 %0, %1, a[88:91], %0" : "+v"(acc[4][0]) : "v"(wf[3]));
  SCHED_FENCE();
  wf[2] = tr_frag(lb1[0][1] + 54272, lb1[0][1] + 54272 + 128);
  pk[2] = pack_bf16(e1[0], e1[1]);
  pk[3] = pack_bf16(e1[2], e1[3]);
  __builtin_amdgcn_raw_buffer_store_b128((u32x4){pk[0], pk[1], pk[2], pk[3]}, ract, vlin, sactA + 0, 16);
  e0[0] = fmul_s(acc[0][1][0], acc[0][1][0]);
  e0[1] = fmul_s(acc[0][1][1], acc[0][1][1]);
  SCHED_FENCE();
  asm volatile("v_mfma_f32_32x32x16_bf16 %0, %1, a[92:95], %0" : "+v"(acc[4][1]) : "v"(wf[3]));
  SCHED_FENCE();
  glds16(rw, vW2, gw[5] + scB + 0, dW + 94208);
  e0[2] = fmul_s(acc[0][1][2], acc[0][1][2]);
  e0[3] = fmul_s(acc[0][1][3], acc[0][1][3]);
  e0[0] = ffma_s(e0[0], ca1[1], ca0[1]);
  e0[1] = ffma_s(e0[1], ca1[1], ca0[1]);
  SCHED_FENCE();
  asm volatile("v_mfma_f32_32x32x16_bf16 %0, %1, a[96:99], %0" : "+v"(acc[4][0]) : "v"(wf[0]));
  SCHED_FENCE();
  wf[3] = tr_frag(lb1[0][1] + 54784, lb1[0][1] + 54784 + 128);
  e0[2] = ffma_s(e0[2], ca1[1], ca0[1]);
  e0[3] = ffma_s(e0[3], ca1[1], ca0[1]);
  e0[0] = fmul_s(e0[0], acc[0][1][0]);
  e0[1] = fmul_s(e0[1], acc[0][1][1]);
  SCHED_FENCE();
  asm volatile("v_mfma_f32_32x32x16_bf16 %0, %1, a[100:103], %0" : "+v"(acc[4][1]) : "v"(wf[0]));
  SCHED_FENCE();
  e0[2] = fmul_s(e0[2], acc[0][1][2]);
  e0[3] = fmul_s(e0[3], acc[0][1][3]);
  e0[0] = __builtin_amdgcn_exp2f(e0[0]);
  e0[1] = __builtin_amdgcn_exp2f(e0[1]);
  SCHED_FENCE();
  asm volatile("v_mfma_f32_32x32x16_bf16 %0, %1, a[104:107], %0" : "+v"(acc[4][0]) : "v"(wf[1]));
  SCHED_FENCE();
  wf[0] = tr_frag(lb1[0][2] + 0, lb1[0][2] + 0 + 128);
  e0[2] = __builtin_amdgcn_exp2f(e0[2]);
  e0[3] = __builtin_amdgcn_exp2f(e0[3]);
  e1[0] = fmul_s(acc[0][1][0], acc[0][1][8]);
  e1[1] = fmul_s(acc[0][1][1], acc[0][1][9]);
  SCHED_FENCE();
  asm volatile("v_mfma_f32_32x32x16_bf16 %0, %1, a[108:111], %0" : "+v"(acc[4][1]) : "v"(wf[1]));
  SCHED_FENCE();
  e1[2] = fmul_s(acc[0][1][2], acc[0][1][10]);
  e1[3] = fmul_s(acc[0][1][3], acc[0][1][11]);
  e0[0] = ffma_s(e0[0], cv[1], cv[1]);
  e0[1] = ffma_s(e0[1], cv[1], cv[1]);
  e0[2] = ffma_s(e0[2], cv[1], cv[1]);
  SCHED_FENCE();
  asm volatile("v_mfma_f32_32x32x16_bf16 %0, %1, a[112:115], %0" : "+v"(acc[4][0]) : "v"(wf[2]));
  SCHED_FENCE();
  wf[1] = tr_frag(lb1[0][2] + 512, lb1[0][2] + 512 + 128);
  e0[3] = ffma_s(e0[3], cv[1], cv[1]);
  e0[0] = __builtin_amdgcn_rcpf(e0[0]);
  e0[1] = __builtin_amdgcn_rcpf(e0[1]);
  e0[2] = __builtin_amdgcn_rcpf(e0[2]);
  SCHED_FENCE();
  asm volatile("v_mfma_f32_32x32x16_bf16 %0, %1, a[116:119], %0" : "+v"(acc[4][1]) : "v"(wf[2]));
  SCHED_FENCE();
  glds16(rw, vW2, gw[5] + scB + 294912, dW + 0);
  e0[3] = __builtin_amdgcn_rcpf(e0[3]);
  e1[0] = fmul_s(e1[0], e0[0]);
  e1[1] = fmul_s(e1[1], e0[1]);
  e1[2] = fmul_s(e1[2], e0[2]);
  SCHED_FENCE();
  asm volatile("v_mfma_f32_32x32x16_bf16 %0, %1, a[120:123], %0" : "+v"(acc[4][0]) : "v"(wf[3]));
  SCHED_FENCE();
  wf[2] = tr_frag(lb1[0][2] + 1024, lb1[0][2] + 1024 + 128);
  e1[3] = fmul_s(e1[3], e0[3]);
  pk[0] = pack_bf16(e1[0], e1[1]);
  pk[1] = pack_bf16(e1[2], e1[3]);
  e0[0] = fmul_s(acc[0][1][4], acc[0][1][4]);
  SCHED_FENCE();
  asm volatile("v_mfma_f32_32x32x16_bf16 %0, %1, a[124:127], %0" : "+v"(acc[4][1]) : "v"(wf[3]));
  SCHED_FENCE();
  e0[1] = fmul_s(acc[0][1][5], acc[0][1][5]);
  e0[2] = fmul_s(acc[0][1][6], acc[0][1][6]);
  e0[3] = fmul_s(acc[0][1][7], acc[0][1][7]);
  e0[0] = ffma_s(e0[0], ca1[1], ca0[1]);
  SCHED_FENCE();
  asm volatile("v_mfma_f32_32x32x16_bf16 %0, %1, a[128:131], %0" : "+v"(acc[4][0]) : "v"(wf[0]));
  SCHED_FENCE();
  wf[3] = tr_frag(lb1[0][2] + 1536, lb1[0][2] + 1536 + 128);
  e0[1] = ffma_s(e0[1], ca1[1], ca0[1]);
  e0[2] = ffma_s(e0[2], ca1[1], ca0[1]);
  e0[3] = ffma_s(e0[3], ca1[1], ca0[1]);
  e0[0] = fmul_s(e0[0], acc[0][1][4]);
  SCHED_FENCE();
  asm volatile("v_mfma_f32_32x32x16_bf16 %0, %1, a[132:135], %0" : "+v"(acc[4][1]) : "v"(wf[0]));
  SCHED_FENCE();
  e0[1] = fmul_s(e0[1], acc[0][1][5]);
  e0[2] = fmul_s(e0[2], acc[0][1][6]);
  e0[3] = fmul_s(e0[3], acc[0][1][7]);
  e0[0] = __builtin_amdgcn_exp2f(e0[0]);
  e0[1] = __builtin_amdgcn_exp2f(e0[1]);
  SCHED_FENCE();
  asm volatile("v_mfma_f32_32x32x16_bf16 %0, %1, a[136:139], %0" : "+v"(acc[4][0]) : "v"(wf[1]));
  SCHED_FENCE();
  wf[0] = tr_frag(lb1[0][2] + 4096, lb1[0][2] + 4096 + 128);
  e0[2] = __builtin_amdgcn_exp2f(e0[2]);
  e0[3] = __builtin_amdgcn_exp2f(e0[3]);
  e1[0] = fmul_s(acc[0][1][4], acc[0][1][12]);
  e1[1] = fmul_s(acc[0][1][5], acc[0][1][13]);
  SCHED_FENCE();
  asm volatile("v_mfma_f32_32x32x16_bf16 %0, %1, a[140:143], %0" : "+v"(acc[4][1]) : "v"(wf[1]));
  SCHED_FENCE();
  glds16(rw, vW2, gw[6] + scB + 0, dW + 4096);
  e1[2] = fmul_s(acc[0][1][6], acc[0][1][14]);
  e1[3] = fmul_s(acc[0][1][7], acc[0][1][15]);
  e0[0] = ffma_s(e0[0], cv[1], cv[1]);
  e0[1] = ffma_s(e0[1], cv[1], cv[1]);
  SCHED_FENCE();
  asm volatile("v_mfma_f32_32x32x16_bf16 %0, %1, a[144:147], %0" : "+v"(acc[4][0]) : "v"(wf[2]));
  SCHED_FENCE();
  wf[1] = tr_frag(lb1[0][2] + 4608, lb1[0][2] + 4608 + 128);
  e0[2] = ffma_s(e0[2], cv[1], cv[1]);
  e0[3] = ffma_s(e0[3], cv[1], cv[1]);
  e0[0] = __builtin_amdgcn_rcpf(e0[0]);
  e0[1] = __builtin_amdgcn_rcpf(e0[1]);
  SCHED_FENCE();
  asm volatile("v_mfma_f32_32x32x16_bf16 %0, %1, a[148:151], %0" : "+v"(acc[4][1]) : "v"(wf[2]));
  SCHED_FENCE();
  e0[2] = __builtin_amdgcn_rcpf(e0[2]);
  e0[3] = __builtin_amdgcn_rcpf(e0[3]);
  e1[0] = fmul_s(e1[0], e0[0]);
  e1[1] = fmul_s(e1[1], e0[1]);
  SCHED_FENCE();
  asm volatile("v_mfma_f32_32x32x16_bf16 %0, %1, a[152:155], %0" : "+v"(acc[4][0]) : "v"(wf[3]));
  SCHED_FENCE();
  wf[2] = tr_frag(lb1[0][2] + 5120, lb1[0][2] + 5120 + 128);
  e1[2] = fmul_s(e1[2], e0[2]);
  e1[3] = fmul_s(e1[3], e0[3]);
  pk[2] = pack_bf16(e1[0], e1[1]);
  pk[3] = pack_bf16(e1[2], e1[3]);
  __builtin_amdgcn_raw_buffer_store_b128((u32x4){pk[0], pk[1], pk[2], pk[3]}, ract, vlin, sactA + 294912, 16);
  SCHED_FENCE();
  asm volatile("v_mfma_f32_32x32x16_bf16 %0, %1, a[156:159], %0" : "+v"(acc[4][1]) : "v"(wf[3]));
  SCHED_FENCE();
  e0[0] = fmul_s(acc[1][0][0], acc[1][0][0]);
  e0[1] = fmul_s(acc[1][0][1], acc[1][0][1]);
  e0[2] = fmul_s(acc[1][0][2], acc[1][0][2]);
  e0[3] = fmul_s(acc[1][0][3], acc[1][0][3]);
  SCHED_FENCE();
  asm volatile("v_mfma_f32_32x32x16_bf16 %0, %1, a[160:163], %0" : "+v"(acc[4][0]) : "v"(wf[0]));
  SCHED_FENCE();
  wf[3] = tr_frag(lb1[0][2] + 5632, lb1[0][2] + 5632 + 128);
  e0[0] = ffma_s(e0[0], ca1[0], ca0[0]);
  e0[1] = ffma_s(e0[1], ca1[0], ca0[0]);
  e0[2] = ffma_s(e0[2], ca1[0], ca0[0]);
  e0[3] = ffma_s(e0[3], ca1[0], ca0[0]);
  SCHED_FENCE();
  asm volatile("v_mfma_f32_32x32x16_bf16 %0, %1, a[164:167], %0" : "+v"(acc[4][1]) : "v"(wf[0]));
  SCHED_FENCE();
  glds16(rw, vW2, gw[6] + scB + 294912, dW + 8192);
  e0[0] = fmul_s(e0[0], acc[1][0][0]);
  e0[1] = fmul_s(e0[1], acc[1][0][1]);
  e0[2] = fmul_s(e0[2], acc[1][0][2]);
  e0[3] = fmul_s(e0[3], acc[1][0][3]);
  SCHED_FENCE();
  asm volatile("v_mfma_f32_32x32x16_bf16 %0, %1, a[168:171], %0" : "+v"(acc[4][0]) : "v"(wf[1]));
  SCHED_FENCE();
  wait_vm_barrier<16>();
  wf[0] = tr_frag(lb1[0][2] + 8192, lb1[0][2] + 8192 + 128);
  e0[0] = __builtin_amdgcn_exp2f(e0[0]);
  e0[1] = __builtin_amdgcn_exp2f(e0[1]);
  e0[2] = __builtin_amdgcn_exp2f(e0[2]);
  e0[3] = __builtin_amdgcn_exp2f(e0[3]);
  SCHED_FENCE();
  asm volatile("v_mfma_f32_32x32x16_bf16 %0, %1, a[172:175], %0" : "+v"(acc[4][1]) : "v"(wf[1]));
  SCHED_FENCE();
  e1[0] = fmul_s(acc[1][0][0], acc[1][0][8]);
  e1[1] = fmul_s(acc[1][0][1], acc[1][0][9]);
  e1[2] = fmul_s(acc[1][0][2], acc[1][0][10]);
  e1[3] = fmul_s(acc[1][0][3], acc[1][0][11]);
  SCHED_FENCE();
  asm volatile("v_mfma_f32_32x32x16_bf16 %0, %1, a[176:179], %0" : "+v"(acc[4][0]) : "v"(wf[2]));
  SCHED_FENCE();
  wf[1] = tr_frag(lb1[0][2] + 8704, lb1[0][2] + 8704 + 128);
  e0[0] = ffma_s(e0[0], cv[0], cv[0]);
  e0[1] = ffma_s(e0[1], cv[0], cv[0]);
  e0[2] = ffma_s(e0[2], cv[0], cv[0]);
  e0[3] = ffma_s(e0[3], cv[0], cv[0]);
  e0[0] = __builtin_amdgcn_rcpf(e0[0]);
  SCHED_FENCE();
  asm volatile("v_mfma_f32_32x32x16_bf16 %0, %1, a[180:183], %0" : "+v"(acc[4][1]) : "v"(wf[2]));
  SCHED_FENCE();
  e0[1] = __builtin_amdgcn_rcpf(e0[1]);
  e0[2] = __builtin_amdgcn_rcpf(e0[2]);
  e0[3] = __builtin_amdgcn_rcpf(e0[3]);
  e1[0] = fmul_s(e1[0], e0[0]);
  SCHED_FENCE();
  asm volatile("v_mfma_f32_32x32x16_bf16 %0, %1, a[184:187], %0" : "+v"(acc[4][0]) : "v"(wf[3]));
  SCHED_FENCE();
  wf[2] = tr_frag(lb1[0][2] + 9216, lb1[0][2] + 9216 + 128);
  e1[1] = fmul_s(e1[1], e0[1]);
  e1[2] = fmul_s(e1[2], e0[2]);
  e1[3] = fmul_s(e1[3], e0[3]);
  pk[0] = pack_bf16(e1[0], e1[1]);
  SCHED_FENCE();
  asm volatile("v_mfma_f32_32x32x16_bf16 %0, %1, a[188:191], %0" : "+v"(acc[4][1]) : "v"(wf[3]));
  SCHED_FENCE();
  glds16(rw, vW2, gw[7] + scB + 0, dW + 12288);
  pk[1] = pack_bf16(e1[2], e1[3]);
  e0[0] = fmul_s(acc[1][0][4], acc[1][0][4]);
  e0[1] = fmul_s(acc[1][0][5], acc[1][0][5]);
  e0[2] = fmul_s(acc[1][0][6], acc[1][0][6]);
  SCHED_FENCE();
  asm volatile("v_mfma_f32_32x32x16_bf16 %0, %1, a[192:195], %0" : "+v"(acc[4][0]) : "v"(wf[0]));
  SCHED_FENCE();
  wf[3] = tr_frag(lb1[0][2] + 9728, lb1[0][2] + 9728 + 128);
  e0[3] = fmul_s(acc[1][0][7], acc[1][0][7]);
  e0[0] = ffma_s(e0[0], ca1[0], ca0[0]);
  e0[1] = ffma_s(e0[1], ca1[0], ca0[0]);
  e0[2] = ffma_s(e0[2], ca1[0], ca0[0]);
  SCHED_FENCE();
  asm volatile("v_mfma_f32_32x32x16_bf16 %0, %1, a[196:199], %0" : "+v"(acc[4][1]) : "v"(wf[0]));
  SCHED_FENCE();
  e0[3] = ffma_s(e0[3], ca1[0], ca0[0]);
  e0[0] = fmul_s(e0[0], acc[1][0][4]);
  e0[1] = fmul_s(e0[1], acc[1][0][5]);
  e0[2] = fmul_s(e0[2], acc[1][0][6]);
  e0[3] = fmul_s(e0[3], acc[1][0][7]);
  SCHED_FENCE();
  asm volatile("v_mfma_f32_32x32x16_bf16 %0, %1, a[200:203], %0" : "+v"(acc[4][0]) : "v"(wf[1]));
  SCHED_FENCE();
  wf[0] = tr_frag(lb1[0][2] + 12288, lb1[0][2] + 12288 + 128);
  e0[0] = __builtin_amdgcn_exp2f(e0[0]);
  e0[1] = __builtin_amdgcn_exp2f(e0[1]);
  e0[2] = __builtin_amdgcn_exp2f(e0[2]);
  e0[3] = __builtin_amdgcn_exp2f(e0[3]);
  SCHED_FENCE();
  asm volatile("v_mfma_f32_32x32x16_bf16 %0, %1, a[204:207], %0" : "+v"(acc[4][1]) : "v"(wf[1]));
  SCHED_FENCE();
  e1[0] = fmul_s(acc[1][0][4], acc[1][0][12]);
  e1[1] = fmul_s(acc[1][0][5], acc[1][0][13]);
  e1[2] = fmul_s(acc[1][0][6], acc[1][0][14]);
  e1[3] = fmul_s(acc[1][0][7], acc[1][0][15]);
  SCHED_FENCE();
  asm volatile("v_mfma_f32_32x32x16_bf16 %0, %1, a[208:211], %0" : "+v"(acc[4][0]) : "v"(wf[2]));
  SCHED_FENCE();
  wf[1] = tr_frag(lb1[0][2] + 12800, lb1[0][2] + 12800 + 128);
  e0[0] = ffma_s(e0[0], cv[0], cv[0]);
  e0[1] = ffma_s(e0[1], cv[0], cv[0]);
  e0[2] = ffma_s(e0[2], cv[0], cv[0]);
  e0[3] = ffma_s(e0[3], cv[0], cv[0]);
  SCHED_FENCE();
  asm volatile("v_mfma_f32_32x32x16_bf16 %0, %1, a[212:215], %0" : "+v"(acc[4][1]) : "v"(wf[2]));
  SCHED_FENCE();
  glds16(rw, vW2, gw[7] + scB + 294912, dW + 16384);
  e0[0] = __builtin_amdgcn_rcpf(e0[0]);
  e0[1] = __builtin_amdgcn_rcpf(e0[1]);
  e0[2] = __builtin_amdgcn_rcpf(e0[2]);
  e0[3] = __builtin_amdgcn_rcpf(e0[3]);
  SCHED_FENCE();
  asm volatile("v_mfma_f32_32x32x16_bf16 %0, %1, a[216:219], %0" : "+v"(acc[4][0]) : "v"(wf[3]));
  SCHED_FENCE();
  wf[2] = tr_frag(lb1[0][2] + 13312, lb1[0][2] + 13312 + 128);
  e1[0] = fmul_s(e1[0], e0[0]);
  e1[1] = fmul_s(e1[1], e0[1]);
  e1[2] = fmul_s(e1[2], e0[2]);
  e1[3] = fmul_s(e1[3], e0[3]);
  G1_BASES2();
  SCHED_FENCE();
  asm volatile("v_mfma_f32_32x32x16_bf16 %0, %1, a[220:223], %0" : "+v"(acc[4][1]) : "v"(wf[3]));
  SCHED_FENCE();
  pk[2] = pack_bf16(e1[0], e1[1]);
  pk[3] = pack_bf16(e1[2], e1[3]);
  __builtin_amdgcn_raw_buffer_store_b128((u32x4){pk[0], pk[1], pk[2], pk[3]}, ract, vlin, sactA + 1024, 16);
  e0[0] = fmul_s(acc[1][1][0], acc[1][1][0]);
  e0[1] = fmul_s(acc[1][1][1], acc[1][1][1]);
  SCHED_FENCE();
  asm volatile("v_mfma_f32_32x32x16_bf16 %0, %1, a[224:227], %0" : "+v"(acc[4][0]) : "v"(wf[0]));
  SCHED_FENCE();
  wf[3] = tr_frag(lb1[0][2] + 13824, lb1[0][2] + 13824 + 128);
  e0[2] = fmul_s(acc[1][1][2], acc[1][1][2]);
  e0[3] = fmul_s(acc[1][1][3], acc[1][1][3]);
  e0[0] = ffma_s(e0[0], ca1[1], ca0[1]);
  e0[1] = ffma_s(e0[1], ca1[1], ca0[1]);
  SCHED_FENCE();
  asm volatile("v_mfma_f32_32x32x16_bf16 %0, %1, a[228:231], %0" : "+v"(acc[4][1]) : "v"(wf[0]));
  SCHED_FENCE();
  e0[2] = ffma_s(e0[2], ca1[1], ca0[1]);
  e0[3] = ffma_s(e0[3], ca1[1], ca0[1]);
  e0[0] = fmul_s(e0[0], acc[1][1][0]);
  e0[1] = fmul_s(e0[1], acc[1][1][1]);
  SCHED_FENCE();
  asm volatile("v_mfma_f32_32x32x16_bf16 %0, %1, a[232:235], %0" : "+v"(acc[4][0]) : "v"(wf[1]));
  SCHED_FENCE();
  wf[0] = tr_frag(lb1[0][0] + 49152, lb1[0][0] + 49152 + 128);
  e0[2] = fmul_s(e0[2], acc[1][1][2]);
  e0[3] = fmul_s(e0[3], acc[1][1][3]);
  e0[0] = __builtin_amdgcn_exp2f(e0[0]);
  e0[1] = __builtin_amdgcn_exp2f(e0[1]);
  SCHED_FENCE();
  asm volatile("v_mfma_f32_32x32x16_bf16 %0, %1, a[236:239], %0" : "+v"(acc[4][1]) : "v"(wf[1]));
  SCHED_FENCE();
  glds16(rw, vW2, gw[8] + scB + 0, dW + 20480);
  e0[2] = __builtin_amdgcn_exp2f(e0[2]);
  e0[3] = __builtin_amdgcn_exp2f(e0[3]);
  e1[0] = fmul_s(acc[1][1][0], acc[1][1][8]);
  e1[1] = fmul_s(acc[1][1][1], acc[1][1][9]);
  SCHED_FENCE();
  asm volatile("v_mfma_f32_32x32x16_bf16 %0, %1, a[240:243], %0" : "+v"(acc[4][0]) : "v"(wf[2]));
  SCHED_FENCE();
  wf[1] = tr_frag(lb1[0][0] + 49664, lb1[0][0] + 49664 + 128);
  e1[2] = fmul_s(acc[1][1][2], acc[1][1][10]);
  e1[3] = fmul_s(acc[1][1][3], acc[1][1][11]);
  e0[0] = ffma_s(e0[0], cv[1], cv[1]);
  e0[1] = ffma_s(e0[1], cv[1], cv[1]);
  e0[2] = ffma_s(e0[2], cv[1], cv[1]);
  SCHED_FENCE();
  asm volatile("v_mfma_f32_32x32x16_bf16 %0, %1, a[244:247], %0" : "+v"(acc[4][1]) : "v"(wf[2]));
  SCHED_FENCE();
  e0[3] = ffma_s(e0[3], cv[1], cv[1]);
  e0[0] = __builtin_amdgcn_rcpf(e0[0]);
  e0[1] = __builtin_amdgcn_rcpf(e0[1]);
  e0[2] = __builtin_amdgcn_rcpf(e0[2]);
  SCHED_FENCE();
  asm volatile("v_mfma_f32_32x32x16_bf16 %0, %1, a[248:251], %0" : "+v"(acc[4][0]) : "v"(wf[3]));
  SCHED_FENCE();
  wf[2] = tr_frag(lb1[0][0] + 50176, lb1[0][0] + 50176 + 128);
  e0[3] = __builtin_amdgcn_rcpf(e0[3]);
  e1[0] = fmul_s(e1[0], e0[0]);
  e1[1] = fmul_s(e1[1], e0[1]);
  e1[2] = fmul_s(e1[2], e0[2]);
  SCHED_FENCE();
  asm volatile("v_mfma_f32_32x32x16_bf16 %0, %1, a[252:255], %0" : "+v"(acc[4][1]) : "v"(wf[3]));
  SCHED_FENCE();
  e1[3] = fmul_s(e1[3], e0[3]);
  pk[0] = pack_bf16(e1[0], e1[1]);
  pk[1] = pack_bf16(e1[2], e1[3]);
  e0[0] = fmul_s(acc[1][1][4], acc[1][1][4]);
  SCHED_FENCE();
  asm volatile("v_mfma_f32_32x32x16_bf16 %0, %1, %2, %0" : "+v"(acc[4][0]) : "v"(wf[0]), "v"(tv[0][0]));
  SCHED_FENCE();
  wf[3] = tr_frag(lb1[0][0] + 50688, lb1[0][0] + 50688 + 128);
  e0[1] = fmul_s(acc[1][1][5], acc[1][1][5]);
  e0[2] = fmul_s(acc[1][1][6], acc[1][1][6]);
  e0[3] = fmul_s(acc[1][1][7], acc[1][1][7]);
  e0[0] = ffma_s(e0[0], ca1[1], ca0[1]);
  SCHED_FENCE();
  asm volatile("v_mfma_f32_32x32x16_bf16 %0, %1, %2, %0" : "+v"(acc[4][1]) : "v"(wf[0]), "v"(tv[1][0]));
  SCHED_FENCE();
  glds16(rw, vW2, gw[8] + scB + 294912, dW + 24576);
  e0[1] = ffma_s(e0[1], ca1[1], ca0[1]);
  e0[2] = ffma_s(e0[2], ca1[1], ca0[1]);
  e0[3] = ffma_s(e0[3], ca1[1], ca0[1]);
  e0[0] = fmul_s(e0[0], acc[1][1][4]);
  SCHED_FENCE();
  asm volatile("v_mfma_f32_32x32x16_bf16 %0, %1, %2, %0" : "+v"(acc[4][0]) : "v"(wf[1]), "v"(tv[0][1]));
  SCHED_FENCE();
  wait_vm_barrier<15>();
  wf[0] = tr_frag(lb2[0][0] + 53248, lb2[0][0] + 53248 + 256);
  e0[1] = fmul_s(e0[1], acc[1][1][5]);
  e0[2] = fmul_s(e0[2], acc[1][1][6]);
  e0[3] = fmul_s(e0[3], acc[1][1][7]);
  e0[0] = __builtin_amdgcn_exp2f(e0[0]);
  e0[1] = __builtin_amdgcn_exp2f(e0[1]);
  SCHED_FENCE();
  asm volatile("v_mfma_f32_32x32x16_bf16 %0, %1, %2, %0" : "+v"(acc[4][1]) : "v"(wf[1]), "v"(tv[1][1]));
  SCHED_FENCE();
  e0[2] = __builtin_amdgcn_exp2f(e0[2]);
  e0[3] = __builtin_amdgcn_exp2f(e0[3]);
  e1[0] = fmul_s(acc[1][1][4], acc[1][1][12]);
  e1[1] = fmul_s(acc[1][1][5], acc[1][1][13]);
  SCHED_FENCE();
  asm volatile("v_mfma_f32_32x32x16_bf16 %0, %1, %2, %0" : "+v"(acc[4][0]) : "v"(wf[2]), "v"(tv[0][2]));
  SCHED_FENCE();
  wf[1] = tr_frag(lb2[1][0] + 53248, lb2[1][0] + 53248 + 256);
  e1[2] = fmul_s(acc[1][1][6], acc[1][1][14]);
  e1[3] = fmul_s(acc[1][1][7], acc[1][1][15]);
  e0[0] = ffma_s(e0[0], cv[1], cv[1]);
  e0[1] = ffma_s(e0[1], cv[1], cv[1]);
  SCHED_FENCE();
  asm volatile("v_mfma_f32_32x32x16_bf16 %0, %1, %2, %0" : "+v"(acc[4][1]) : "v"(wf[2]), "v"(tv[1][2]));
  SCHED_FENCE();
  e0[2] = ffma_s(e0[2], cv[1], cv[1]);
  e0[3] = ffma_s(e0[3], cv[1], cv[1]);
  e0[0] = __builtin_amdgcn_rcpf(e0[0]);
  e0[1] = __builtin_amdgcn_rcpf(e0[1]);
  SCHED_FENCE();
  asm volatile("v_mfma_f32_32x32x16_bf16 %0, %1, %2, %0" : "+v"(acc[4][0]) : "v"(wf[3]), "v"(tv[0][3]));
  SCHED_FENCE();
  wf[2] = tr_frag(lb2[0][0] + 54272, lb2[0][0] + 54272 + 256);
  e0[2] = __builtin_amdgcn_rcpf(e0[2]);
  e0[3] = __builtin_amdgcn_rcpf(e0[3]);
  e1[0] = fmul_s(e1[0], e0[0]);
  e1[1] = fmul_s(e1[1], e0[1]);
  SCHED_FENCE();
  asm volatile("v_mfma_f32_32x32x16_bf16 %0, %1, %2, %0" : "+v"(acc[4][1]) : "v"(wf[3]), "v"(tv[1][3]));
  SCHED_FENCE();
  glds16(rw, vW2, gw[0] + scB + 64, dW + 28672);
  e1[2] = fmul_s(e1[2], e0[2]);
  e1[3] = fmul_s(e1[3], e0[3]);
  pk[2] = pack_bf16(e1[0], e1[1]);
  pk[3] = pack_bf16(e1[2], e1[3]);
  __builtin_amdgcn_raw_buffer_store_b128((u32x4){pk[0], pk[1], pk[2], pk[3]}, ract, vlin, sactA + 295936, 16);
  SCHED_FENCE();
  asm volatile("v_mfma_f32_32x32x16_bf16 %0, %1, a[0:3], 0" : "=&v"(acc[0][0]) : "v"(wf[0]));
  SCHED_FENCE();
  wf[3] = tr_frag(lb2[1][0] + 54272, lb2[1][0] + 54272 + 256);
  e0[0] = fmul_s(acc[2][0][0], acc[2][0][0]);
  e0[1] = fmul_s(acc[2][0][1], acc[2][0][1]);
  e0[2] = fmul_s(acc[2][0][2], acc[2][0][2]);
  SCHED_FENCE();
  asm volatile("v_mfma_f32_32x32x16_bf16 %0, %1, a[4:7], 0" : "=&v"(acc[0][1]) : "v"(wf[0]));
  SCHED_FENCE();
  e0[3] = fmul_s(acc[2][0][3], acc[2][0][3]);
  e0[0] = ffma_s(e0[0], ca1[0], ca0[0]);
  e0[1] = ffma_s(e0[1], ca1[0], ca0[0]);
  SCHED_FENCE();
  asm volatile("v_mfma_f32_32x32x16_bf16 %0, %1, a[0:3], 0" : "=&v"(acc[1][0]) : "v"(wf[1]));
  SCHED_FENCE();
  wf[0] = tr_frag(lb2[0][1] + 0, lb2[0][1] + 0 + 256);
  e0[2] = ffma_s(e0[2], ca1[0], ca0[0]);
  e0[3] = ffma_s(e0[3], ca1[0], ca0[0]);
  e0[0] = fmul_s(e0[0], acc[2][0][0]);
  SCHED_FENCE();
  asm volatile("v_mfma_f32_32x32x16_bf16 %0, %1, a[4:7], 0" : "=&v"(acc[1][1]) : "v"(wf[1]));
  SCHED_FENCE();
  e0[1] = fmul_s(e0[1], acc[2][0][1]);
  e0[2] = fmul_s(e0[2], acc[2][0][2]);
  e0[3] = fmul_s(e0[3], acc[2][0][3]);
  e0[0] = __builtin_amdgcn_exp2f(e0[0]);
  SCHED_FENCE();
  asm volatile("v_mfma_f32_32x32x16_bf16 %0, %1, a[8:11], %0" : "+v"(acc[0][0]) : "v"(wf[2]));
  SCHED_FENCE();
  wf[1] = tr_frag(lb2[1][1] + 0, lb2[1][1] + 0 + 256);
  e0[1] = __builtin_amdgcn_exp2f(e0[1]);
  e0[2] = __builtin_amdgcn_exp2f(e0[2]);
  e0[3] = __builtin_amdgcn_exp2f(e0[3]);
  SCHED_FENCE();
  asm volatile("v_mfma_f32_32x32x16_bf16 %0, %1, a[12:15], %0" : "+v"(acc[0][1]) : "v"(wf[2]));
  SCHED_FENCE();
  glds16(rw, vW2, gw[0] + scB + 294976, dW + 32768);
  e1[0] = fmul_s(acc[2][0][0], acc[2][0][8]);
  e1[1] = fmul_s(acc[2][0][1], acc[2][0][9]);
  e1[2] = fmul_s(acc[2][0][2], acc[2][0][10]);
  SCHED_FENCE();
  asm volatile("v_mfma_f32_32x32x16_bf16 %0, %1, a[8:11], %0" : "+v"(acc[1][0]) : "v"(wf[3]));
  SCHED_FENCE();
  wf[2] = tr_frag(lb2[0][1] + 1024, lb2[0][1] + 1024 + 256);
  e1[3] = fmul_s(acc[2][0][3], acc[2][0][11]);
  e0[0] = ffma_s(e0[0], cv[0], cv[0]);
  e0[1] = ffma_s(e0[1], cv[0], cv[0]);
  e0[2] = ffma_s(e0[2], cv[0], cv[0]);
  SCHED_FENCE();
  asm volatile("v_mfma_f32_32x32x16_bf16 %0, %1, a[12:15], %0" : "+v"(acc[1][1]) : "v"(wf[3]));
  SCHED_FENCE();
  e0[3] = ffma_s(e0[3], cv[0], cv[0]);
  e0[0] = __builtin_amdgcn_rcpf(e0[0]);
  e0[1] = __builtin_amdgcn_rcpf(e0[1]);
  SCHED_FENCE();
  asm volatile("v_mfma_f32_32x32x16_bf16 %0, %1, a[16:19], %0" : "+v"(acc[0][0]) : "v"(wf[0]));
  SCHED_FENCE();
  wf[3] = tr_frag(lb2[1][1] + 1024, lb2[1][1] + 1024 + 256);
  e0[2] = __builtin_amdgcn_rcpf(e0[2]);
  e0[3] = __builtin_amdgcn_rcpf(e0[3]);
  e1[0] = fmul_s(e1[0], e0[0]);
  SCHED_FENCE();
  asm volatile("v_mfma_f32_32x32x16_bf16 %0, %1, a[20:23], %0" : "+v"(acc[0][1]) : "v"(wf[0]));
  SCHED_FENCE();
  e1[1] = fmul_s(e1[1], e0[1]);
  e1[2] = fmul_s(e1[2], e0[2]);
  e1[3] = fmul_s(e1[3], e0[3]);
  SCHED_FENCE();
  asm volatile("v_mfma_f32_32x32x16_bf16 %0, %1, a[16:19], %0" : "+v"(acc[1][0]) : "v"(wf[1]));
  SCHED_FENCE();
  wf[0] = tr_frag(lb2[0][1] + 4096, lb2[0][1] + 4096 + 256);
  pk[0] = pack_bf16(e1[0], e1[1]);
  pk[1] = pack_bf16(e1[2], e1[3]);
  e0[0] = fmul_s(acc[2][0][4], acc[2][0][4]);
  e0[1] = fmul_s(acc[2][0][5], acc[2][0][5]);
  SCHED_FENCE();
  asm volatile("v_mfma_f32_32x32x16_bf16 %0, %1, a[20:23], %0" : "+v"(acc[1][1]) : "v"(wf[1]));
  SCHED_FENCE();
  glds16(rw, vW2, gw[1] + scB + 64, dW + 36864);
  e0[2] = fmul_s(acc[2][0][6], acc[2][0][6]);
  e0[3] = fmul_s(acc[2][0][7], acc[2][0][7]);
  e0[0] = ffma_s(e0[0], ca1[0], ca0[0]);
  SCHED_FENCE();
  asm volatile("v_mfma_f32_32x32x16_bf16 %0, %1, a[24:27], %0" : "+v"(acc[0][0]) : "v"(wf[2]));
  SCHED_FENCE();
  wf[1] = tr_frag(lb2[1][1] + 4096, lb2[1][1] + 4096 + 256);
  e0[1] = ffma_s(e0[1], ca1[0], ca0[0]);
  e0[2] = ffma_s(e0[2], ca1[0], ca0[0]);
  e0[3] = ffma_s(e0[3], ca1[0], ca0[0]);
  SCHED_FENCE();
  asm volatile("v_mfma_f32_32x32x16_bf16 %0, %1, a[28:31], %0" : "+v"(acc[0][1]) : "v"(wf[2]));
  SCHED_FENCE();
  e0[0] = fmul_s(e0[0], acc[2][0][4]);
  e0[1] = fmul_s(e0[1], acc[2][0][5]);
  e0[2] = fmul_s(e0[2], acc[2][0][6]);
  e0[3] = fmul_s(e0[3], acc[2][0][7]);
  SCHED_FENCE();
  asm volatile("v_mfma_f32_32x32x16_bf16 %0, %1, a[24:27], %0" : "+v"(acc[1][0]) : "v"(wf[3]));
  SCHED_FENCE();
  wf[2] = tr_frag(lb2[0][1] + 5120, lb2[0][1] + 5120 + 256);
  e0[0] = __builtin_amdgcn_exp2f(e0[0]);
  e0[1] = __builtin_amdgcn_exp2f(e0[1]);
  e0[2] = __builtin_amdgcn_exp2f(e0[2]);
  SCHED_FENCE();
  asm volatile("v_mfma_f32_32x32x16_bf16 %0, %1, a[28:31], %0" : "+v"(acc[1][1]) : "v"(wf[3]));
  SCHED_FENCE();
  e0[3] = __builtin_amdgcn_exp2f(e0[3]);
  e1[0] = fmul_s(acc[2][0][4], acc[2][0][12]);
  e1[1] = fmul_s(acc[2][0][5], acc[2][0][13]);
  SCHED_FENCE();
  asm volatile("v_mfma_f32_32x32x16_bf16 %0, %1, a[32:35], %0" : "+v"(acc[0][0]) : "v"(wf[0]));
  SCHED_FENCE();
  wf[3] = tr_frag(lb2[1][1] + 5120, lb2[1][1] + 5120 + 256);
  e1[2] = fmul_s(acc[2][0][6], acc[2][0][14]);
  e1[3] = fmul_s(acc[2][0][7], acc[2][0][15]);
  e0[0] = ffma_s(e0[0], cv[0], cv[0]);
  SCHED_FENCE();
  asm volatile("v_mfma_f32_32x32x16_bf16 %0, %1, a[36:39], %0" : "+v"(acc[0][1]) : "v"(wf[0]));
  SCHED_FENCE();
  glds16(rw, vW2, gw[1] + scB + 294976, dW + 40960);
  e0[1] = ffma_s(e0[1], cv[0], cv[0]);
  e0[2] = ffma_s(e0[2], cv[0], cv[0]);
  e0[3] = ffma_s(e0[3], cv[0], cv[0]);
  e0[0] = __builtin_amdgcn_rcpf(e0[0]);
  SCHED_FENCE();
  asm volatile("v_mfma_f32_32x32x16_bf16 %0, %1, a[32:35], %0" : "+v"(acc[1][0]) : "v"(wf[1]));
  SCHED_FENCE();
  wf[0] = tr_frag(lb2[0][1] + 8192, lb2[0][1] + 8192 + 256);
  e0[1] = __builtin_amdgcn_rcpf(e0[1]);
  e0[2] = __builtin_amdgcn_rcpf(e0[2]);
  e0[3] = __builtin_amdgcn_rcpf(e0[3]);
  SCHED_FENCE();
  asm volatile("v_mfma_f32_32x32x16_bf16 %0, %1, a[36:39], %0" : "+v"(acc[1][1]) : "v"(wf[1]));
  SCHED_FENCE();
  e1[0] = fmul_s(e1[0], e0[0]);
  e1[1] = fmul_s(e1[1], e0[1]);
  e1[2] = fmul_s(e1[2], e0[2]);
  SCHED_FENCE();
  asm volatile("v_mfma_f32_32x32x16_bf16 %0, %1, a[40:43], %0" : "+v"(acc[0][0]) : "v"(wf[2]));
  SCHED_FENCE();
  wf[1] = tr_frag(lb2[1][1] + 8192, lb2[1][1] + 8192 + 256);
  e1[3] = fmul_s(e1[3], e0[3]);
  pk[2] = pack_bf16(e1[0], e1[1]);
  pk[3] = pack_bf16(e1[2], e1[3]);
  __builtin_amdgcn_raw_buffer_store_b128((u32x4){pk[0], pk[1], pk[2], pk[3]}, ract, vlin, sactA + 2048, 16);
  SCHED_FENCE();
  asm volatile("v_mfma_f32_32x32x16_bf16 %0, %1, a[44:47], %0" : "+v"(acc[0][1]) : "v"(wf[2]));
  SCHED_FENCE();
  e0[0] = fmul_s(acc[2][1][0], acc[2][1][0]);
  e0[1] = fmul_s(acc[2][1][1], acc[2][1][1]);
  e0[2] = fmul_s(acc[2][1][2], acc[2][1][2]);
  SCHED_FENCE();
  asm volatile("v_mfma_f32_32x32x16_bf16 %0, %1, a[40:43], %0" : "+v"(acc[1][0]) : "v"(wf[3]));
  SCHED_FENCE();
  wf[2] = tr_frag(lb2[0][1] + 9216, lb2[0][1] + 9216 + 256);
  e0[3] = fmul_s(acc[2][1][3], acc[2][1][3]);
  e0[0] = ffma_s(e0[0], ca1[1], ca0[1]);
  e0[1] = ffma_s(e0[1], ca1[1], ca0[1]);
  SCHED_FENCE();
  asm volatile("v_mfma_f32_32x32x16_bf16 %0, %1, a[44:47], %0" : "+v"(acc[1][1]) : "v"(wf[3]));
  SCHED_FENCE();
  glds16(rw, vW2, gw[2] + scB + 64, dW + 45056);
  e0[2] = ffma_s(e0[2], ca1[1], ca0[1]);
  e0[3] = ffma_s(e0[3], ca1[1], ca0[1]);
  e0[0] = fmul_s(e0[0], acc[2][1][0]);
  e0[1] = fmul_s(e0[1], acc[2][1][1]);
  SCHED_FENCE();
  asm volatile("v_mfma_f32_32x32x16_bf16 %0, %1, a[48:51], %0" : "+v"(acc[0][0]) : "v"(wf[0]));
  SCHED_FENCE();
  wf[3] = tr_frag(lb2[1][1] + 9216, lb2[1][1] + 9216 + 256);
  e0[2] = fmul_s(e0[2], acc[2][1][2]);
  e0[3] = fmul_s(e0[3], acc[2][1][3]);
  e0[0] = __builtin_amdgcn_exp2f(e0[0]);
  SCHED_FENCE();
  asm volatile("v_mfma_f32_32x32x16_bf16 %0, %1, a[52:55], %0" : "+v"(acc[0][1]) : "v"(wf[0]));
  SCHED_FENCE();
  e0[1] = __builtin_amdgcn_exp2f(e0[1]);
  e0[2] = __builtin_amdgcn_exp2f(e0[2]);
  e0[3] = __builtin_amdgcn_exp2f(e0[3]);
  SCHED_FENCE();
  asm volatile("v_mfma_f32_32x32x16_bf16 %0, %1, a[48:51], %0" : "+v"(acc[1][0]) : "v"(wf[1]));
  SCHED_FENCE();
  wf[0] = tr_frag(lb2[0][1] + 12288, lb2[0][1] + 12288 + 256);
  e1[0] = fmul_s(acc[2][1][0], acc[2][1][8]);
  e1[1] = fmul_s(acc[2][1][1], acc[2][1][9]);
  e1[2] = fmul_s(acc[2][1][2], acc[2][1][10]);
  SCHED_FENCE();
  asm volatile("v_mfma_f32_32x32x16_bf16 %0, %1, a[52:55], %0" : "+v"(acc[1][1]) : "v"(wf[1]));
  SCHED_FENCE();
  e1[3] = fmul_s(acc[2][1][3], acc[2][1][11]);
  e0[0] = ffma_s(e0[0], cv[1], cv[1]);
  e0[1] = ffma_s(e0[1], cv[1], cv[1]);
  e0[2] = ffma_s(e0[2], cv[1], cv[1]);
  SCHED_FENCE();
  asm volatile("v_mfma_f32_32x32x16_bf16 %0, %1, a[56:59], %0" : "+v"(acc[0][0]) : "v"(wf[2]));
  SCHED_FENCE();
  wf[1] = tr_frag(lb2[1][1] + 12288, lb2[1][1] + 12288 + 256);
  e0[3] = ffma_s(e0[3], cv[1], cv[1]);
  e0[0] = __builtin_amdgcn_rcpf(e0[0]);
  e0[1] = __builtin_amdgcn_rcpf(e0[1]);
  SCHED_FENCE();
  asm volatile("v_mfma_f32_32x32x16_bf16 %0, %1, a[60:63], %0" : "+v"(acc[0][1]) : "v"(wf[2]));
  SCHED_FENCE();
  glds16(rw, vW2, gw[2] + scB + 294976, dW + 98304);
  e0[2] = __builtin_amdgcn_rcpf(e0[2]);
  e0[3] = __builtin_amdgcn_rcpf(e0[3]);
  e1[0] = fmul_s(e1[0], e0[0]);
  SCHED_FENCE();
  asm volatile("v_mfma_f32_32x32x16_bf16 %0, %1, a[56:59], %0" : "+v"(acc[1][0]) : "v"(wf[3]));
  SCHED_FENCE();
  wf[2] = tr_frag(lb2[0][1] + 13312, lb2[0][1] + 13312 + 256);
  e1[1] = fmul_s(e1[1], e0[1]);
  e1[2] = fmul_s(e1[2], e0[2]);
  e1[3] = fmul_s(e1[3], e0[3]);
  pk[0] = pack_bf16(e1[0], e1[1]);
  SCHED_FENCE();
  asm volatile("v_mfma_f32_32x32x16_bf16 %0, %1, a[60:63], %0" : "+v"(acc[1][1]) : "v"(wf[3]));
  SCHED_FENCE();
  pk[1] = pack_bf16(e1[2], e1[3]);
  e0[0] = fmul_s(acc[2][1][4], acc[2][1][4]);
  e0[1] = fmul_s(acc[2][1][5], acc[2][1][5]);
  SCHED_FENCE();
  asm volatile("v_mfma_f32_32x32x16_bf16 %0, %1, a[64:67], %0" : "+v"(acc[0][0]) : "v"(wf[0]));
  SCHED_FENCE();
  wf[3] = tr_frag(lb2[1][1] + 13312, lb2[1][1] + 13312 + 256);
  e0[2] = fmul_s(acc[2][1][6], acc[2][1][6]);
  e0[3] = fmul_s(acc[2][1][7], acc[2][1][7]);
  e0[0] = ffma_s(e0[0], ca1[1], ca0[1]);
  SCHED_FENCE();
  asm volatile("v_mfma_f32_32x32x16_bf16 %0, %1, a[68:71], %0" : "+v"(acc[0][1]) : "v"(wf[0]));
  SCHED_FENCE();
  e0[1] = ffma_s(e0[1], ca1[1], ca0[1]);
  e0[2] = ffma_s(e0[2], ca1[1], ca0[1]);
  e0[3] = ffma_s(e0[3], ca1[1], ca0[1]);
  SCHED_FENCE();
  asm volatile("v_mfma_f32_32x32x16_bf16 %0, %1, a[64:67], %0" : "+v"(acc[1][0]) : "v"(wf[1]));
  SCHED_FENCE();
  wf[0] = tr_frag(lb2[0][1] + 16384, lb2[0][1] + 16384 + 256);
  e0[0] = fmul_s(e0[0], acc[2][1][4]);
  e0[1] = fmul_s(e0[1], acc[2][1][5]);
  e0[2] = fmul_s(e0[2], acc[2][1][6]);
  e0[3] = fmul_s(e0[3], acc[2][1][7]);
  SCHED_FENCE();
  asm volatile("v_mfma_f32_32x32x16_bf16 %0, %1, a[68:71], %0" : "+v"(acc[1][1]) : "v"(wf[1]));
  SCHED_FENCE();
  glds16(rw, vW2, gw[3] + scB + 64, dW + 102400);
  e0[0] = __builtin_amdgcn_exp2f(e0[0]);
  e0[1] = __builtin_amdgcn_exp2f(e0[1]);
  e0[2] = __builtin_amdgcn_exp2f(e0[2]);
  SCHED_FENCE();
  asm volatile("v_mfma_f32_32x32x16_bf16 %0, %1, a[72:75], %0" : "+v"(acc[0][0]) : "v"(wf[2]));
  SCHED_FENCE();
  wf[1] = tr_frag(lb2[1][1] + 16384, lb2[1][1] + 16384 + 256);
  e0[3] = __builtin_amdgcn_exp2f(e0[3]);
  e1[0] = fmul_s(acc[2][1][4], acc[2][1][12]);
  e1[1] = fmul_s(acc[2][1][5], acc[2][1][13]);
  SCHED_FENCE();
  asm volatile("v_mfma_f32_32x32x16_bf16 %0, %1, a[76:79], %0" : "+v"(acc[0][1]) : "v"(wf[2]));
  SCHED_FENCE();
  e1[2] = fmul_s(acc[2][1][6], acc[2][1][14]);
  e1[3] = fmul_s(acc[2][1][7], acc[2][1][15]);
  e0[0] = ffma_s(e0[0], cv[1], cv[1]);
  e0[1] = ffma_s(e0[1], cv[1], cv[1]);
  SCHED_FENCE();
  asm volatile("v_mfma_f32_32x32x16_bf16 %0, %1, a[72:75], %0" : "+v"(acc[1][0]) : "v"(wf[3]));
  SCHED_FENCE();
  wf[2] = tr_frag(lb2[0][1] + 17408, lb2[0][1] + 17408 + 256);
  e0[2] = ffma_s(e0[2], cv[1], cv[1]);
  e0[3] = ffma_s(e0[3], cv[1], cv[1]);
  e0[0] = __builtin_amdgcn_rcpf(e0[0]);
  SCHED_FENCE();
  asm volatile("v_mfma_f32_32x32x16_bf16 %0, %1, a[76:79], %0" : "+v"(acc[1][1]) : "v"(wf[3]));
  SCHED_FENCE();
  e0[1] = __builtin_amdgcn_rcpf(e0[1]);
  e0[2] = __builtin_amdgcn_rcpf(e0[2]);
  e0[3] = __builtin_amdgcn_rcpf(e0[3]);
  SCHED_FENCE();
  asm volatile("v_mfma_f32_32x32x16_bf16 %0, %1, a[80:83], %0" : "+v"(acc[0][0]) : "v"(wf[0]));
  SCHED_FENCE();
  wf[3] = tr_frag(lb2[1][1] + 17408, lb2[1][1] + 17408 + 256);
  e1[0] = fmul_s(e1[0], e0[0]);
  e1[1] = fmul_s(e1[1], e0[1]);
  e1[2] = fmul_s(e1[2], e0[2]);
  e1[3] = fmul_s(e1[3], e0[3]);
  SCHED_FENCE();
  asm volatile("v_mfma_f32_32x32x16_bf16 %0, %1, a[84:87], %0" : "+v"(acc[0][1]) : "v"(wf[0]));
  SCHED_FENCE();
  glds16(rw, vW2, gw[3] + scB + 294976, dW + 106496);
  pk[2] = pack_bf16(e1[0], e1[1]);
  pk[3] = pack_bf16(e1[2], e1[3]);
  __builtin_amdgcn_raw_buffer_store_b128((u32x4){pk[0], pk[1], pk[2], pk[3]}, ract, vlin, sactA + 296960, 16);
  SCHED_FENCE();
  asm volatile("v_mfma_f32_32x32x16_bf16 %0, %1, a[80:83], %0" : "+v"(acc[1][0]) : "v"(wf[1]));
  SCHED_FENCE();
  wait_vm_barrier<19>();
  wf[0] = tr_frag(lb2[0][1] + 20480, lb2[0][1] + 20480 + 256);
  e0[0] = fmul_s(acc[3][0][0], acc[3][0][0]);
  e0[1] = fmul_s(acc[3][0][1], acc[3][0][1]);
  e0[2] = fmul_s(acc[3][0][2], acc[3][0][2]);
  SCHED_FENCE();
  asm volatile("v_mfma_f32_32x32x16_bf16 %0, %1, a[84:87], %0" : "+v"(acc[1][1]) : "v"(wf[1]));
  SCHED_FENCE();
  e0[3] = fmul_s(acc[3][0][3], acc[3][0][3]);
  e0[0] = ffma_s(e0[0], ca1[0], ca0[0]);
  e0[1] = ffma_s(e0[1], ca1[0], ca0[0]);
  SCHED_FENCE();
  asm volatile("v_mfma_f32_32x32x16_bf16 %0, %1, a[88:91], %0" : "+v"(acc[0][0]) : "v"(wf[2]));
  SCHED_FENCE();
  wf[1] = tr_frag(lb2[1][1] + 20480, lb2[1][1] + 20480 + 256);
  e0[2] = ffma_s(e0[2], ca1[0], ca0[0]);
  e0[3] = ffma_s(e0[3], ca1[0], ca0[0]);
  e0[0] = fmul_s(e0[0], acc[3][0][0]);
  e0[1] = fmul_s(e0[1], acc[3][0][1]);
  SCHED_FENCE();
  asm volatile("v_mfma_f32_32x32x16_bf16 %0, %1, a[92:95], %0" : "+v"(acc[0][1]) : "v"(wf[2]));
  SCHED_FENCE();
  e0[2] = fmul_s(e0[2], acc[3][0][2]);
  e0[3] = fmul_s(e0[3], acc[3][0][3]);
  e0[0] = __builtin_amdgcn_exp2f(e0[0]);
  SCHED_FENCE();
  asm volatile("v_mfma_f32_32x32x16_bf16 %0, %1, a[88:91], %0" : "+v"(acc[1][0]) : "v"(wf[3]));
  SCHED_FENCE();
  wf[2] = tr_frag(lb2[0][1] + 21504, lb2[0][1] + 21504 + 256);
  e0[1] = __builtin_amdgcn_exp2f(e0[1]);
  e0[2] = __builtin_amdgcn_exp2f(e0[2]);
  e0[3] = __builtin_amdgcn_exp2f(e0[3]);
  SCHED_FENCE();
  asm volatile("v_mfma_f32_32x32x16_bf16 %0, %1, a[92:95], %0" : "+v"(acc[1][1]) : "v"(wf[3]));
  SCHED_FENCE();
  glds16(rw, vW2, gw[4] + scB + 64, dW + 110592);
  e1[0] = fmul_s(acc[3][0][0], acc[3][0][8]);
  e1[1] = fmul_s(acc[3][0][1], acc[3][0][9]);
  e1[2] = fmul_s(acc[3][0][2], acc[3][0][10]);
  e1[3] = fmul_s(acc[3][0][3], acc[3][0][11]);
  SCHED_FENCE();
  asm volatile("v_mfma_f32_32x32x16_bf16 %0, %1, a[96:99], %0" : "+v"(acc[0][0]) : "v"(wf[0]));
  SCHED_FENCE();
  wf[3] = tr_frag(lb2[1][1] + 21504, lb2[1][1] + 21504 + 256);
  e0[0] = ffma_s(e0[0], cv[0], cv[0]);
  e0[1] = ffma_s(e0[1], cv[0], cv[0]);
  e0[2] = ffma_s(e0[2], cv[0], cv[0]);
  SCHED_FENCE();
  asm volatile("v_mfma_f32_32x32x16_bf16 %0, %1, a[100:103], %0" : "+v"(acc[0][1]) : "v"(wf[0]));
  SCHED_FENCE();
  e0[3] = ffma_s(e0[3], cv[0], cv[0]);
  e0[0] = __builtin_amdgcn_rcpf(e0[0]);
  e0[1] = __builtin_amdgcn_rcpf(e0[1]);
  SCHED_FENCE();
  asm volatile("v_mfma_f32_32x32x16_bf16 %0, %1, a[96:99], %0" : "+v"(acc[1][0]) : "v"(wf[1]));
  SCHED_FENCE();
  wf[0] = tr_frag(lb2[0][1] + 24576, lb2[0][1] + 24576 + 256);
  e0[2] = __builtin_amdgcn_rcpf(e0[2]);
  e0[3] = __builtin_amdgcn_rcpf(e0[3]);
  e1[0] = fmul_s(e1[0], e0[0]);
  pf = tr_frag(lp + 0, lp + 1024);
  SCHED_FENCE();
  asm volatile("v_mfma_f32_32x32x16_bf16 %0, %1, a[100:103], %0" : "+v"(acc[1][1]) : "v"(wf[1]));
  SCHED_FENCE();
  e1[1] = fmul_s(e1[1], e0[1]);
  e1[2] = fmul_s(e1[2], e0[2]);
  e1[3] = fmul_s(e1[3], e0[3]);
  pk[0] = pack_bf16(e1[0], e1[1]);
  SCHED_FENCE();
  asm volatile("v_mfma_f32_32x32x16_bf16 %0, %1, a[104:107], %0" : "+v"(acc[0][0]) : "v"(wf[2]));
  SCHED_FENCE();
  wf[1] = tr_frag(lb2[1][1] + 24576, lb2[1][1] + 24576 + 256);
  pk[1] = pack_bf16(e1[2], e1[3]);
  e0[0] = fmul_s(acc[3][0][4], acc[3][0][4]);
  e0[1] = fmul_s(acc[3][0][5], acc[3][0][5]);
  __builtin_amdgcn_raw_buffer_store_b128(__builtin_bit_cast(u32x4, pf), rf2, vlin, oP[0] + 0, 16);
  SCHED_FENCE();
  asm volatile("v_mfma_f32_32x32x16_bf16 %0, %1, a[108:111], %0" : "+v"(acc[0][1]) : "v"(wf[2]));
  SCHED_FENCE();
  glds16(rw, vW2, gw[4] + scB + 294976, dW + 114688);
  e0[2] = fmul_s(acc[3][0][6], acc[3][0][6]);
  e0[3] = fmul_s(acc[3][0][7], acc[3][0][7]);
  e0[0] = ffma_s(e0[0], ca1[0], ca0[0]);
  pf = tr_frag(lp + 32, lp + 1056);
  SCHED_FENCE();
  asm volatile("v_mfma_f32_32x32x16_bf16 %0, %1, a[104:107], %0" : "+v"(acc[1][0]) : "v"(wf[3]));
  SCHED_FENCE();
  wf[2] = tr_frag(lb2[0][1] + 25600, lb2[0][1] + 25600 + 256);
  e0[1] = ffma_s(e0[1], ca1[0], ca0[0]);
  e0[2] = ffma_s(e0[2], ca1[0], ca0[0]);
  e0[3] = ffma_s(e0[3], ca1[0], ca0[0]);
  e0[0] = fmul_s(e0[0], acc[3][0][4]);
  SCHED_FENCE();
  asm volatile("v_mfma_f32_32x32x16_bf16 %0, %1, a[108:111], %0" : "+v"(acc[1][1]) : "v"(wf[3]));
  SCHED_FENCE();
  e0[1] = fmul_s(e0[1], acc[3][0][5]);
  e0[2] = fmul_s(e0[2], acc[3][0][6]);
  e0[3] = fmul_s(e0[3], acc[3][0][7]);
  __builtin_amdgcn_raw_buffer_store_b128(__builtin_bit_cast(u32x4, pf), rf2, vlin, oP[0] + 147456, 16);
  SCHED_FENCE();
  asm volatile("v_mfma_f32_32x32x16_bf16 %0, %1, a[112:115], %0" : "+v"(acc[0][0]) : "v"(wf[0]));
  SCHED_FENCE();
  wf[3] = tr_frag(lb2[1][1] + 25600, lb2[1][1] + 25600 + 256);
  e0[0] = __builtin_amdgcn_exp2f(e0[0]);
  e0[1] = __builtin_amdgcn_exp2f(e0[1]);
  e0[2] = __builtin_amdgcn_exp2f(e0[2]);
  pf = tr_frag(lp + 64, lp + 1088);
  SCHED_FENCE();
  asm volatile("v_mfma_f32_32x32x16_bf16 %0, %1, a[116:119], %0" : "+v"(acc[0][1]) : "v"(wf[0]));
  SCHED_FENCE();
  e0[3] = __builtin_amdgcn_exp2f(e0[3]);
  e1[0] = fmul_s(acc[3][0][4], acc[3][0][12]);
  e1[1] = fmul_s(acc[3][0][5], acc[3][0][13]);
  e1[2] = fmul_s(acc[3][0][6], acc[3][0][14]);
  SCHED_FENCE();
  asm volatile("v_mfma_f32_32x32x16_bf16 %0, %1, a[112:115], %0" : "+v"(acc[1][0]) : "v"(wf[1]));
  SCHED_FENCE();
  wf[0] = tr_frag(lb2[0][1] + 28672, lb2[0][1] + 28672 + 256);
  e1[3] = fmul_s(acc[3][0][7], acc[3][0][15]);
  e0[0] = ffma_s(e0[0], cv[0], cv[0]);
  e0[1] = ffma_s(e0[1], cv[0], cv[0]);
  __builtin_amdgcn_raw_buffer_store_b128(__builtin_bit_cast(u32x4, pf), rf2, vlin, oP[0] + 294912, 16);
  SCHED_FENCE();
  asm volatile("v_mfma_f32_32x32x16_bf16 %0, %1, a[116:119], %0" : "+v"(acc[1][1]) : "v"(wf[1]));
  SCHED_FENCE();
  glds16(rw, vW2, gw[5] + scB + 64, dW + 118784);
  e0[2] = ffma_s(e0[2], cv[0], cv[0]);
  e0[3] = ffma_s(e0[3], cv[0], cv[0]);
  e0[0] = __builtin_amdgcn_rcpf(e0[0]);
  pf = tr_frag(lp + 96, lp + 1120);
  SCHED_FENCE();
  asm volatile("v_mfma_f32_32x32x16_bf16 %0, %1, a[120:123], %0" : "+v"(acc[0][0]) : "v"(wf[2]));
  SCHED_FENCE();
  wf[1] = tr_frag(lb2[1][1] + 28672, lb2[1][1] + 28672 + 256);
  e0[1] = __builtin_amdgcn_rcpf(e0[1]);
  e0[2] = __builtin_amdgcn_rcpf(e0[2]);
  e0[3] = __builtin_amdgcn_rcpf(e0[3]);
  SCHED_FENCE();
  asm volatile("v_mfma_f32_32x32x16_bf16 %0, %1, a[124:127], %0" : "+v"(acc[0][1]) : "v"(wf[2]));
  SCHED_FENCE();
  e1[0] = fmul_s(e1[0], e0[0]);
  e1[1] = fmul_s(e1[1], e0[1]);
  e1[2] = fmul_s(e1[2], e0[2]);
  e1[3] = fmul_s(e1[3], e0[3]);
  __builtin_amdgcn_raw_buffer_store_b128(__builtin_bit_cast(u32x4, pf), rf2, vlin, oP[0] + 442368, 16);
  SCHED_FENCE();
  asm volatile("v_mfma_f32_32x32x16_bf16 %0, %1, a[120:123], %0" : "+v"(acc[1][0]) : "v"(wf[3]));
  SCHED_FENCE();
  wf[2] = tr_frag(lb2[0][1] + 29696, lb2[0][1] + 29696 + 256);
  pk[2] = pack_bf16(e1[0], e1[1]);
  pk[3] = pack_bf16(e1[2], e1[3]);
  __builtin_amdgcn_raw_buffer_store_b128((u32x4){pk[0], pk[1], pk[2], pk[3]}, ract, vlin, sactA + 3072, 16);
  pf = tr_frag(lp + 4096, lp + 5120);
  SCHED_FENCE();
  asm volatile("v_mfma_f32_32x32x16_bf16 %0, %1, a[124:127], %0" : "+v"(acc[1][1]) : "v"(wf[3]));
  SCHED_FENCE();
  e0[0] = fmul_s(acc[3][1][0], acc[3][1][0]);
  e0[1] = fmul_s(acc[3][1][1], acc[3][1][1]);
  e0[2] = fmul_s(acc[3][1][2], acc[3][1][2]);
  SCHED_FENCE();
  asm volatile("v_mfma_f32_32x32x16_bf16 %0, %1, a[128:131], %0" : "+v"(acc[0][0]) : "v"(wf[0]));
  SCHED_FENCE();
  wf[3] = tr_frag(lb2[1][1] + 29696, lb2[1][1] + 29696 + 256);
  e0[3] = fmul_s(acc[3][1][3], acc[3][1][3]);
  e0[0] = ffma_s(e0[0], ca1[1], ca0[1]);
  e0[1] = ffma_s(e0[1], ca1[1], ca0[1]);
  e0[2] = ffma_s(e0[2], ca1[1], ca0[1]);
  __builtin_amdgcn_raw_buffer_store_b128(__builtin_bit_cast(u32x4, pf), rf2, vlin, oP[1] + 0, 16);
  SCHED_FENCE();
  asm volatile("v_mfma_f32_32x32x16_bf16 %0, %1, a[132:135], %0" : "+v"(acc[0][1]) : "v"(wf[0]));
  SCHED_FENCE();
  glds16(rw, vW2, gw[5] + scB + 294976, dW + 49152);
  e0[3] = ffma_s(e0[3], ca1[1], ca0[1]);
  e0[0] = fmul_s(e0[0], acc[3][1][0]);
  e0[1] = fmul_s(e0[1], acc[3][1][1]);
  pf = tr_frag(lp + 4128, lp + 5152);
  SCHED_FENCE();
  asm volatile("v_mfma_f32_32x32x16_bf16 %0, %1, a[128:131], %0" : "+v"(acc[1][0]) : "v"(wf[1]));
  SCHED_FENCE();
  wf[0] = tr_frag(lb2[0][1] + 32768, lb2[0][1] + 32768 + 256);
  e0[2] = fmul_s(e0[2], acc[3][1][2]);
  e0[3] = fmul_s(e0[3], acc[3][1][3]);
  e0[0] = __builtin_amdgcn_exp2f(e0[0]);
  SCHED_FENCE();
  asm volatile("v_mfma_f32_32x32x16_bf16 %0, %1, a[132:135], %0" : "+v"(acc[1][1]) : "v"(wf[1]));
  SCHED_FENCE();
  e0[1] = __builtin_amdgcn_exp2f(e0[1]);
  e0[2] = __builtin_amdgcn_exp2f(e0[2]);
  e0[3] = __builtin_amdgcn_exp2f(e0[3]);
  __builtin_amdgcn_raw_buffer_store_b128(__builtin_bit_cast(u32x4, pf), rf2, vlin, oP[1] + 147456, 16);
  SCHED_FENCE();
  asm volatile("v_mfma_f32_32x32x16_bf16 %0, %1, a[136:139], %0" : "+v"(acc[0][0]) : "v"(wf[2]));
  SCHED_FENCE();
  wf[1] = tr_frag(lb2[1][1] + 32768, lb2[1][1] + 32768 + 256);
  e1[0] = fmul_s(acc[3][1][0], acc[3][1][8]);
  e1[1] = fmul_s(acc[3][1][1], acc[3][1][9]);
  e1[2] = fmul_s(acc[3][1][2], acc[3][1][10]);
  e1[3] = fmul_s(acc[3][1][3], acc[3][1][11]);
  pf = tr_frag(lp + 4160, lp + 5184);
  SCHED_FENCE();
  asm volatile("v_mfma_f32_32x32x16_bf16 %0, %1, a[140:143], %0" : "+v"(acc[0][1]) : "v"(wf[2]));
  SCHED_FENCE();
  e0[0] = ffma_s(e0[0], cv[1], cv[1]);
  e0[1] = ffma_s(e0[1], cv[1], cv[1]);
  e0[2] = ffma_s(e0[2], cv[1], cv[1]);
  SCHED_FENCE();
  asm volatile("v_mfma_f32_32x32x16_bf16 %0, %1, a[136:139], %0" : "+v"(acc[1][0]) : "v"(wf[3]));
  SCHED_FENCE();
  wf[2] = tr_frag(lb2[0][1] + 33792, lb2[0][1] + 33792 + 256);
  e0[3] = ffma_s(e0[3], cv[1], cv[1]);
  e0[0] = __builtin_amdgcn_rcpf(e0[0]);
  e0[1] = __builtin_amdgcn_rcpf(e0[1]);
  __builtin_amdgcn_raw_buffer_store_b128(__builtin_bit_cast(u32x4, pf), rf2, vlin, oP[1] + 294912, 16);
  SCHED_FENCE();
  asm volatile("v_mfma_f32_32x32x16_bf16 %0, %1, a[140:143], %0" : "+v"(acc[1][1]) : "v"(wf[3]));
  SCHED_FENCE();
  glds16(rw, vW2, gw[6] + scB + 64, dW + 122880);
  e0[2] = __builtin_amdgcn_rcpf(e0[2]);
  e0[3] = __builtin_amdgcn_rcpf(e0[3]);
  e1[0] = fmul_s(e1[0], e0[0]);
  e1[1] = fmul_s(e1[1], e0[1]);
  pf = tr_frag(lp + 4192, lp + 5216);
  SCHED_FENCE();
  asm volatile("v_mfma_f32_32x32x16_bf16 %0, %1, a[144:147], %0" : "+v"(acc[0][0]) : "v"(wf[0]));
  SCHED_FENCE();
  wf[3] = tr_frag(lb2[1][1] + 33792, lb2[1][1] + 33792 + 256);
  e1[2] = fmul_s(e1[2], e0[2]);
  e1[3] = fmul_s(e1[3], e0[3]);
  pk[0] = pack_bf16(e1[0], e1[1]);
  SCHED_FENCE();
  asm volatile("v_mfma_f32_32x32x16_bf16 %0, %1, a[148:151], %0" : "+v"(acc[0][1]) : "v"(wf[0]));
  SCHED_FENCE();
  pk[1] = pack_bf16(e1[2], e1[3]);
  e0[0] = fmul_s(acc[3][1][4], acc[3][1][4]);
  e0[1] = fmul_s(acc[3][1][5], acc[3][1][5]);
  __builtin_amdgcn_raw_buffer_store_b128(__builtin_bit_cast(u32x4, pf), rf2, vlin, oP[1] + 442368, 16);
  SCHED_FENCE();
  asm volatile("v_mfma_f32_32x32x16_bf16 %0, %1, a[144:147], %0" : "+v"(acc[1][0]) : "v"(wf[1]));
  SCHED_FENCE();
  wf[0] = tr_frag(lb2[0][1] + 36864, lb2[0][1] + 36864 + 256);
  e0[2] = fmul_s(acc[3][1][6], acc[3][1][6]);
  e0[3] = fmul_s(acc[3][1][7], acc[3][1][7]);
  e0[0] = ffma_s(e0[0], ca1[1], ca0[1]);
  SCHED_FENCE();
  asm volatile("v_mfma_f32_32x32x16_bf16 %0, %1, a[148:151], %0" : "+v"(acc[1][1]) : "v"(wf[1]));
  SCHED_FENCE();
  e0[1] = ffma_s(e0[1], ca1[1], ca0[1]);
  e0[2] = ffma_s(e0[2], ca1[1], ca0[1]);
  e0[3] = ffma_s(e0[3], ca1[1], ca0[1]);
  e0[0] = fmul_s(e0[0], acc[3][1][4]);
  SCHED_FENCE();
  asm volatile("v_mfma_f32_32x32x16_bf16 %0, %1, a[152:155], %0" : "+v"(acc[0][0]) : "v"(wf[2]));
  SCHED_FENCE();
  wf[1] = tr_frag(lb2[1][1] + 36864, lb2[1][1] + 36864 + 256);
  e0[1] = fmul_s(e0[1], acc[3][1][5]);
  e0[2] = fmul_s(e0[2], acc[3][1][6]);
  e0[3] = fmul_s(e0[3], acc[3][1][7]);
  SCHED_FENCE();
  asm volatile("v_mfma_f32_32x32x16_bf16 %0, %1, a[156:159], %0" : "+v"(acc[0][1]) : "v"(wf[2]));
  SCHED_FENCE();
  glds16(rw, vW2, gw[6] + scB + 294976, dW + 126976);
  e0[0] = __builtin_amdgcn_exp2f(e0[0]);
  e0[1] = __builtin_amdgcn_exp2f(e0[1]);
  e0[2] = __builtin_amdgcn_exp2f(e0[2]);
  SCHED_FENCE();
  asm volatile("v_mfma_f32_32x32x16_bf16 %0, %1, a[152:155], %0" : "+v"(acc[1][0]) : "v"(wf[3]));
  SCHED_FENCE();
  wf[2] = tr_frag(lb2[0][1] + 37888, lb2[0][1] + 37888 + 256);
  e0[3] = __builtin_amdgcn_exp2f(e0[3]);
  e1[0] = fmul_s(acc[3][1][4], acc[3][1][12]);
  e1[1] = fmul_s(acc[3][1][5], acc[3][1][13]);
  e1[2] = fmul_s(acc[3][1][6], acc[3][1][14]);
  SCHED_FENCE();
  asm volatile("v_mfma_f32_32x32x16_bf16 %0, %1, a[156:159], %0" : "+v"(acc[1][1]) : "v"(wf[3]));
  SCHED_FENCE();
  e1[3] = fmul_s(acc[3][1][7], acc[3][1][15]);
  e0[0] = ffma_s(e0[0], cv[1], cv[1]);
  e0[1] = ffma_s(e0[1], cv[1], cv[1]);
  SCHED_FENCE();
  asm volatile("v_mfma_f32_32x32x16_bf16 %0, %1, a[160:163], %0" : "+v"(acc[0][0]) : "v"(wf[0]));
  SCHED_FENCE();
  wf[3] = tr_frag(lb2[1][1] + 37888, lb2[1][1] + 37888 + 256);
  e0[2] = ffma_s(e0[2], cv[1], cv[1]);
  e0[3] = ffma_s(e0[3], cv[1], cv[1]);
  e0[0] = __builtin_amdgcn_rcpf(e0[0]);
  SCHED_FENCE();
  asm volatile("v_mfma_f32_32x32x16_bf16 %0, %1, a[164:167], %0" : "+v"(acc[0][1]) : "v"(wf[0]));
  SCHED_FENCE();
  e0[1] = __builtin_amdgcn_rcpf(e0[1]);
  e0[2] = __builtin_amdgcn_rcpf(e0[2]);
  e0[3] = __builtin_amdgcn_rcpf(e0[3]);
  e1[0] = fmul_s(e1[0], e0[0]);
  SCHED_FENCE();
  asm volatile("v_mfma_f32_32x32x16_bf16 %0, %1, a[160:163], %0" : "+v"(acc[1][0]) : "v"(wf[1]));
  SCHED_FENCE();
  wf[0] = tr_frag(lb2[0][0] + 0, lb2[0][0] + 0 + 256);
  e1[1] = fmul_s(e1[1], e0[1]);
  e1[2] = fmul_s(e1[2], e0[2]);
  e1[3] = fmul_s(e1[3], e0[3]);
  SCHED_FENCE();
  asm volatile("v_mfma_f32_32x32x16_bf16 %0, %1, a[164:167], %0" : "+v"(acc[1][1]) : "v"(wf[1]));
  SCHED_FENCE();
  pk[2] = pack_bf16(e1[0], e1[1]);
  pk[3] = pack_bf16(e1[2], e1[3]);
  __builtin_amdgcn_raw_buffer_store_b128((u32x4){pk[0], pk[1], pk[2], pk[3]}, ract, vlin, sactA + 297984, 16);
  SCHED_FENCE();
  asm volatile("v_mfma_f32_32x32x16_bf16 %0, %1, a[168:171], %0" : "+v"(acc[0][0]) : "v"(wf[2]));
  SCHED_FENCE();
  wf[1] = tr_frag(lb2[1][0] + 0, lb2[1][0] + 0 + 256);
  e0[0] = fmul_s(acc[4][0][0], acc[4][0][0]);
  e0[1] = fmul_s(acc[4][0][1], acc[4][0][1]);
  e0[2] = fmul_s(acc[4][0][2], acc[4][0][2]);
  SCHED_FENCE();
  asm volatile("v_mfma_f32_32x32x16_bf16 %0, %1, a[172:175], %0" : "+v"(acc[0][1]) : "v"(wf[2]));
  SCHED_FENCE();
  e0[3] = fmul_s(acc[4][0][3], acc[4][0][3]);
  e0[0] = ffma_s(e0[0], ca1[0], ca0[0]);
  e0[1] = ffma_s(e0[1], ca1[0], ca0[0]);
  e0[2] = ffma_s(e0[2], ca1[0], ca0[0]);
  SCHED_FENCE();
  asm volatile("v_mfma_f32_32x32x16_bf16 %0, %1, a[168:171], %0" : "+v"(acc[1][0]) : "v"(wf[3]));
  SCHED_FENCE();
  wf[2] = tr_frag(lb2[0][0] + 1024, lb2[0][0] + 1024 + 256);
  e0[3] = ffma_s(e0[3], ca1[0], ca0[0]);
  e0[0] = fmul_s(e0[0], acc[4][0][0]);
  e0[1] = fmul_s(e0[1], acc[4][0][1]);
  SCHED_FENCE();
  asm volatile("v_mfma_f32_32x32x16_bf16 %0, %1, a[172:175], %0" : "+v"(acc[1][1]) : "v"(wf[3]));
  SCHED_FENCE();
  e0[2] = fmul_s(e0[2], acc[4][0][2]);
  e0[3] = fmul_s(e0[3], acc[4][0][3]);
  e0[0] = __builtin_amdgcn_exp2f(e0[0]);
  SCHED_FENCE();
  asm volatile("v_mfma_f32_32x32x16_bf16 %0, %1, a[176:179], %0" : "+v"(acc[0][0]) : "v"(wf[0]));
  SCHED_FENCE();
  wf[3] = tr_frag(lb2[1][0] + 1024, lb2[1][0] + 1024 + 256);
  e0[1] = __builtin_amdgcn_exp2f(e0[1]);
  e0[2] = __builtin_amdgcn_exp2f(e0[2]);
  e0[3] = __builtin_amdgcn_exp2f(e0[3]);
  e1[0] = fmul_s(acc[4][0][0], acc[4][0][8]);
  SCHED_FENCE();
  asm volatile("v_mfma_f32_32x32x16_bf16 %0, %1, a[180:183], %0" : "+v"(acc[0][1]) : "v"(wf[0]));
  SCHED_FENCE();
  e1[1] = fmul_s(acc[4][0][1], acc[4][0][9]);
  e1[2] = fmul_s(acc[4][0][2], acc[4][0][10]);
  e1[3] = fmul_s(acc[4][0][3], acc[4][0][11]);
  SCHED_FENCE();
  asm volatile("v_mfma_f32_32x32x16_bf16 %0, %1, a[176:179], %0" : "+v"(acc[1][0]) : "v"(wf[1]));
  SCHED_FENCE();
  wait_vm_barrier<27>();
  wf[0] = tr_frag(lb2[0][0] + 4096, lb2[0][0] + 4096 + 256);
  e0[0] = ffma_s(e0[0], cv[0], cv[0]);
  e0[1] = ffma_s(e0[1], cv[0], cv[0]);
  e0[2] = ffma_s(e0[2], cv[0], cv[0]);
  SCHED_FENCE();
  asm volatile("v_mfma_f32_32x32x16_bf16 %0, %1, a[180:183], %0" : "+v"(acc[1][1]) : "v"(wf[1]));
  SCHED_FENCE();
  glds16(rw, vW2, gw[7] + scB + 64, dW + 53248);
  e0[3] = ffma_s(e0[3], cv[0], cv[0]);
  e0[0] = __builtin_amdgcn_rcpf(e0[0]);
  e0[1] = __builtin_amdgcn_rcpf(e0[1]);
  SCHED_FENCE();
  asm volatile("v_mfma_f32_32x32x16_bf16 %0, %1, a[184:187], %0" : "+v"(acc[0][0]) : "v"(wf[2]));
  SCHED_FENCE();
  wf[1] = tr_frag(lb2[1][0] + 4096, lb2[1][0] + 4096 + 256);
  e0[2] = __builtin_amdgcn_rcpf(e0[2]);
  e0[3] = __builtin_amdgcn_rcpf(e0[3]);
  e1[0] = fmul_s(e1[0], e0[0]);
  e1[1] = fmul_s(e1[1], e0[1]);
  SCHED_FENCE();
  asm volatile("v_mfma_f32_32x32x16_bf16 %0, %1, a[188:191], %0" : "+v"(acc[0][1]) : "v"(wf[2]));
  SCHED_FENCE();
  e1[2] = fmul_s(e1[2], e0[2]);
  e1[3] = fmul_s(e1[3], e0[3]);
  pk[0] = pack_bf16(e1[0], e1[1]);
  SCHED_FENCE();
  asm volatile("v_mfma_f32_32x32x16_bf16 %0, %1, a[184:187], %0" : "+v"(acc[1][0]) : "v"(wf[3]));
  SCHED_FENCE();
  wf[2] = tr_frag(lb2[0][0] + 5120, lb2[0][0] + 5120 + 256);
  pk[1] = pack_bf16(e1[2], e1[3]);
  e0[0] = fmul_s(acc[4][0][4], acc[4][0][4]);
  e0[1] = fmul_s(acc[4][0][5], acc[4][0][5]);
  SCHED_FENCE();
  asm volatile("v_mfma_f32_32x32x16_bf16 %0, %1, a[188:191], %0" : "+v"(acc[1][1]) : "v"(wf[3]));
  SCHED_FENCE();
  e0[2] = fmul_s(acc[4][0][6], acc[4][0][6]);
  e0[3] = fmul_s(acc[4][0][7], acc[4][0][7]);
  e0[0] = ffma_s(e0[0], ca1[0], ca0[0]);
  e0[1] = ffma_s(e0[1], ca1[0], ca0[0]);
  SCHED_FENCE();
  asm volatile("v_mfma_f32_32x32x16_bf16 %0, %1, a[192:195], %0" : "+v"(acc[0][0]) : "v"(wf[0]));
  SCHED_FENCE();
  wf[3] = tr_frag(lb2[1][0] + 5120, lb2[1][0] + 5120 + 256);
  e0[2] = ffma_s(e0[2], ca1[0], ca0[0]);
  e0[3] = ffma_s(e0[3], ca1[0], ca0[0]);
  e0[0] = fmul_s(e0[0], acc[4][0][4]);
  SCHED_FENCE();
  asm volatile("v_mfma_f32_32x32x16_bf16 %0, %1, a[196:199], %0" : "+v"(acc[0][1]) : "v"(wf[0]));
  SCHED_FENCE();
  glds16(rw, vW2, gw[7] + scB + 294976, dW + 57344);
  e0[1] = fmul_s(e0[1], acc[4][0][5]);
  e0[2] = fmul_s(e0[2], acc[4][0][6]);
  e0[3] = fmul_s(e0[3], acc[4][0][7]);
  SCHED_FENCE();
  asm volatile("v_mfma_f32_32x32x16_bf16 %0, %1, a[192:195], %0" : "+v"(acc[1][0]) : "v"(wf[1]));
  SCHED_FENCE();
  wf[0] = tr_frag(lb2[0][0] + 8192, lb2[0][0] + 8192 + 256);
  e0[0] = __builtin_amdgcn_exp2f(e0[0]);
  e0[1] = __builtin_amdgcn_exp2f(e0[1]);
  e0[2] = __builtin_amdgcn_exp2f(e0[2]);
  e0[3] = __builtin_amdgcn_exp2f(e0[3]);
  SCHED_FENCE();
  asm volatile("v_mfma_f32_32x32x16_bf16 %0, %1, a[196:199], %0" : "+v"(acc[1][1]) : "v"(wf[1]));
  SCHED_FENCE();
  e1[0] = fmul_s(acc[4][0][4], acc[4][0][12]);
  e1[1] = fmul_s(acc[4][0][5], acc[4][0][13]);
  e1[2] = fmul_s(acc[4][0][6], acc[4][0][14]);
  SCHED_FENCE();
  asm volatile("v_mfma_f32_32x32x16_bf16 %0, %1, a[200:203], %0" : "+v"(acc[0][0]) : "v"(wf[2]));
  SCHED_FENCE();
  wf[1] = tr_frag(lb2[1][0] + 8192, lb2[1][0] + 8192 + 256);
  e1[3] = fmul_s(acc[4][0][7], acc[4][0][15]);
  e0[0] = ffma_s(e0[0], cv[0], cv[0]);
  e0[1] = ffma_s(e0[1], cv[0], cv[0]);
  SCHED_FENCE();
  asm volatile("v_mfma_f32_32x32x16_bf16 %0, %1, a[204:207], %0" : "+v"(acc[0][1]) : "v"(wf[2]));
  SCHED_FENCE();
  e0[2] = ffma_s(e0[2], cv[0], cv[0]);
  e0[3] = ffma_s(e0[3], cv[0], cv[0]);
  e0[0] = __builtin_amdgcn_rcpf(e0[0]);
  SCHED_FENCE();
  asm volatile("v_mfma_f32_32x32x16_bf16 %0, %1, a[200:203], %0" : "+v"(acc[1][0]) : "v"(wf[3]));
  SCHED_FENCE();
  wf[2] = tr_frag(lb2[0][0] + 9216, lb2[0][0] + 9216 + 256);
  e0[1] = __builtin_amdgcn_rcpf(e0[1]);
  e0[2] = __builtin_amdgcn_rcpf(e0[2]);
  e0[3] = __builtin_amdgcn_rcpf(e0[3]);
  e1[0] = fmul_s(e1[0], e0[0]);
  SCHED_FENCE();
  asm volatile("v_mfma_f32_32x32x16_bf16 %0, %1, a[204:207], %0" : "+v"(acc[1][1]) : "v"(wf[3]));
  SCHED_FENCE();
  glds16(rw, vW2, gw[8] + scB + 64, dW + 61440);
  e1[1] = fmul_s(e1[1], e0[1]);
  e1[2] = fmul_s(e1[2], e0[2]);
  e1[3] = fmul_s(e1[3], e0[3]);
  SCHED_FENCE();
  asm volatile("v_mfma_f32_32x32x16_bf16 %0, %1, a[208:211], %0" : "+v"(acc[0][0]) : "v"(wf[0]));
  SCHED_FENCE();
  wf[3] = tr_frag(lb2[1][0] + 9216, lb2[1][0] + 9216 + 256);
  pk[2] = pack_bf16(e1[0], e1[1]);
  pk[3] = pack_bf16(e1[2], e1[3]);
  __builtin_amdgcn_raw_buffer_store_b128((u32x4){pk[0], pk[1], pk[2], pk[3]}, ract, vlin, sactC + 0, 16);
  SCHED_FENCE();
  asm volatile("v_mfma_f32_32x32x16_bf16 %0, %1, a[212:215], %0" : "+v"(acc[0][1]) : "v"(wf[0]));
  SCHED_FENCE();
  e0[0] = fmul_s(acc[4][1][0], acc[4][1][0]);
  e0[1] = fmul_s(acc[4][1][1], acc[4][1][1]);
  e0[2] = fmul_s(acc[4][1][2], acc[4][1][2]);
  e0[3] = fmul_s(acc[4][1][3], acc[4][1][3]);
  SCHED_FENCE();
  asm volatile("v_mfma_f32_32x32x16_bf16 %0, %1, a[208:211], %0" : "+v"(acc[1][0]) : "v"(wf[1]));
  SCHED_FENCE();
  wf[0] = tr_frag(lb2[0][0] + 12288, lb2[0][0] + 12288 + 256);
  e0[0] = ffma_s(e0[0], ca1[1], ca0[1]);
  e0[1] = ffma_s(e0[1], ca1[1], ca0[1]);
  e0[2] = ffma_s(e0[2], ca1[1], ca0[1]);
  SCHED_FENCE();
  asm volatile("v_mfma_f32_32x32x16_bf16 %0, %1, a[212:215], %0" : "+v"(acc[1][1]) : "v"(wf[1]));
  SCHED_FENCE();
  e0[3] = ffma_s(e0[3], ca1[1], ca0[1]);
  e0[0] = fmul_s(e0[0], acc[4][1][0]);
  e0[1] = fmul_s(e0[1], acc[4][1][1]);
  SCHED_FENCE();
  asm volatile("v_mfma_f32_32x32x16_bf16 %0, %1, a[216:219], %0" : "+v"(acc[0][0]) : "v"(wf[2]));
  SCHED_FENCE();
  wf[1] = tr_frag(lb2[1][0] + 12288, lb2[1][0] + 12288 + 256);
  e0[2] = fmul_s(e0[2], acc[4][1][2]);
  e0[3] = fmul_s(e0[3], acc[4][1][3]);
  e0[0] = __builtin_amdgcn_exp2f(e0[0]);
  SCHED_FENCE();
  asm volatile("v_mfma_f32_32x32x16_bf16 %0, %1, a[220:223], %0" : "+v"(acc[0][1]) : "v"(wf[2]));
  SCHED_FENCE();
  glds16(rw, vW2, gw[8] + scB + 294976, dW + 65536);
  e0[1] = __builtin_amdgcn_exp2f(e0[1]);
  e0[2] = __builtin_amdgcn_exp2f(e0[2]);
  e0[3] = __builtin_amdgcn_exp2f(e0[3]);
  e1[0] = fmul_s(acc[4][1][0], acc[4][1][8]);
  SCHED_FENCE();
  asm volatile("v_mfma_f32_32x32x16_bf16 %0, %1, a[216:219], %0" : "+v"(acc[1][0]) : "v"(wf[3]));
  SCHED_FENCE();
  wf[2] = tr_frag(lb2[0][0] + 13312, lb2[0][0] + 13312 + 256);
  e1[1] = fmul_s(acc[4][1][1], acc[4][1][9]);
  e1[2] = fmul_s(acc[4][1][2], acc[4][1][10]);
  e1[3] = fmul_s(acc[4][1][3], acc[4][1][11]);
  SCHED_FENCE();
  asm volatile("v_mfma_f32_32x32x16_bf16 %0, %1, a[220:223], %0" : "+v"(acc[1][1]) : "v"(wf[3]));
  SCHED_FENCE();
  e0[0] = ffma_s(e0[0], cv[1], cv[1]);
  e0[1] = ffma_s(e0[1], cv[1], cv[1]);
  e0[2] = ffma_s(e0[2], cv[1], cv[1]);
  SCHED_FENCE();
  asm volatile("v_mfma_f32_32x32x16_bf16 %0, %1, a[224:227], %0" : "+v"(acc[0][0]) : "v"(wf[0]));
  SCHED_FENCE();
  wf[3] = tr_frag(lb2[1][0] + 13312, lb2[1][0] + 13312 + 256);
  e0[3] = ffma_s(e0[3], cv[1], cv[1]);
  e0[0] = __builtin_amdgcn_rcpf(e0[0]);
  e0[1] = __builtin_amdgcn_rcpf(e0[1]);
  e0[2] = __builtin_amdgcn_rcpf(e0[2]);
  SCHED_FENCE();
  asm volatile("v_mfma_f32_32x32x16_bf16 %0, %1, a[228:231], %0" : "+v"(acc[0][1]) : "v"(wf[0]));
  SCHED_FENCE();
  e0[3] = __builtin_amdgcn_rcpf(e0[3]);
  e1[0] = fmul_s(e1[0], e0[0]);
  e1[1] = fmul_s(e1[1], e0[1]);
  SCHED_FENCE();
  asm volatile("v_mfma_f32_32x32x16_bf16 %0, %1, a[224:227], %0" : "+v"(acc[1][0]) : "v"(wf[1]));
  SCHED_FENCE();
  wf[0] = tr_frag(lb2[0][0] + 16384, lb2[0][0] + 16384 + 256);
  e1[2] = fmul_s(e1[2], e0[2]);
  e1[3] = fmul_s(e1[3], e0[3]);
  pk[0] = pack_bf16(e1[0], e1[1]);
  SCHED_FENCE();
  asm volatile("v_mfma_f32_32x32x16_bf16 %0, %1, a[228:231], %0" : "+v"(acc[1][1]) : "v"(wf[1]));
  SCHED_FENCE();
  pk[1] = pack_bf16(e1[2], e1[3]);
  e0[0] = fmul_s(acc[4][1][4], acc[4][1][4]);
  e0[1] = fmul_s(acc[4][1][5], acc[4][1][5]);
  e0[2] = fmul_s(acc[4][1][6], acc[4][1][6]);
  SCHED_FENCE();
  asm volatile("v_mfma_f32_32x32x16_bf16 %0, %1, a[232:235], %0" : "+v"(acc[0][0]) : "v"(wf[2]));
  SCHED_FENCE();
  wf[1] = tr_frag(lb2[1][0] + 16384, lb2[1][0] + 16384 + 256);
  e0[3] = fmul_s(acc[4][1][7], acc[4][1][7]);
  e0[0] = ffma_s(e0[0], ca1[1], ca0[1]);
  e0[1] = ffma_s(e0[1], ca1[1], ca0[1]);
  SCHED_FENCE();
  asm volatile("v_mfma_f32_32x32x16_bf16 %0, %1, a[236:239], %0" : "+v"(acc[0][1]) : "v"(wf[2]));
  SCHED_FENCE();
  e0[2] = ffma_s(e0[2], ca1[1], ca0[1]);
  e0[3] = ffma_s(e0[3], ca1[1], ca0[1]);
  e0[0] = fmul_s(e0[0], acc[4][1][4]);
  SCHED_FENCE();
  asm volatile("v_mfma_f32_32x32x16_bf16 %0, %1, a[232:235], %0" : "+v"(acc[1][0]) : "v"(wf[3]));
  SCHED_FENCE();
  wf[2] = tr_frag(lb2[0][0] + 17408, lb2[0][0] + 17408 + 256);
  e0[1] = fmul_s(e0[1], acc[4][1][5]);
  e0[2] = fmul_s(e0[2], acc[4][1][6]);
  e0[3] = fmul_s(e0[3], acc[4][1][7]);
  SCHED_FENCE();
  asm volatile("v_mfma_f32_32x32x16_bf16 %0, %1, a[236:239], %0" : "+v"(acc[1][1]) : "v"(wf[3]));
  SCHED_FENCE();
  e0[0] = __builtin_amdgcn_exp2f(e0[0]);
  e0[1] = __builtin_amdgcn_exp2f(e0[1]);
  e0[2] = __builtin_amdgcn_exp2f(e0[2]);
  e0[3] = __builtin_amdgcn_exp2f(e0[3]);
  SCHED_FENCE();
  asm volatile("v_mfma_f32_32x32x16_bf16 %0, %1, a[240:243], %0" : "+v"(acc[0][0]) : "v"(wf[0]));
  SCHED_FENCE();
  wf[3] = tr_frag(lb2[1][0] + 17408, lb2[1][0] + 17408 + 256);
  e1[0] = fmul_s(acc[4][1][4], acc[4][1][12]);
  e1[1] = fmul_s(acc[4][1][5], acc[4][1][13]);
  e1[2] = fmul_s(acc[4][1][6], acc[4][1][14]);
  SCHED_FENCE();
  asm volatile("v_mfma_f32_32x32x16_bf16 %0, %1, a[244:247], %0" : "+v"(acc[0][1]) : "v"(wf[0]));
  SCHED_FENCE();
  e1[3] = fmul_s(acc[4][1][7], acc[4][1][15]);
  e0[0] = ffma_s(e0[0], cv[1], cv[1]);
  e0[1] = ffma_s(e0[1], cv[1], cv[1]);
  SCHED_FENCE();
  asm volatile("v_mfma_f32_32x32x16_bf16 %0, %1, a[240:243], %0" : "+v"(acc[1][0]) : "v"(wf[1]));
  SCHED_FENCE();
  wf[0] = tr_frag(lb2[0][0] + 20480, lb2[0][0] + 20480 + 256);
  e0[2] = ffma_s(e0[2], cv[1], cv[1]);
  e0[3] = ffma_s(e0[3], cv[1], cv[1]);
  e0[0] = __builtin_amdgcn_rcpf(e0[0]);
  e0[1] = __builtin_amdgcn_rcpf(e0[1]);
  SCHED_FENCE();
  asm volatile("v_mfma_f32_32x32x16_bf16 %0, %1, a[244:247], %0" : "+v"(acc[1][1]) : "v"(wf[1]));
  SCHED_FENCE();
  e0[2] = __builtin_amdgcn_rcpf(e0[2]);
  e0[3] = __builtin_amdgcn_rcpf(e0[3]);
  e1[0] = fmul_s(e1[0], e0[0]);
  SCHED_FENCE();
  asm volatile("v_mfma_f32_32x32x16_bf16 %0, %1, a[248:251], %0" : "+v"(acc[0][0]) : "v"(wf[2]));
  SCHED_FENCE();
  wf[1] = tr_frag(lb2[1][0] + 20480, lb2[1][0] + 20480 + 256);
  e1[1] = fmul_s(e1[1], e0[1]);
  e1[2] = fmul_s(e1[2], e0[2]);
  e1[3] = fmul_s(e1[3], e0[3]);
  SCHED_FENCE();
  asm volatile("v_mfma_f32_32x32x16_bf16 %0, %1, a[252:255], %0" : "+v"(acc[0][1]) : "v"(wf[2]));
  SCHED_FENCE();
  pk[2] = pack_bf16(e1[0], e1[1]);
  pk[3] = pack_bf16(e1[2], e1[3]);
  __builtin_amdgcn_raw_buffer_store_b128((u32x4){pk[0], pk[1], pk[2], pk[3]}, ract, vlin, sactC + 294912, 16);
  SCHED_FENCE();
  asm volatile("v_mfma_f32_32x32x16_bf16 %0, %1, a[248:251], %0" : "+v"(acc[1][0]) : "v"(wf[3]));
  SCHED_FENCE();
  wf[2] = tr_frag(lb2[0][0] + 21504, lb2[0][0] + 21504 + 256);
  SCHED_FENCE();
  asm volatile("v_mfma_f32_32x32x16_bf16 %0, %1, a[252:255], %0" : "+v"(acc[1][1]) : "v"(wf[3]));
  SCHED_FENCE();
  asm volatile("v_mfma_f32_32x32x16_bf16 %0, %1, %2, %0" : "+v"(acc[0][0]) : "v"(wf[0]), "v"(tv[0][0]));
  SCHED_FENCE();
  wf[3] = tr_frag(lb2[1][0] + 21504, lb2[1][0] + 21504 + 256);
  SCHED_FENCE();
  asm volatile("v_mfma_f32_32x32x16_bf16 %0, %1, %2, %0" : "+v"(acc[0][1]) : "v"(wf[0]), "v"(tv[1][0]));
  SCHED_FENCE();
  asm volatile("v_mfma_f32_32x32x16_bf16 %0, %1, %2, %0" : "+v"(acc[1][0]) : "v"(wf[1]), "v"(tv[0][0]));
  SCHED_FENCE();
  wf[0] = tr_frag(lb2[0][0] + 24576, lb2[0][0] + 24576 + 256);
  SCHED_FENCE();
  asm volatile("v_mfma_f32_32x32x16_bf16 %0, %1, %2, %0" : "+v"(acc[1][1]) : "v"(wf[1]), "v"(tv[1][0]));
  SCHED_FENCE();
  asm volatile("v_mfma_f32_32x32x16_bf16 %0, %1, %2, %0" : "+v"(acc[0][0]) : "v"(wf[2]), "v"(tv[0][1]));
  SCHED_FENCE();
  wf[1] = tr_frag(lb2[1][0] + 24576, lb2[1][0] + 24576 + 256);
  SCHED_FENCE();
  asm volatile("v_mfma_f32_32x32x16_bf16 %0, %1, %2, %0" : "+v"(acc[0][1]) : "v"(wf[2]), "v"(tv[1][1]));
  SCHED_FENCE();
  asm volatile("v_mfma_f32_32x32x16_bf16 %0, %1, %2, %0" : "+v"(acc[1][0]) : "v"(wf[3]), "v"(tv[0][1]));
  SCHED_FENCE();
  wf[2] = tr_frag(lb2[0][0] + 25600, lb2[0][0] + 25600 + 256);
  SCHED_FENCE();
  asm volatile("v_mfma_f32_32x32x16_bf16 %0, %1, %2, %0" : "+v"(acc[1][1]) : "v"(wf[3]), "v"(tv[1][1]));
  SCHED_FENCE();
  asm volatile("v_mfma_f32_32x32x16_bf16 %0, %1, %2, %0" : "+v"(acc[0][0]) : "v"(wf[0]), "v"(tv[0][2]));
  SCHED_FENCE();
  wf[3] = tr_frag(lb2[1][0] + 25600, lb2[1][0] + 25600 + 256);
  SCHED_FENCE();
  asm volatile("v_mfma_f32_32x32x16_bf16 %0, %1, %2, %0" : "+v"(acc[0][1]) : "v"(wf[0]), "v"(tv[1][2]));
  SCHED_FENCE();
  asm volatile("v_mfma_f32_32x32x16_bf16 %0, %1, %2, %0" : "+v"(acc[1][0]) : "v"(wf[1]), "v"(tv[0][2]));
  SCHED_FENCE();
  wait_vm_barrier<25>();
  wf[0] = tr_frag(lb2[0][0] + 28672, lb2[0][0] + 28672 + 256);
  SCHED_FENCE();
  asm volatile("v_mfma_f32_32x32x16_bf16 %0, %1, %2, %0" : "+v"(acc[1][1]) : "v"(wf[1]), "v"(tv[1][2]));
  SCHED_FENCE();
  asm volatile("v_mfma_f32_32x32x16_bf16 %0, %1, %2, %0" : "+v"(acc[0][0]) : "v"(wf[2]), "v"(tv[0][3]));
  SCHED_FENCE();
  wf[1] = tr_frag(lb2[0][0] + 29696, lb2[0][0] + 29696 + 256);
  SCHED_FENCE();
  asm volatile("v_mfma_f32_32x32x16_bf16 %0, %1, %2, %0" : "+v"(acc[0][1]) : "v"(wf[2]), "v"(tv[1][3]));
  SCHED_FENCE();
  asm volatile("v_mfma_f32_32x32x16_bf16 %0, %1, %2, %0" : "+v"(acc[1][0]) : "v"(wf[3]), "v"(tv[0][3]));
  SCHED_FENCE();
  wf[2] = tr_frag(lb2[0][0] + 32768, lb2[0][0] + 32768 + 256);
  SCHED_FENCE();
  asm volatile("v_mfma_f32_32x32x16_bf16 %0, %1, %2, %0" : "+v"(acc[1][1]) : "v"(wf[3]), "v"(tv[1][3]));
  SCHED_FENCE();
  asm volatile("v_mfma_f32_32x32x16_bf16 %0, %1, a[0:3], 0" : "=&v"(acc[2][0]) : "v"(wf[0]));
  SCHED_FENCE();
  wf[3] = tr_frag(lb2[0][0] + 33792, lb2[0][0] + 33792 + 256);
  SCHED_FENCE();
  asm volatile("v_mfma_f32_32x32x16_bf16 %0, %1, a[4:7], 0" : "=&v"(acc[2][1]) : "v"(wf[0]));
  SCHED_FENCE();
  asm volatile("v_mfma_f32_32x32x16_bf16 %0, %1, a[8:11], %0" : "+v"(acc[2][0]) : "v"(wf[1]));
  SCHED_FENCE();
  wf[0] = tr_frag(lb2[0][0] + 36864, lb2[0][0] + 36864 + 256);
  SCHED_FENCE();
  asm volatile("v_mfma_f32_32x32x16_bf16 %0, %1, a[12:15], %0" : "+v"(acc[2][1]) : "v"(wf[1]));
  SCHED_FENCE();
  asm volatile("v_mfma_f32_32x32x16_bf16 %0, %1, a[16:19], %0" : "+v"(acc[2][0]) : "v"(wf[2]));
  SCHED_FENCE();
  wf[1] = tr_frag(lb2[0][0] + 37888, lb2[0][0] + 37888 + 256);
  SCHED_FENCE();
  asm volatile("v_mfma_f32_32x32x16_bf16 %0, %1, a[20:23], %0" : "+v"(acc[2][1]) : "v"(wf[2]));
  SCHED_FENCE();
  asm volatile("v_mfma_f32_32x32x16_bf16 %0, %1, a[24:27], %0" : "+v"(acc[2][0]) : "v"(wf[3]));
  SCHED_FENCE();
  wf[2] = tr_frag(lb2[0][0] + 40960, lb2[0][0] + 40960 + 256);
  e0[0] = fmul_s(acc[0][0][0], acc[0][0][0]);
  e0[1] = fmul_s(acc[0][0][1], acc[0][0][1]);
  e0[2] = fmul_s(acc[0][0][2], acc[0][0][2]);
  SCHED_FENCE();
  asm volatile("v_mfma_f32_32x32x16_bf16 %0, %1, a[28:31], %0" : "+v"(acc[2][1]) : "v"(wf[3]));
  SCHED_FENCE();
  e0[3] = fmul_s(acc[0][0][3], acc[0][0][3]);
  e0[0] = ffma_s(e0[0], ca1[0], ca0[0]);
  e0[1] = ffma_s(e0[1], ca1[0], ca0[0]);
  SCHED_FENCE();
  asm volatile("v_mfma_f32_32x32x16_bf16 %0, %1, a[32:35], %0" : "+v"(acc[2][0]) : "v"(wf[0]));
  SCHED_FENCE();
  wf[3] = tr_frag(lb2[0][0] + 41984, lb2[0][0] + 41984 + 256);
  e0[2] = ffma_s(e0[2], ca1[0], ca0[0]);
  e0[3] = ffma_s(e0[3], ca1[0], ca0[0]);
  e0[0] = fmul_s(e0[0], acc[0][0][0]);
  SCHED_FENCE();
  asm volatile("v_mfma_f32_32x32x16_bf16 %0, %1, a[36:39], %0" : "+v"(acc[2][1]) : "v"(wf[0]));
  SCHED_FENCE();
  e0[1] = fmul_s(e0[1], acc[0][0][1]);
  e0[2] = fmul_s(e0[2], acc[0][0][2]);
  e0[3] = fmul_s(e0[3], acc[0][0][3]);
  e0[0] = __builtin_amdgcn_exp2f(e0[0]);
  SCHED_FENCE();
  asm volatile("v_mfma_f32_32x32x16_bf16 %0, %1, a[40:43], %0" : "+v"(acc[2][0]) : "v"(wf[1]));
  SCHED_FENCE();
  wf[0] = tr_frag(lb2[0][0] + 45056, lb2[0][0] + 45056 + 256);
  e0[1] = __builtin_amdgcn_exp2f(e0[1]);
  e0[2] = __builtin_amdgcn_exp2f(e0[2]);
  e0[3] = __builtin_amdgcn_exp2f(e0[3]);
  SCHED_FENCE();
  asm volatile("v_mfma_f32_32x32x16_bf16 %0, %1, a[44:47], %0" : "+v"(acc[2][1]) : "v"(wf[1]));
  SCHED_FENCE();
  e1[0] = fmul_s(acc[0][0][0], acc[0][0][8]);
  e1[1] = fmul_s(acc[0][0][1], acc[0][0][9]);
  e1[2] = fmul_s(acc[0][0][2], acc[0][0][10]);
  SCHED_FENCE();
  asm volatile("v_mfma_f32_32x32x16_bf16 %0, %1, a[48:51], %0" : "+v"(acc[2][0]) : "v"(wf[2]));
  SCHED_FENCE();
  wf[1] = tr_frag(lb2[0][0] + 46080, lb2[0][0] + 46080 + 256);
  e1[3] = fmul_s(acc[0][0][3], acc[0][0][11]);
  e0[0] = ffma_s(e0[0], cv[0], cv[0]);
  e0[1] = ffma_s(e0[1], cv[0], cv[0]);
  e0[2] = ffma_s(e0[2], cv[0], cv[0]);
  SCHED_FENCE();
  asm volatile("v_mfma_f32_32x32x16_bf16 %0, %1, a[52:55], %0" : "+v"(acc[2][1]) : "v"(wf[2]));
  SCHED_FENCE();
  e0[3] = ffma_s(e0[3], cv[0], cv[0]);
  e0[0] = __builtin_amdgcn_rcpf(e0[0]);
  e0[1] = __builtin_amdgcn_rcpf(e0[1]);
  SCHED_FENCE();
  asm volatile("v_mfma_f32_32x32x16_bf16 %0, %1, a[56:59], %0" : "+v"(acc[2][0]) : "v"(wf[3]));
  SCHED_FENCE();
  wf[2] = tr_frag(lb2[0][1] + 40960, lb2[0][1] + 40960 + 256);
  e0[2] = __builtin_amdgcn_rcpf(e0[2]);
  e0[3] = __builtin_amdgcn_rcpf(e0[3]);
  e1[0] = fmul_s(e1[0], e0[0]);
  SCHED_FENCE();
  asm volatile("v_mfma_f32_32x32x16_bf16 %0, %1, a[60:63], %0" : "+v"(acc[2][1]) : "v"(wf[3]));
  SCHED_FENCE();
  e1[1] = fmul_s(e1[1], e0[1]);
  e1[2] = fmul_s(e1[2], e0[2]);
  e1[3] = fmul_s(e1[3], e0[3]);
  SCHED_FENCE();
  asm volatile("v_mfma_f32_32x32x16_bf16 %0, %1, a[64:67], %0" : "+v"(acc[2][0]) : "v"(wf[0]));
  SCHED_FENCE();
  wf[3] = tr_frag(lb2[0][1] + 41984, lb2[0][1] + 41984 + 256);
  pk[0] = pack_bf16(e1[0], e1[1]);
  pk[1] = pack_bf16(e1[2], e1[3]);
  e0[0] = fmul_s(acc[0][0][4], acc[0][0][4]);
  e0[1] = fmul_s(acc[0][0][5], acc[0][0][5]);
  SCHED_FENCE();
  asm volatile("v_mfma_f32_32x32x16_bf16 %0, %1, a[68:71], %0" : "+v"(acc[2][1]) : "v"(wf[0]));
  SCHED_FENCE();
  e0[2] = fmul_s(acc[0][0][6], acc[0][0][6]);
  e0[3] = fmul_s(acc[0][0][7], acc[0][0][7]);
  e0[0] = ffma_s(e0[0], ca1[0], ca0[0]);
  SCHED_FENCE();
  asm volatile("v_mfma_f32_32x32x16_bf16 %0, %1, a[72:75], %0" : "+v"(acc[2][0]) : "v"(wf[1]));
  SCHED_FENCE();
  wait_vm_barrier<0>();
  G1_SIGNALW();
  G1_SIGNALE();
  wf[0] = tr_frag(lb2[0][1] + 45056, lb2[0][1] + 45056 + 256);
  e0[1] = ffma_s(e0[1], ca1[0], ca0[0]);
  e0[2] = ffma_s(e0[2], ca1[0], ca0[0]);
  e0[3] = ffma_s(e0[3], ca1[0], ca0[0]);
  SCHED_FENCE();
  asm volatile("v_mfma_f32_32x32x16_bf16 %0, %1, a[76:79], %0" : "+v"(acc[2][1]) : "v"(wf[1]));
  SCHED_FENCE();
  e0[0] = fmul_s(e0[0], acc[0][0][4]);
  e0[1] = fmul_s(e0[1], acc[0][0][5]);
  e0[2] = fmul_s(e0[2], acc[0][0][6]);
  e0[3] = fmul_s(e0[3], acc[0][0][7]);
  SCHED_FENCE();
  asm volatile("v_mfma_f32_32x32x16_bf16 %0, %1, a[80:83], %0" : "+v"(acc[2][0]) : "v"(wf[2]));
  SCHED_FENCE();
  wf[1] = tr_frag(lb2[0][1] + 46080, lb2[0][1] + 46080 + 256);
  e0[0] = __builtin_amdgcn_exp2f(e0[0]);
  e0[1] = __builtin_amdgcn_exp2f(e0[1]);
  e0[2] = __builtin_amdgcn_exp2f(e0[2]);
  SCHED_FENCE();
  asm volatile("v_mfma_f32_32x32x16_bf16 %0, %1, a[84:87], %0" : "+v"(acc[2][1]) : "v"(wf[2]));
  SCHED_FENCE();
  e0[3] = __builtin_amdgcn_exp2f(e0[3]);
  e1[0] = fmul_s(acc[0][0][4], acc[0][0][12]);
  e1[1] = fmul_s(acc[0][0][5], acc[0][0][13]);
  SCHED_FENCE();
  asm volatile("v_mfma_f32_32x32x16_bf16 %0, %1, a[88:91], %0" : "+v"(acc[2][0]) : "v"(wf[3]));
  SCHED_FENCE();
  wf[2] = tr_frag(lb2[0][1] + 49152, lb2[0][1] + 49152 + 256);
  e1[2] = fmul_s(acc[0][0][6], acc[0][0][14]);
  e1[3] = fmul_s(acc[0][0][7], acc[0][0][15]);
  e0[0] = ffma_s(e0[0], cv[0], cv[0]);
  SCHED_FENCE();
  asm volatile("v_mfma_f32_32x32x16_bf16 %0, %1, a[92:95], %0" : "+v"(acc[2][1]) : "v"(wf[3]));
  SCHED_FENCE();
  e0[1] = ffma_s(e0[1], cv[0], cv[0]);
  e0[2] = ffma_s(e0[2], cv[0], cv[0]);
  e0[3] = ffma_s(e0[3], cv[0], cv[0]);
  e0[0] = __builtin_amdgcn_rcpf(e0[0]);
  SCHED_FENCE();
  asm volatile("v_mfma_f32_32x32x16_bf16 %0, %1, a[96:99], %0" : "+v"(acc[2][0]) : "v"(wf[0]));
  SCHED_FENCE();
  wf[3] = tr_frag(lb2[0][1] + 50176, lb2[0][1] + 50176 + 256);
  e0[1] = __builtin_amdgcn_rcpf(e0[1]);
  e0[2] = __builtin_amdgcn_rcpf(e0[2]);
  e0[3] = __builtin_amdgcn_rcpf(e0[3]);
  SCHED_FENCE();
  asm volatile("v_mfma_f32_32x32x16_bf16 %0, %1, a[100:103], %0" : "+v"(acc[2][1]) : "v"(wf[0]));
  SCHED_FENCE();
  e1[0] = fmul_s(e1[0], e0[0]);
  e1[1] = fmul_s(e1[1], e0[1]);
  e1[2] = fmul_s(e1[2], e0[2]);
  SCHED_FENCE();
  asm volatile("v_mfma_f32_32x32x16_bf16 %0, %1, a[104:107], %0" : "+v"(acc[2][0]) : "v"(wf[1]));
  SCHED_FENCE();
  wf[0] = tr_frag(lb2[0][1] + 53248, lb2[0][1] + 53248 + 256);
  e1[3] = fmul_s(e1[3], e0[3]);
  pk[2] = pack_bf16(e1[0], e1[1]);
  pk[3] = pack_bf16(e1[2], e1[3]);
  __builtin_amdgcn_raw_buffer_store_b128((u32x4){pk[0], pk[1], pk[2], pk[3]}, ract, vlin, sactB + 0, 16);
  SCHED_FENCE();
  asm volatile("v_mfma_f32_32x32x16_bf16 %0, %1, a[108:111], %0" : "+v"(acc[2][1]) : "v"(wf[1]));
  SCHED_FENCE();
  e0[0] = fmul_s(acc[0][1][0], acc[0][1][0]);
  e0[1] = fmul_s(acc[0][1][1], acc[0][1][1]);
  e0[2] = fmul_s(acc[0][1][2], acc[0][1][2]);
  SCHED_FENCE();
  asm volatile("v_mfma_f32_32x32x16_bf16 %0, %1, a[112:115], %0" : "+v"(acc[2][0]) : "v"(wf[2]));
  SCHED_FENCE();
  wf[1] = tr_frag(lb2[0][1] + 54272, lb2[0][1] + 54272 + 256);
  e0[3] = fmul_s(acc[0][1][3], acc[0][1][3]);
  e0[0] = ffma_s(e0[0], ca1[1], ca0[1]);
  e0[1] = ffma_s(e0[1], ca1[1], ca0[1]);
  SCHED_FENCE();
  asm volatile("v_mfma_f32_32x32x16_bf16 %0, %1, a[116:119], %0" : "+v"(acc[2][1]) : "v"(wf[2]));
  SCHED_FENCE();
  e0[2] = ffma_s(e0[2], ca1[1], ca0[1]);
  e0[3] = ffma_s(e0[3], ca1[1], ca0[1]);
  e0[0] = fmul_s(e0[0], acc[0][1][0]);
  e0[1] = fmul_s(e0[1], acc[0][1][1]);
  SCHED_FENCE();
  asm volatile("v_mfma_f32_32x32x16_bf16 %0, %1, a[120:123], %0" : "+v"(acc[2][0]) : "v"(wf[3]));
  SCHED_FENCE();
  wf[2] = tr_frag(lb2[0][2] + 0, lb2[0][2] + 0 + 256);
  e0[2] = fmul_s(e0[2], acc[0][1][2]);
  e0[3] = fmul_s(e0[3], acc[0][1][3]);
  e0[0] = __builtin_amdgcn_exp2f(e0[0]);
  SCHED_FENCE();
  asm volatile("v_mfma_f32_32x32x16_bf16 %0, %1, a[124:127], %0" : "+v"(acc[2][1]) : "v"(wf[3]));
  SCHED_FENCE();
  e0[1] = __builtin_amdgcn_exp2f(e0[1]);
  e0[2] = __builtin_amdgcn_exp2f(e0[2]);
  e0[3] = __builtin_amdgcn_exp2f(e0[3]);
  SCHED_FENCE();
  asm volatile("v_mfma_f32_32x32x16_bf16 %0, %1, a[128:131], %0" : "+v"(acc[2][0]) : "v"(wf[0]));
  SCHED_FENCE();
  wf[3] = tr_frag(lb2[0][2] + 1024, lb2[0][2] + 1024 + 256);
  e1[0] = fmul_s(acc[0][1][0], acc[0][1][8]);
  e1[1] = fmul_s(acc[0][1][1], acc[0][1][9]);
  e1[2] = fmul_s(acc[0][1][2], acc[0][1][10]);
  SCHED_FENCE();
  asm volatile("v_mfma_f32_32x32x16_bf16 %0, %1, a[132:135], %0" : "+v"(acc[2][1]) : "v"(wf[0]));
  SCHED_FENCE();
  e1[3] = fmul_s(acc[0][1][3], acc[0][1][11]);
  e0[0] = ffma_s(e0[0], cv[1], cv[1]);
  e0[1] = ffma_s(e0[1], cv[1], cv[1]);
  e0[2] = ffma_s(e0[2], cv[1], cv[1]);
  SCHED_FENCE();
  asm volatile("v_mfma_f32_32x32x16_bf16 %0, %1, a[136:139], %0" : "+v"(acc[2][0]) : "v"(wf[1]));
  SCHED_FENCE();
  wf[0] = tr_frag(lb2[0][2] + 4096, lb2[0][2] + 4096 + 256);
  e0[3] = ffma_s(e0[3], cv[1], cv[1]);
  e0[0] = __builtin_amdgcn_rcpf(e0[0]);
  e0[1] = __builtin_amdgcn_rcpf(e0[1]);
  SCHED_FENCE();
  asm volatile("v_mfma_f32_32x32x16_bf16 %0, %1, a[140:143], %0" : "+v"(acc[2][1]) : "v"(wf[1]));
  SCHED_FENCE();
  e0[2] = __builtin_amdgcn_rcpf(e0[2]);
  e0[3] = __builtin_amdgcn_rcpf(e0[3]);
  e1[0] = fmul_s(e1[0], e0[0]);
  SCHED_FENCE();
  asm volatile("v_mfma_f32_32x32x16_bf16 %0, %1, a[144:147], %0" : "+v"(acc[2][0]) : "v"(wf[2]));
  SCHED_FENCE();
  wf[1] = tr_frag(lb2[0][2] + 5120, lb2[0][2] + 5120 + 256);
  e1[1] = fmul_s(e1[1], e0[1]);
  e1[2] = fmul_s(e1[2], e0[2]);
  e1[3] = fmul_s(e1[3], e0[3]);
  pk[0] = pack_bf16(e1[0], e1[1]);
  SCHED_FENCE();
  asm volatile("v_mfma_f32_32x32x16_bf16 %0, %1, a[148:151], %0" : "+v"(acc[2][1]) : "v"(wf[2]));
  SCHED_FENCE();
  pk[1] = pack_bf16(e1[2], e1[3]);
  e0[0] = fmul_s(acc[0][1][4], acc[0][1][4]);
  e0[1] = fmul_s(acc[0][1][5], acc[0][1][5]);
  SCHED_FENCE();
  asm volatile("v_mfma_f32_32x32x16_bf16 %0, %1, a[152:155], %0" : "+v"(acc[2][0]) : "v"(wf[3]));
  SCHED_FENCE();
  wf[2] = tr_frag(lb2[0][0] + 49152, lb2[0][0] + 49152 + 256);
  e0[2] = fmul_s(acc[0][1][6], acc[0][1][6]);
  e0[3] = fmul_s(acc[0][1][7], acc[0][1][7]);
  e0[0] = ffma_s(e0[0], ca1[1], ca0[1]);
  SCHED_FENCE();
  asm volatile("v_mfma_f32_32x32x16_bf16 %0, %1, a[156:159], %0" : "+v"(acc[2][1]) : "v"(wf[3]));
  SCHED_FENCE();
  e0[1] = ffma_s(e0[1], ca1[1], ca0[1]);
  e0[2] = ffma_s(e0[2], ca1[1], ca0[1]);
  e0[3] = ffma_s(e0[3], ca1[1], ca0[1]);
  SCHED_FENCE();
  asm volatile("v_mfma_f32_32x32x16_bf16 %0, %1, a[160:163], %0" : "+v"(acc[2][0]) : "v"(wf[0]));
  SCHED_FENCE();
  wf[3] = tr_frag(lb2[0][0] + 50176, lb2[0][0] + 50176 + 256);
  e0[0] = fmul_s(e0[0], acc[0][1][4]);
  e0[1] = fmul_s(e0[1], acc[0][1][5]);
  e0[2] = fmul_s(e0[2], acc[0][1][6]);
  e0[3] = fmul_s(e0[3], acc[0][1][7]);
  SCHED_FENCE();
  asm volatile("v_mfma_f32_32x32x16_bf16 %0, %1, a[164:167], %0" : "+v"(acc[2][1]) : "v"(wf[0]));
  SCHED_FENCE();
  e0[0] = __builtin_amdgcn_exp2f(e0[0]);
  e0[1] = __builtin_amdgcn_exp2f(e0[1]);
  e0[2] = __builtin_amdgcn_exp2f(e0[2]);
  SCHED_FENCE();
  asm volatile("v_mfma_f32_32x32x16_bf16 %0, %1, a[168:171], %0" : "+v"(acc[2][0]) : "v"(wf[1]));
  SCHED_FENCE();
  wait_vm_barrier<2>();
  wf[0] = tr_frag(lb2[0][2] + 8192, lb2[0][2] + 8192 + 256);
  e0[3] = __builtin_amdgcn_exp2f(e0[3]);
  e1[0] = fmul_s(acc[0][1][4], acc[0][1][12]);
  e1[1] = fmul_s(acc[0][1][5], acc[0][1][13]);
  SCHED_FENCE();
  asm volatile("v_mfma_f32_32x32x16_bf16 %0, %1, a[172:175], %0" : "+v"(acc[2][1]) : "v"(wf[1]));
  SCHED_FENCE();
  e1[2] = fmul_s(acc[0][1][6], acc[0][1][14]);
  e1[3] = fmul_s(acc[0][1][7], acc[0][1][15]);
  e0[0] = ffma_s(e0[0], cv[1], cv[1]);
  e0[1] = ffma_s(e0[1], cv[1], cv[1]);
  SCHED_FENCE();
  asm volatile("v_mfma_f32_32x32x16_bf16 %0, %1, a[176:179], %0" : "+v"(acc[2][0]) : "v"(wf[2]));
  SCHED_FENCE();
  wf[1] = tr_frag(lb2[0][2] + 9216, lb2[0][2] + 9216 + 256);
  e0[2] = ffma_s(e0[2], cv[1], cv[1]);
  e0[3] = ffma_s(e0[3], cv[1], cv[1]);
  e0[0] = __builtin_amdgcn_rcpf(e0[0]);
  SCHED_FENCE();
  asm volatile("v_mfma_f32_32x32x16_bf16 %0, %1, a[180:183], %0" : "+v"(acc[2][1]) : "v"(wf[2]));
  SCHED_FENCE();
  e0[1] = __builtin_amdgcn_rcpf(e0[1]);
  e0[2] = __builtin_amdgcn_rcpf(e0[2]);
  e0[3] = __builtin_amdgcn_rcpf(e0[3]);
  SCHED_FENCE();
  asm volatile("v_mfma_f32_32x32x16_bf16 %0, %1, a[184:187], %0" : "+v"(acc[2][0]) : "v"(wf[3]));
  SCHED_FENCE();
  wf[2] = tr_frag(lb2[0][2] + 12288, lb2[0][2] + 12288 + 256);
  e1[0] = fmul_s(e1[0], e0[0]);
  e1[1] = fmul_s(e1[1], e0[1]);
  e1[2] = fmul_s(e1[2], e0[2]);
  e1[3] = fmul_s(e1[3], e0[3]);
  SCHED_FENCE();
  asm volatile("v_mfma_f32_32x32x16_bf16 %0, %1, a[188:191], %0" : "+v"(acc[2][1]) : "v"(wf[3]));
  SCHED_FENCE();
  pk[2] = pack_bf16(e1[0], e1[1]);
  pk[3] = pack_bf16(e1[2], e1[3]);
  __builtin_amdgcn_raw_buffer_store_b128((u32x4){pk[0], pk[1], pk[2], pk[3]}, ract, vlin, sactB + 294912, 16);
  SCHED_FENCE();
  asm volatile("v_mfma_f32_32x32x16_bf16 %0, %1, a[192:195], %0" : "+v"(acc[2][0]) : "v"(wf[0]));
  SCHED_FENCE();
  wf[3] = tr_frag(lb2[0][2] + 13312, lb2[0][2] + 13312 + 256);
  e0[0] = fmul_s(acc[1][0][0], acc[1][0][0]);
  e0[1] = fmul_s(acc[1][0][1], acc[1][0][1]);
  e0[2] = fmul_s(acc[1][0][2], acc[1][0][2]);
  SCHED_FENCE();
  asm volatile("v_mfma_f32_32x32x16_bf16 %0, %1, a[196:199], %0" : "+v"(acc[2][1]) : "v"(wf[0]));
  SCHED_FENCE();
  e0[3] = fmul_s(acc[1][0][3], acc[1][0][3]);
  e0[0] = ffma_s(e0[0], ca1[0], ca0[0]);
  e0[1] = ffma_s(e0[1], ca1[0], ca0[0]);
  SCHED_FENCE();
  asm volatile("v_mfma_f32_32x32x16_bf16 %0, %1, a[200:203], %0" : "+v"(acc[2][0]) : "v"(wf[1]));
  SCHED_FENCE();
  wf[0] = tr_frag(lb2[0][0] + 53248, lb2[0][0] + 53248 + 256);
  e0[2] = ffma_s(e0[2], ca1[0], ca0[0]);
  e0[3] = ffma_s(e0[3], ca1[0], ca0[0]);
  e0[0] = fmul_s(e0[0], acc[1][0][0]);
  e0[1] = fmul_s(e0[1], acc[1][0][1]);
  SCHED_FENCE();
  asm volatile("v_mfma_f32_32x32x16_bf16 %0, %1, a[204:207], %0" : "+v"(acc[2][1]) : "v"(wf[1]));
  SCHED_FENCE();
  e0[2] = fmul_s(e0[2], acc[1][0][2]);
  e0[3] = fmul_s(e0[3], acc[1][0][3]);
  e0[0] = __builtin_amdgcn_exp2f(e0[0]);
  SCHED_FENCE();
  asm volatile("v_mfma_f32_32x32x16_bf16 %0, %1, a[208:211], %0" : "+v"(acc[2][0]) : "v"(wf[2]));
  SCHED_FENCE();
  wf[1] = tr_frag(lb2[0][0] + 54272, lb2[0][0] + 54272 + 256);
  e0[1] = __builtin_amdgcn_exp2f(e0[1]);
  e0[2] = __builtin_amdgcn_exp2f(e0[2]);
  e0[3] = __builtin_amdgcn_exp2f(e0[3]);
  SCHED_FENCE();
  asm volatile("v_mfma_f32_32x32x16_bf16 %0, %1, a[212:215], %0" : "+v"(acc[2][1]) : "v"(wf[2]));
  SCHED_FENCE();
  e1[0] = fmul_s(acc[1][0][0], acc[1][0][8]);
  e1[1] = fmul_s(acc[1][0][1], acc[1][0][9]);
  e1[2] = fmul_s(acc[1][0][2], acc[1][0][10]);
  e1[3] = fmul_s(acc[1][0][3], acc[1][0][11]);
  SCHED_FENCE();
  asm volatile("v_mfma_f32_32x32x16_bf16 %0, %1, a[216:219], %0" : "+v"(acc[2][0]) : "v"(wf[3]));
  SCHED_FENCE();
  wf[2] = tr_frag(lb2[0][1] + 0, lb2[0][1] + 0 + 256);
  e0[0] = ffma_s(e0[0], cv[0], cv[0]);
  e0[1] = ffma_s(e0[1], cv[0], cv[0]);
  e0[2] = ffma_s(e0[2], cv[0], cv[0]);
  SCHED_FENCE();
  asm volatile("v_mfma_f32_32x32x16_bf16 %0, %1, a[220:223], %0" : "+v"(acc[2][1]) : "v"(wf[3]));
  SCHED_FENCE();
  e0[3] = ffma_s(e0[3], cv[0], cv[0]);
  e0[0] = __builtin_amdgcn_rcpf(e0[0]);
  e0[1] = __builtin_amdgcn_rcpf(e0[1]);
  SCHED_FENCE();
  asm volatile("v_mfma_f32_32x32x16_bf16 %0, %1, a[224:227], %0" : "+v"(acc[2][0]) : "v"(wf[0]));
  SCHED_FENCE();
  wf[3] = tr_frag(lb2[0][1] + 1024, lb2[0][1] + 1024 + 256);
  e0[2] = __builtin_amdgcn_rcpf(e0[2]);
  e0[3] = __builtin_amdgcn_rcpf(e0[3]);
  e1[0] = fmul_s(e1[0], e0[0]);
  SCHED_FENCE();
  asm volatile("v_mfma_f32_32x32x16_bf16 %0, %1, a[228:231], %0" : "+v"(acc[2][1]) : "v"(wf[0]));
  SCHED_FENCE();
  e1[1] = fmul_s(e1[1], e0[1]);
  e1[2] = fmul_s(e1[2], e0[2]);
  e1[3] = fmul_s(e1[3], e0[3]);
  pk[0] = pack_bf16(e1[0], e1[1]);
  SCHED_FENCE();
  asm volatile("v_mfma_f32_32x32x16_bf16 %0, %1, a[232:235], %0" : "+v"(acc[2][0]) : "v"(wf[1]));
  SCHED_FENCE();
  wf[0] = tr_frag(lb2[0][1] + 4096, lb2[0][1] + 4096 + 256);
  pk[1] = pack_bf16(e1[2], e1[3]);
  e0[0] = fmul_s(acc[1][0][4], acc[1][0][4]);
  e0[1] = fmul_s(acc[1][0][5], acc[1][0][5]);
  SCHED_FENCE();
  asm volatile("v_mfma_f32_32x32x16_bf16 %0, %1, a[236:239], %0" : "+v"(acc[2][1]) : "v"(wf[1]));
  SCHED_FENCE();
  e0[2] = fmul_s(acc[1][0][6], acc[1][0][6]);
  e0[3] = fmul_s(acc[1][0][7], acc[1][0][7]);
  e0[0] = ffma_s(e0[0], ca1[0], ca0[0]);
  SCHED_FENCE();
  asm volatile("v_mfma_f32_32x32x16_bf16 %0, %1, a[240:243], %0" : "+v"(acc[2][0]) : "v"(wf[2]));
  SCHED_FENCE();
  wf[1] = tr_frag(lb2[0][1] + 5120, lb2[0][1] + 5120 + 256);
  e0[1] = ffma_s(e0[1], ca1[0], ca0[0]);
  e0[2] = ffma_s(e0[2], ca1[0], ca0[0]);
  e0[3] = ffma_s(e0[3], ca1[0], ca0[0]);
  e0[0] = fmul_s(e0[0], acc[1][0][4]);
  SCHED_FENCE();
  asm volatile("v_mfma_f32_32x32x16_bf16 %0, %1, a[244:247], %0" : "+v"(acc[2][1]) : "v"(wf[2]));
  SCHED_FENCE();
  e0[1] = fmul_s(e0[1], acc[1][0][5]);
  e0[2] = fmul_s(e0[2], acc[1][0][6]);
  e0[3] = fmul_s(e0[3], acc[1][0][7]);
  SCHED_FENCE();
  asm volatile("v_mfma_f32_32x32x16_bf16 %0, %1, a[248:251], %0" : "+v"(acc[2][0]) : "v"(wf[3]));
  SCHED_FENCE();
  wf[2] = tr_frag(lb2[0][1] + 8192, lb2[0][1] + 8192 + 256);
  e0[0] = __builtin_amdgcn_exp2f(e0[0]);
  e0[1] = __builtin_amdgcn_exp2f(e0[1]);
  e0[2] = __builtin_amdgcn_exp2f(e0[2]);
  SCHED_FENCE();
  asm volatile("v_mfma_f32_32x32x16_bf16 %0, %1, a[252:255], %0" : "+v"(acc[2][1]) : "v"(wf[3]));
  SCHED_FENCE();
  e0[3] = __builtin_amdgcn_exp2f(e0[3]);
  e1[0] = fmul_s(acc[1][0][4], acc[1][0][12]);
  e1[1] = fmul_s(acc[1][0][5], acc[1][0][13]);
  e1[2] = fmul_s(acc[1][0][6], acc[1][0][14]);
  SCHED_FENCE();
  asm volatile("v_mfma_f32_32x32x16_bf16 %0, %1, %2, %0" : "+v"(acc[2][0]) : "v"(wf[0]), "v"(tv[0][0]));
  SCHED_FENCE();
  wf[3] = tr_frag(lb2[0][1] + 9216, lb2[0][1] + 9216 + 256);
  e1[3] = fmul_s(acc[1][0][7], acc[1][0][15]);
  e0[0] = ffma_s(e0[0], cv[0], cv[0]);
  e0[1] = ffma_s(e0[1], cv[0], cv[0]);
  SCHED_FENCE();
  asm volatile("v_mfma_f32_32x32x16_bf16 %0, %1, %2, %0" : "+v"(acc[2][1]) : "v"(wf[0]), "v"(tv[1][0]));
  SCHED_FENCE();
  e0[2] = ffma_s(e0[2], cv[0], cv[0]);
  e0[3] = ffma_s(e0[3], cv[0], cv[0]);
  e0[0] = __builtin_amdgcn_rcpf(e0[0]);
  SCHED_FENCE();
  asm volatile("v_mfma_f32_32x32x16_bf16 %0, %1, %2, %0" : "+v"(acc[2][0]) : "v"(wf[1]), "v"(tv[0][1]));
  SCHED_FENCE();
  wf[0] = tr_frag(lb2[1][0] + 28672, lb2[1][0] + 28672 + 256);
  e0[1] = __builtin_amdgcn_rcpf(e0[1]);
  e0[2] = __builtin_amdgcn_rcpf(e0[2]);
  e0[3] = __builtin_amdgcn_rcpf(e0[3]);
  SCHED_FENCE();
  asm volatile("v_mfma_f32_32x32x16_bf16 %0, %1, %2, %0" : "+v"(acc[2][1]) : "v"(wf[1]), "v"(tv[1][1]));
  SCHED_FENCE();
  e1[0] = fmul_s(e1[0], e0[0]);
  e1[1] = fmul_s(e1[1], e0[1]);
  e1[2] = fmul_s(e1[2], e0[2]);
  e1[3] = fmul_s(e1[3], e0[3]);
  SCHED_FENCE();
  asm volatile("v_mfma_f32_32x32x16_bf16 %0, %1, %2, %0" : "+v"(acc[2][0]) : "v"(wf[2]), "v"(tv[0][2]));
  SCHED_FENCE();
  wf[1] = tr_frag(lb2[1][0] + 29696, lb2[1][0] + 29696 + 256);
  pk[2] = pack_bf16(e1[0], e1[1]);
  pk[3] = pack_bf16(e1[2], e1[3]);
  __builtin_amdgcn_raw_buffer_store_b128((u32x4){pk[0], pk[1], pk[2], pk[3]}, ract, vlin, sactB + 1024, 16);
  SCHED_FENCE();
  asm volatile("v_mfma_f32_32x32x16_bf16 %0, %1, %2, %0" : "+v"(acc[2][1]) : "v"(wf[2]), "v"(tv[1][2]));
  SCHED_FENCE();
  e0[0] = fmul_s(acc[1][1][0], acc[1][1][0]);
  e0[1] = fmul_s(acc[1][1][1], acc[1][1][1]);
  e0[2] = fmul_s(acc[1][1][2], acc[1][1][2]);
  SCHED_FENCE();
  asm volatile("v_mfma_f32_32x32x16_bf16 %0, %1, %2, %0" : "+v"(acc[2][0]) : "v"(wf[3]), "v"(tv[0][3]));
  SCHED_FENCE();
  wf[2] = tr_frag(lb2[1][0] + 32768, lb2[1][0] + 32768 + 256);
  e0[3] = fmul_s(acc[1][1][3], acc[1][1][3]);
  e0[0] = ffma_s(e0[0], ca1[1], ca0[1]);
  e0[1] = ffma_s(e0[1], ca1[1], ca0[1]);
  e0[2] = ffma_s(e0[2], ca1[1], ca0[1]);
  SCHED_FENCE();
  asm volatile("v_mfma_f32_32x32x16_bf16 %0, %1, %2, %0" : "+v"(acc[2][1]) : "v"(wf[3]), "v"(tv[1][3]));
  SCHED_FENCE();
  e0[3] = ffma_s(e0[3], ca1[1], ca0[1]);
  e0[0] = fmul_s(e0[0], acc[1][1][0]);
  e0[1] = fmul_s(e0[1], acc[1][1][1]);
  SCHED_FENCE();
  asm volatile("v_mfma_f32_32x32x16_bf16 %0, %1, a[0:3], 0" : "=&v"(acc[3][0]) : "v"(wf[0]));
  SCHED_FENCE();
  wf[3] = tr_frag(lb2[1][0] + 33792, lb2[1][0] + 33792 + 256);
  e0[2] = fmul_s(e0[2], acc[1][1][2]);
  e0[3] = fmul_s(e0[3], acc[1][1][3]);
  e0[0] = __builtin_amdgcn_exp2f(e0[0]);
  SCHED_FENCE();
  asm volatile("v_mfma_f32_32x32x16_bf16 %0, %1, a[4:7], 0" : "=&v"(acc[3][1]) : "v"(wf[0]));
  SCHED_FENCE();
  e0[1] = __builtin_amdgcn_exp2f(e0[1]);
  e0[2] = __builtin_amdgcn_exp2f(e0[2]);
  e0[3] = __builtin_amdgcn_exp2f(e0[3]);
  SCHED_FENCE();
  asm volatile("v_mfma_f32_32x32x16_bf16 %0, %1, a[8:11], %0" : "+v"(acc[3][0]) : "v"(wf[1]));
  SCHED_FENCE();
  wf[0] = tr_frag(lb2[1][0] + 36864, lb2[1][0] + 36864 + 256);
  e1[0] = fmul_s(acc[1][1][0], acc[1][1][8]);
  e1[1] = fmul_s(acc[1][1][1], acc[1][1][9]);
  e1[2] = fmul_s(acc[1][1][2], acc[1][1][10]);
  e1[3] = fmul_s(acc[1][1][3], acc[1][1][11]);
  SCHED_FENCE();
  asm volatile("v_mfma_f32_32x32x16_bf16 %0, %1, a[12:15], %0" : "+v"(acc[3][1]) : "v"(wf[1]));
  SCHED_FENCE();
  e0[0] = ffma_s(e0[0], cv[1], cv[1]);
  e0[1] = ffma_s(e0[1], cv[1], cv[1]);
  e0[2] = ffma_s(e0[2], cv[1], cv[1]);
  SCHED_FENCE();
  asm volatile("v_mfma_f32_32x32x16_bf16 %0, %1, a[16:19], %0" : "+v"(acc[3][0]) : "v"(wf[2]));
  SCHED_FENCE();
  wf[1] = tr_frag(lb2[1][0] + 37888, lb2[1][0] + 37888 + 256);
  e0[3] = ffma_s(e0[3], cv[1], cv[1]);
  e0[0] = __builtin_amdgcn_rcpf(e0[0]);
  e0[1] = __builtin_amdgcn_rcpf(e0[1]);
  SCHED_FENCE();
  asm volatile("v_mfma_f32_32x32x16_bf16 %0, %1, a[20:23], %0" : "+v"(acc[3][1]) : "v"(wf[2]));
  SCHED_FENCE();
  e0[2] = __builtin_amdgcn_rcpf(e0[2]);
  e0[3] = __builtin_amdgcn_rcpf(e0[3]);
  e1[0] = fmul_s(e1[0], e0[0]);
  e1[1] = fmul_s(e1[1], e0[1]);
  SCHED_FENCE();
  asm volatile("v_mfma_f32_32x32x16_bf16 %0, %1, a[24:27], %0" : "+v"(acc[3][0]) : "v"(wf[3]));
  SCHED_FENCE();
  wf[2] = tr_frag(lb2[1][0] + 40960, lb2[1][0] + 40960 + 256);
  e1[2] = fmul_s(e1[2], e0[2]);
  e1[3] = fmul_s(e1[3], e0[3]);
  pk[0] = pack_bf16(e1[0], e1[1]);
  SCHED_FENCE();
  asm volatile("v_mfma_f32_32x32x16_bf16 %0, %1, a[28:31], %0" : "+v"(acc[3][1]) : "v"(wf[3]));
  SCHED_FENCE();
  pk[1] = pack_bf16(e1[2], e1[3]);
  e0[0] = fmul_s(acc[1][1][4], acc[1][1][4]);
  e0[1] = fmul_s(acc[1][1][5], acc[1][1][5]);
  SCHED_FENCE();
  asm volatile("v_mfma_f32_32x32x16_bf16 %0, %1, a[32:35], %0" : "+v"(acc[3][0]) : "v"(wf[0]));
  SCHED_FENCE();
  wf[3] = tr_frag(lb2[1][0] + 41984, lb2[1][0] + 41984 + 256);
  e0[2] = fmul_s(acc[1][1][6], acc[1][1][6]);
  e0[3] = fmul_s(acc[1][1][7], acc[1][1][7]);
  e0[0] = ffma_s(e0[0], ca1[1], ca0[1]);
  SCHED_FENCE();
  asm volatile("v_mfma_f32_32x32x16_bf16 %0, %1, a[36:39], %0" : "+v"(acc[3][1]) : "v"(wf[0]));
  SCHED_FENCE();
  e0[1] = ffma_s(e0[1], ca1[1], ca0[1]);
  e0[2] = ffma_s(e0[2], ca1[1], ca0[1]);
  e0[3] = ffma_s(e0[3], ca1[1], ca0[1]);
  e0[0] = fmul_s(e0[0], acc[1][1][4]);
  SCHED_FENCE();
  asm volatile("v_mfma_f32_32x32x16_bf16 %0, %1, a[40:43], %0" : "+v"(acc[3][0]) : "v"(wf[1]));
  SCHED_FENCE();
  wf[0] = tr_frag(lb2[1][0] + 45056, lb2[1][0] + 45056 + 256);
  e0[1] = fmul_s(e0[1], acc[1][1][5]);
  e0[2] = fmul_s(e0[2], acc[1][1][6]);
  e0[3] = fmul_s(e0[3], acc[1][1][7]);
  SCHED_FENCE();
  asm volatile("v_mfma_f32_32x32x16_bf16 %0, %1, a[44:47], %0" : "+v"(acc[3][1]) : "v"(wf[1]));
  SCHED_FENCE();
  e0[0] = __builtin_amdgcn_exp2f(e0[0]);
  e0[1] = __builtin_amdgcn_exp2f(e0[1]);
  e0[2] = __builtin_amdgcn_exp2f(e0[2]);
  SCHED_FENCE();
  asm volatile("v_mfma_f32_32x32x16_bf16 %0, %1, a[48:51], %0" : "+v"(acc[3][0]) : "v"(wf[2]));
  SCHED_FENCE();
  wf[1] = tr_frag(lb2[1][0] + 46080, lb2[1][0] + 46080 + 256);
  e0[3] = __builtin_amdgcn_exp2f(e0[3]);
  e1[0] = fmul_s(acc[1][1][4], acc[1][1][12]);
  e1[1] = fmul_s(acc[1][1][5], acc[1][1][13]);
  e1[2] = fmul_s(acc[1][1][6], acc[1][1][14]);
  SCHED_FENCE();
  asm volatile("v_mfma_f32_32x32x16_bf16 %0, %1, a[52:55], %0" : "+v"(acc[3][1]) : "v"(wf[2]));
  SCHED_FENCE();
  e1[3] = fmul_s(acc[1][1][7], acc[1][1][15]);
  e0[0] = ffma_s(e0[0], cv[1], cv[1]);
  e0[1] = ffma_s(e0[1], cv[1], cv[1]);
  SCHED_FENCE();
  asm volatile("v_mfma_f32_32x32x16_bf16 %0, %1, a[56:59], %0" : "+v"(acc[3][0]) : "v"(wf[3]));
  SCHED_FENCE();
  wf[2] = tr_frag(lb2[1][1] + 40960, lb2[1][1] + 40960 + 256);
  e0[2] = ffma_s(e0[2], cv[1], cv[1]);
  e0[3] = ffma_s(e0[3], cv[1], cv[1]);
  e0[0] = __builtin_amdgcn_rcpf(e0[0]);
  SCHED_FENCE();
  asm volatile("v_mfma_f32_32x32x16_bf16 %0, %1, a[60:63], %0" : "+v"(acc[3][1]) : "v"(wf[3]));
  SCHED_FENCE();
  e0[1] = __builtin_amdgcn_rcpf(e0[1]);
  e0[2] = __builtin_amdgcn_rcpf(e0[2]);
  e0[3] = __builtin_amdgcn_rcpf(e0[3]);
  e1[0] = fmul_s(e1[0], e0[0]);
  SCHED_FENCE();
  asm volatile("v_mfma_f32_32x32x16_bf16 %0, %1, a[64:67], %0" : "+v"(acc[3][0]) : "v"(wf[0]));
  SCHED_FENCE();
  wf[3] = tr_frag(lb2[1][1] + 41984, lb2[1][1] + 41984 + 256);
  e1[1] = fmul_s(e1[1], e0[1]);
  e1[2] = fmul_s(e1[2], e0[2]);
  e1[3] = fmul_s(e1[3], e0[3]);
  SCHED_FENCE();
  asm volatile("v_mfma_f32_32x32x16_bf16 %0, %1, a[68:71], %0" : "+v"(acc[3][1]) : "v"(wf[0]));
  SCHED_FENCE();
  pk[2] = pack_bf16(e1[0], e1[1]);
  pk[3] = pack_bf16(e1[2], e1[3]);
  __builtin_amdgcn_raw_buffer_store_b128((u32x4){pk[0], pk[1], pk[2], pk[3]}, ract, vlin, sactB + 295936, 16);
  SCHED_FENCE();
  asm volatile("v_mfma_f32_32x32x16_bf16 %0, %1, a[72:75], %0" : "+v"(acc[3][0]) : "v"(wf[1]));
  SCHED_FENCE();
  wf[0] = tr_frag(lb2[1][1] + 45056, lb2[1][1] + 45056 + 256);
  e0[0] = fmul_s(acc[2][0][0], acc[2][0][0]);
  e0[1] = fmul_s(acc[2][0][1], acc[2][0][1]);
  e0[2] = fmul_s(acc[2][0][2], acc[2][0][2]);
  SCHED_FENCE();
  asm volatile("v_mfma_f32_32x32x16_bf16 %0, %1, a[76:79], %0" : "+v"(acc[3][1]) : "v"(wf[1]));
  SCHED_FENCE();
  e0[3] = fmul_s(acc[2][0][3], acc[2][0][3]);
  e0[0] = ffma_s(e0[0], ca1[0], ca0[0]);
  e0[1] = ffma_s(e0[1], ca1[0], ca0[0]);
  e0[2] = ffma_s(e0[2], ca1[0], ca0[0]);
  SCHED_FENCE();
  asm volatile("v_mfma_f32_32x32x16_bf16 %0, %1, a[80:83], %0" : "+v"(acc[3][0]) : "v"(wf[2]));
  SCHED_FENCE();
  wf[1] = tr_frag(lb2[1][1] + 46080, lb2[1][1] + 46080 + 256);
  e0[3] = ffma_s(e0[3], ca1[0], ca0[0]);
  e0[0] = fmul_s(e0[0], acc[2][0][0]);
  e0[1] = fmul_s(e0[1], acc[2][0][1]);
  SCHED_FENCE();
  asm volatile("v_mfma_f32_32x32x16_bf16 %0, %1, a[84:87], %0" : "+v"(acc[3][1]) : "v"(wf[2]));
  SCHED_FENCE();
  e0[2] = fmul_s(e0[2], acc[2][0][2]);
  e0[3] = fmul_s(e0[3], acc[2][0][3]);
  e0[0] = __builtin_amdgcn_exp2f(e0[0]);
  SCHED_FENCE();
  asm volatile("v_mfma_f32_32x32x16_bf16 %0, %1, a[88:91], %0" : "+v"(acc[3][0]) : "v"(wf[3]));
  SCHED_FENCE();
  wf[2] = tr_frag(lb2[1][1] + 49152, lb2[1][1] + 49152 + 256);
  e0[1] = __builtin_amdgcn_exp2f(e0[1]);
  e0[2] = __builtin_amdgcn_exp2f(e0[2]);
  e0[3] = __builtin_amdgcn_exp2f(e0[3]);
  e1[0] = fmul_s(acc[2][0][0], acc[2][0][8]);
  SCHED_FENCE();
  asm volatile("v_mfma_f32_32x32x16_bf16 %0, %1, a[92:95], %0" : "+v"(acc[3][1]) : "v"(wf[3]));
  SCHED_FENCE();
  e1[1] = fmul_s(acc[2][0][1], acc[2][0][9]);
  e1[2] = fmul_s(acc[2][0][2], acc[2][0][10]);
  e1[3] = fmul_s(acc[2][0][3], acc[2][0][11]);
  SCHED_FENCE();
  asm volatile("v_mfma_f32_32x32x16_bf16 %0, %1, a[96:99], %0" : "+v"(acc[3][0]) : "v"(wf[0]));
  SCHED_FENCE();
  wf[3] = tr_frag(lb2[1][1] + 50176, lb2[1][1] + 50176 + 256);
  e0[0] = ffma_s(e0[0], cv[0], cv[0]);
  e0[1] = ffma_s(e0[1], cv[0], cv[0]);
  e0[2] = ffma_s(e0[2], cv[0], cv[0]);
  SCHED_FENCE();
  asm volatile("v_mfma_f32_32x32x16_bf16 %0, %1, a[100:103], %0" : "+v"(acc[3][1]) : "v"(wf[0]));
  SCHED_FENCE();
  e0[3] = ffma_s(e0[3], cv[0], cv[0]);
  e0[0] = __builtin_amdgcn_rcpf(e0[0]);
  e0[1] = __builtin_amdgcn_rcpf(e0[1]);
  SCHED_FENCE();
  asm volatile("v_mfma_f32_32x32x16_bf16 %0, %1, a[104:107], %0" : "+v"(acc[3][0]) : "v"(wf[1]));
  SCHED_FENCE();
  wf[0] = tr_frag(lb2[1][1] + 53248, lb2[1][1] + 53248 + 256);
  e0[2] = __builtin_amdgcn_rcpf(e0[2]);
  e0[3] = __builtin_amdgcn_rcpf(e0[3]);
  e1[0] = fmul_s(e1[0], e0[0]);
  e1[1] = fmul_s(e1[1], e0[1]);
  SCHED_FENCE();
  asm volatile("v_mfma_f32_32x32x16_bf16 %0, %1, a[108:111], %0" : "+v"(acc[3][1]) : "v"(wf[1]));
  SCHED_FENCE();
  e1[2] = fmul_s(e1[2], e0[2]);
  e1[3] = fmul_s(e1[3], e0[3]);
  pk[0] = pack_bf16(e1[0], e1[1]);
  SCHED_FENCE();
  asm volatile("v_mfma_f32_32x32x16_bf16 %0, %1, a[112:115], %0" : "+v"(acc[3][0]) : "v"(wf[2]));
  SCHED_FENCE();
  wf[1] = tr_frag(lb2[1][1] + 54272, lb2[1][1] + 54272 + 256);
  pk[1] = pack_bf16(e1[2], e1[3]);
  e0[0] = fmul_s(acc[2][0][4], acc[2][0][4]);
  e0[1] = fmul_s(acc[2][0][5], acc[2][0][5]);
  SCHED_FENCE();
  asm volatile("v_mfma_f32_32x32x16_bf16 %0, %1, a[116:119], %0" : "+v"(acc[3][1]) : "v"(wf[2]));
  SCHED_FENCE();
  e0[2] = fmul_s(acc[2][0][6], acc[2][0][6]);
  e0[3] = fmul_s(acc[2][0][7], acc[2][0][7]);
  e0[0] = ffma_s(e0[0], ca1[0], ca0[0]);
  e0[1] = ffma_s(e0[1], ca1[0], ca0[0]);
  SCHED_FENCE();
  asm volatile("v_mfma_f32_32x32x16_bf16 %0, %1, a[120:123], %0" : "+v"(acc[3][0]) : "v"(wf[3]));
  SCHED_FENCE();
  wf[2] = tr_frag(lb2[1][2] + 0, lb2[1][2] + 0 + 256);
  e0[2] = ffma_s(e0[2], ca1[0], ca0[0]);
  e0[3] = ffma_s(e0[3], ca1[0], ca0[0]);
  e0[0] = fmul_s(e0[0], acc[2][0][4]);
  SCHED_FENCE();
  asm volatile("v_mfma_f32_32x32x16_bf16 %0, %1, a[124:127], %0" : "+v"(acc[3][1]) : "v"(wf[3]));
  SCHED_FENCE();
  e0[1] = fmul_s(e0[1], acc[2][0][5]);
  e0[2] = fmul_s(e0[2], acc[2][0][6]);
  e0[3] = fmul_s(e0[3], acc[2][0][7]);
  SCHED_FENCE();
  asm volatile("v_mfma_f32_32x32x16_bf16 %0, %1, a[128:131], %0" : "+v"(acc[3][0]) : "v"(wf[0]));
  SCHED_FENCE();
  wf[3] = tr_frag(lb2[1][2] + 1024, lb2[1][2] + 1024 + 256);
  e0[0] = __builtin_amdgcn_exp2f(e0[0]);
  e0[1] = __builtin_amdgcn_exp2f(e0[1]);
  e0[2] = __builtin_amdgcn_exp2f(e0[2]);
  e0[3] = __builtin_amdgcn_exp2f(e0[3]);
  G1_LOADF1();
  SCHED_FENCE();
  asm volatile("v_mfma_f32_32x32x16_bf16 %0, %1, a[132:135], %0" : "+v"(acc[3][1]) : "v"(wf[0]));
  SCHED_FENCE();
  e1[0] = fmul_s(acc[2][0][4], acc[2][0][12]);
  e1[1] = fmul_s(acc[2][0][5], acc[2][0][13]);
  e1[2] = fmul_s(acc[2][0][6], acc[2][0][14]);
  SCHED_FENCE();
  asm volatile("v_mfma_f32_32x32x16_bf16 %0, %1, a[136:139], %0" : "+v"(acc[3][0]) : "v"(wf[1]));
  SCHED_FENCE();
  wf[0] = tr_frag(lb2[1][2] + 4096, lb2[1][2] + 4096 + 256);
  e1[3] = fmul_s(acc[2][0][7], acc[2][0][15]);
  e0[0] = ffma_s(e0[0], cv[0], cv[0]);
  e0[1] = ffma_s(e0[1], cv[0], cv[0]);
  SCHED_FENCE();
  asm volatile("v_mfma_f32_32x32x16_bf16 %0, %1, a[140:143], %0" : "+v"(acc[3][1]) : "v"(wf[1]));
  SCHED_FENCE();
  e0[2] = ffma_s(e0[2], cv[0], cv[0]);
  e0[3] = ffma_s(e0[3], cv[0], cv[0]);
  e0[0] = __builtin_amdgcn_rcpf(e0[0]);
  SCHED_FENCE();
  asm volatile("v_mfma_f32_32x32x16_bf16 %0, %1, a[144:147], %0" : "+v"(acc[3][0]) : "v"(wf[2]));
  SCHED_FENCE();
  wf[1] = tr_frag(lb2[1][2] + 5120, lb2[1][2] + 5120 + 256);
  e0[1] = __builtin_amdgcn_rcpf(e0[1]);
  e0[2] = __builtin_amdgcn_rcpf(e0[2]);
  e0[3] = __builtin_amdgcn_rcpf(e0[3]);
  e1[0] = fmul_s(e1[0], e0[0]);
  SCHED_FENCE();
  asm volatile("v_mfma_f32_32x32x16_bf16 %0, %1, a[148:151], %0" : "+v"(acc[3][1]) : "v"(wf[2]));
  SCHED_FENCE();
  e1[1] = fmul_s(e1[1], e0[1]);
  e1[2] = fmul_s(e1[2], e0[2]);
  e1[3] = fmul_s(e1[3], e0[3]);
  SCHED_FENCE();
  asm volatile("v_mfma_f32_32x32x16_bf16 %0, %1, a[152:155], %0" : "+v"(acc[3][0]) : "v"(wf[3]));
  SCHED_FENCE();
  wf[2] = tr_frag(lb2[1][0] + 49152, lb2[1][0] + 49152 + 256);
  pk[2] = pack_bf16(e1[0], e1[1]);
  pk[3] = pack_bf16(e1[2], e1[3]);
  __builtin_amdgcn_raw_buffer_store_b128((u32x4){pk[0], pk[1], pk[2], pk[3]}, ract, vlin, sactB + 2048, 16);
  SCHED_FENCE();
  asm volatile("v_mfma_f32_32x32x16_bf16 %0, %1, a[156:159], %0" : "+v"(acc[3][1]) : "v"(wf[3]));
  SCHED_FENCE();
  e0[0] = fmul_s(acc[2][1][0], acc[2][1][0]);
  e0[1] = fmul_s(acc[2][1][1], acc[2][1][1]);
  e0[2] = fmul_s(acc[2][1][2], acc[2][1][2]);
  e0[3] = fmul_s(acc[2][1][3], acc[2][1][3]);
  SCHED_FENCE();
  asm volatile("v_mfma_f32_32x32x16_bf16 %0, %1, a[160:163], %0" : "+v"(acc[3][0]) : "v"(wf[0]));
  SCHED_FENCE();
  wf[3] = tr_frag(lb2[1][0] + 50176, lb2[1][0] + 50176 + 256);
  e0[0] = ffma_s(e0[0], ca1[1], ca0[1]);
  e0[1] = ffma_s(e0[1], ca1[1], ca0[1]);
  e0[2] = ffma_s(e0[2], ca1[1], ca0[1]);
  SCHED_FENCE();
  asm volatile("v_mfma_f32_32x32x16_bf16 %0, %1, a[164:167], %0" : "+v"(acc[3][1]) : "v"(wf[0]));
  SCHED_FENCE();
  e0[3] = ffma_s(e0[3], ca1[1], ca0[1]);
  e0[0] = fmul_s(e0[0], acc[2][1][0]);
  e0[1] = fmul_s(e0[1], acc[2][1][1]);
  SCHED_FENCE();
  asm volatile("v_mfma_f32_32x32x16_bf16 %0, %1, a[168:171], %0" : "+v"(acc[3][0]) : "v"(wf[1]));
  SCHED_FENCE();
  wf[0] = tr_frag(lb2[1][2] + 8192, lb2[1][2] + 8192 + 256);
  e0[2] = fmul_s(e0[2], acc[2][1][2]);
  e0[3] = fmul_s(e0[3], acc[2][1][3]);
  e0[0] = __builtin_amdgcn_exp2f(e0[0]);
  SCHED_FENCE();
  asm volatile("v_mfma_f32_32x32x16_bf16 %0, %1, a[172:175], %0" : "+v"(acc[3][1]) : "v"(wf[1]));
  SCHED_FENCE();
  e0[1] = __builtin_amdgcn_exp2f(e0[1]);
  e0[2] = __builtin_amdgcn_exp2f(e0[2]);
  e0[3] = __builtin_amdgcn_exp2f(e0[3]);
  e1[0] = fmul_s(acc[2][1][0], acc[2][1][8]);
  SCHED_FENCE();
  asm volatile("v_mfma_f32_32x32x16_bf16 %0, %1, a[176:179], %0" : "+v"(acc[3][0]) : "v"(wf[2]));
  SCHED_FENCE();
  wf[1] = tr_frag(lb2[1][2] + 9216, lb2[1][2] + 9216 + 256);
  e1[1] = fmul_s(acc[2][1][1], acc[2][1][9]);
  e1[2] = fmul_s(acc[2][1][2], acc[2][1][10]);
  e1[3] = fmul_s(acc[2][1][3], acc[2][1][11]);
  SCHED_FENCE();
  asm volatile("v_mfma_f32_32x32x16_bf16 %0, %1, a[180:183], %0" : "+v"(acc[3][1]) : "v"(wf[2]));
  SCHED_FENCE();
  e0[0] = ffma_s(e0[0], cv[1], cv[1]);
  e0[1] = ffma_s(e0[1], cv[1], cv[1]);
  e0[2] = ffma_s(e0[2], cv[1], cv[1]);
  SCHED_FENCE();
  asm volatile("v_mfma_f32_32x32x16_bf16 %0, %1, a[184:187], %0" : "+v"(acc[3][0]) : "v"(wf[3]));
  SCHED_FENCE();
  wf[2] = tr_frag(lb2[1][2] + 12288, lb2[1][2] + 12288 + 256);
  e0[3] = ffma_s(e0[3], cv[1], cv[1]);
  e0[0] = __builtin_amdgcn_rcpf(e0[0]);
  e0[1] = __builtin_amdgcn_rcpf(e0[1]);
  e0[2] = __builtin_amdgcn_rcpf(e0[2]);
  SCHED_FENCE();
  asm volatile("v_mfma_f32_32x32x16_bf16 %0, %1, a[188:191], %0" : "+v"(acc[3][1]) : "v"(wf[3]));
  SCHED_FENCE();
  e0[3] = __builtin_amdgcn_rcpf(e0[3]);
  e1[0] = fmul_s(e1[0], e0[0]);
  e1[1] = fmul_s(e1[1], e0[1]);
  SCHED_FENCE();
  asm volatile("v_mfma_f32_32x32x16_bf16 %0, %1, a[192:195], %0" : "+v"(acc[3][0]) : "v"(wf[0]));
  SCHED_FENCE();
  wf[3] = tr_frag(lb2[1][2] + 13312, lb2[1][2] + 13312 + 256);
  e1[2] = fmul_s(e1[2], e0[2]);
  e1[3] = fmul_s(e1[3], e0[3]);
  pk[0] = pack_bf16(e1[0], e1[1]);
  SCHED_FENCE();
  asm volatile("v_mfma_f32_32x32x16_bf16 %0, %1, a[196:199], %0" : "+v"(acc[3][1]) : "v"(wf[0]));
  SCHED_FENCE();
  pk[1] = pack_bf16(e1[2], e1[3]);
  e0[0] = fmul_s(acc[2][1][4], acc[2][1][4]);
  e0[1] = fmul_s(acc[2][1][5], acc[2][1][5]);
  e0[2] = fmul_s(acc[2][1][6], acc[2][1][6]);
  SCHED_FENCE();
  asm volatile("v_mfma_f32_32x32x16_bf16 %0, %1, a[200:203], %0" : "+v"(acc[3][0]) : "v"(wf[1]));
  SCHED_FENCE();
  wf[0] = tr_frag(lb2[1][0] + 53248, lb2[1][0] + 53248 + 256);
  e0[3] = fmul_s(acc[2][1][7], acc[2][1][7]);
  e0[0] = ffma_s(e0[0], ca1[1], ca0[1]);
  e0[1] = ffma_s(e0[1], ca1[1], ca0[1]);
  SCHED_FENCE();
  asm volatile("v_mfma_f32_32x32x16_bf16 %0, %1, a[204:207], %0" : "+v"(acc[3][1]) : "v"(wf[1]));
  SCHED_FENCE();
  e0[2] = ffma_s(e0[2], ca1[1], ca0[1]);
  e0[3] = ffma_s(e0[3], ca1[1], ca0[1]);
  e0[0] = fmul_s(e0[0], acc[2][1][4]);
  SCHED_FENCE();
  asm volatile("v_mfma_f32_32x32x16_bf16 %0, %1, a[208:211], %0" : "+v"(acc[3][0]) : "v"(wf[2]));
  SCHED_FENCE();
  wf[1] = tr_frag(lb2[1][0] + 54272, lb2[1][0] + 54272 + 256);
  e0[1] = fmul_s(e0[1], acc[2][1][5]);
  e0[2] = fmul_s(e0[2], acc[2][1][6]);
  e0[3] = fmul_s(e0[3], acc[2][1][7]);
  SCHED_FENCE();
  asm volatile("v_mfma_f32_32x32x16_bf16 %0, %1, a[212:215], %0" : "+v"(acc[3][1]) : "v"(wf[2]));
  SCHED_FENCE();
  e0[0] = __builtin_amdgcn_exp2f(e0[0]);
  e0[1] = __builtin_amdgcn_exp2f(e0[1]);
  e0[2] = __builtin_amdgcn_exp2f(e0[2]);
  e0[3] = __builtin_amdgcn_exp2f(e0[3]);
  SCHED_FENCE();
  asm volatile("v_mfma_f32_32x32x16_bf16 %0, %1, a[216:219], %0" : "+v"(acc[3][0]) : "v"(wf[3]));
  SCHED_FENCE();
  wf[2] = tr_frag(lb2[1][1] + 0, lb2[1][1] + 0 + 256);
  e1[0] = fmul_s(acc[2][1][4], acc[2][1][12]);
  e1[1] = fmul_s(acc[2][1][5], acc[2][1][13]);
  e1[2] = fmul_s(acc[2][1][6], acc[2][1][14]);
  SCHED_FENCE();
  asm volatile("v_mfma_f32_32x32x16_bf16 %0, %1, a[220:223], %0" : "+v"(acc[3][1]) : "v"(wf[3]));
  SCHED_FENCE();
  e1[3] = fmul_s(acc[2][1][7], acc[2][1][15]);
  e0[0] = ffma_s(e0[0], cv[1], cv[1]);
  e0[1] = ffma_s(e0[1], cv[1], cv[1]);
  SCHED_FENCE();
  asm volatile("v_mfma_f32_32x32x16_bf16 %0, %1, a[224:227], %0" : "+v"(acc[3][0]) : "v"(wf[0]));
  SCHED_FENCE();
  wf[3] = tr_frag(lb2[1][1] + 1024, lb2[1][1] + 1024 + 256);
  e0[2] = ffma_s(e0[2], cv[1], cv[1]);
  e0[3] = ffma_s(e0[3], cv[1], cv[1]);
  e0[0] = __builtin_amdgcn_rcpf(e0[0]);
  e0[1] = __builtin_amdgcn_rcpf(e0[1]);
  SCHED_FENCE();
  asm volatile("v_mfma_f32_32x32x16_bf16 %0, %1, a[228:231], %0" : "+v"(acc[3][1]) : "v"(wf[0]));
  SCHED_FENCE();
  e0[2] = __builtin_amdgcn_rcpf(e0[2]);
  e0[3] = __builtin_amdgcn_rcpf(e0[3]);
  e1[0] = fmul_s(e1[0], e0[0]);
  SCHED_FENCE();
  asm volatile("v_mfma_f32_32x32x16_bf16 %0, %1, a[232:235], %0" : "+v"(acc[3][0]) : "v"(wf[1]));
  SCHED_FENCE();
  wf[0] = tr_frag(lb2[1][1] + 4096, lb2[1][1] + 4096 + 256);
  e1[1] = fmul_s(e1[1], e0[1]);
  e1[2] = fmul_s(e1[2], e0[2]);
  e1[3] = fmul_s(e1[3], e0[3]);
  SCHED_FENCE();
  asm volatile("v_mfma_f32_32x32x16_bf16 %0, %1, a[236:239], %0" : "+v"(acc[3][1]) : "v"(wf[1]));
  SCHED_FENCE();
  pk[2] = pack_bf16(e1[0], e1[1]);
  pk[3] = pack_bf16(e1[2], e1[3]);
  __builtin_amdgcn_raw_buffer_store_b128((u32x4){pk[0], pk[1], pk[2], pk[3]}, ract, vlin, sactB + 296960, 16);
  SCHED_FENCE();
  asm volatile("v_mfma_f32_32x32x16_bf16 %0, %1, a[240:243], %0" : "+v"(acc[3][0]) : "v"(wf[2]));
  SCHED_FENCE();
  wf[1] = tr_frag(lb2[1][1] + 5120, lb2[1][1] + 5120 + 256);
  SCHED_FENCE();
  asm volatile("v_mfma_f32_32x32x16_bf16 %0, %1, a[244:247], %0" : "+v"(acc[3][1]) : "v"(wf[2]));
  SCHED_FENCE();
  asm volatile("v_mfma_f32_32x32x16_bf16 %0, %1, a[248:251], %0" : "+v"(acc[3][0]) : "v"(wf[3]));
  SCHED_FENCE();
  wf[2] = tr_frag(lb2[1][1] + 8192, lb2[1][1] + 8192 + 256);
  SCHED_FENCE();
  asm volatile("v_mfma_f32_32x32x16_bf16 %0, %1, a[252:255], %0" : "+v"(acc[3][1]) : "v"(wf[3]));
  SCHED_FENCE();
  asm volatile("v_mfma_f32_32x32x16_bf16 %0, %1, %2, %0" : "+v"(acc[3][0]) : "v"(wf[0]), "v"(tv[0][0]));
  SCHED_FENCE();
  wf[3] = tr_frag(lb2[1][1] + 9216, lb2[1][1] + 9216 + 256);
  SCHED_FENCE();
  asm volatile("v_mfma_f32_32x32x16_bf16 %0, %1, %2, %0" : "+v"(acc[3][1]) : "v"(wf[0]), "v"(tv[1][0]));
  SCHED_FENCE();
  asm volatile("v_mfma_f32_32x32x16_bf16 %0, %1, %2, %0" : "+v"(acc[3][0]) : "v"(wf[1]), "v"(tv[0][1]));
  SCHED_FENCE();
  asm volatile("v_mfma_f32_32x32x16_bf16 %0, %1, %2, %0" : "+v"(acc[3][1]) : "v"(wf[1]), "v"(tv[1][1]));
  SCHED_FENCE();
  asm volatile("v_mfma_f32_32x32x16_bf16 %0, %1, %2, %0" : "+v"(acc[3][0]) : "v"(wf[2]), "v"(tv[0][2]));
  SCHED_FENCE();
  asm volatile("v_mfma_f32_32x32x16_bf16 %0, %1, %2, %0" : "+v"(acc[3][1]) : "v"(wf[2]), "v"(tv[1][2]));
  SCHED_FENCE();
  asm volatile("v_mfma_f32_32x32x16_bf16 %0, %1, %2, %0" : "+v"(acc[3][0]) : "v"(wf[3]), "v"(tv[0][3]));
  SCHED_FENCE();
  asm volatile("v_mfma_f32_32x32x16_bf16 %0, %1, %2, %0" : "+v"(acc[3][1]) : "v"(wf[3]), "v"(tv[1][3]));
  SCHED_FENCE();
  if (__builtin_amdgcn_readfirstlane(f1a) < 64u) wait_count(cnt + (40 + ksl) * 64, 64u);
  if (__builtin_amdgcn_readfirstlane(f1b) < 8u) wait_count(cntE, 8u);
  asm volatile("s_barrier" ::: "memory");
  GLDS3(rw2, vlin, sWt[0], gk4[0], dW2, 0);
  asm volatile("s_nop 15\n\ts_nop 15" : "+v"(acc[3][0]), "+v"(acc[3][1]));
  e0[0] = fmul_s(acc[3][0][0], acc[3][0][0]);
  e0[1] = fmul_s(acc[3][0][1], acc[3][0][1]);
  e0[2] = fmul_s(acc[3][0][2], acc[3][0][2]);
  e0[3] = fmul_s(acc[3][0][3], acc[3][0][3]);
  e0[4] = fmul_s(acc[3][0][4], acc[3][0][4]);
  e0[5] = fmul_s(acc[3][0][5], acc[3][0][5]);
  SCHED_FENCE();
  GLDS3(rw2, vlin, sWt[1], gk4[0], dW2, 4096);
  e0[6] = fmul_s(acc[3][0][6], acc[3][0][6]);
  e0[7] = fmul_s(acc[3][0][7], acc[3][0][7]);
  e0[0] = ffma_s(e0[0], ca1[0], ca0[0]);
  e0[1] = ffma_s(e0[1], ca1[0], ca0[0]);
  e0[2] = ffma_s(e0[2], ca1[0], ca0[0]);
  e0[3] = ffma_s(e0[3], ca1[0], ca0[0]);
  e0[4] = ffma_s(e0[4], ca1[0], ca0[0]);
  SCHED_FENCE();
  GLDS3(rw2, vlin, sWt[2], gk4[0], dW2, 8192);
  e0[5] = ffma_s(e0[5], ca1[0], ca0[0]);
  e0[6] = ffma_s(e0[6], ca1[0], ca0[0]);
  e0[7] = ffma_s(e0[7], ca1[0], ca0[0]);
  e0[0] = fmul_s(e0[0], acc[3][0][0]);
  e0[1] = fmul_s(e0[1], acc[3][0][1]);
  e0[2] = fmul_s(e0[2], acc[3][0][2]);
  e0[3] = fmul_s(e0[3], acc[3][0][3]);
  SCHED_FENCE();
  GLDS3(rw2, vlin, sWt[3], gk4[0], dW2, 12288);
  e0[4] = fmul_s(e0[4], acc[3][0][4]);
  e0[5] = fmul_s(e0[5], acc[3][0][5]);
  e0[6] = fmul_s(e0[6], acc[3][0][6]);
  e0[7] = fmul_s(e0[7], acc[3][0][7]);
  e0[0] = __builtin_amdgcn_exp2f(e0[0]);
  e0[1] = __builtin_amdgcn_exp2f(e0[1]);
  e0[2] = __builtin_amdgcn_exp2f(e0[2]);
  SCHED_FENCE();
  GLDS3(rw2, vlin, sWt[4], gk4[0], dW2, 16384);
  e0[3] = __builtin_amdgcn_exp2f(e0[3]);
  e0[4] = __builtin_amdgcn_exp2f(e0[4]);
  e0[5] = __builtin_amdgcn_exp2f(e0[5]);
  e0[6] = __builtin_amdgcn_exp2f(e0[6]);
  e0[7] = __builtin_amdgcn_exp2f(e0[7]);
  e1[0] = fmul_s(acc[3][0][0], acc[3][0][8]);
  e1[1] = fmul_s(acc[3][0][1], acc[3][0][9]);
  SCHED_FENCE();
  GLDS3(rw2, vlin, sWt[5], gk4[0], dW2, 20480);
  e1[2] = fmul_s(acc[3][0][2], acc[3][0][10]);
  e1[3] = fmul_s(acc[3][0][3], acc[3][0][11]);
  e1[4] = fmul_s(acc[3][0][4], acc[3][0][12]);
  e1[5] = fmul_s(acc[3][0][5], acc[3][0][13]);
  e1[6] = fmul_s(acc[3][0][6], acc[3][0][14]);
  e1[7] = fmul_s(acc[3][0][7], acc[3][0][15]);
  e0[0] = ffma_s(e0[0], cv[0], cv[0]);
  SCHED_FENCE();
  GLDS3(rw2, vlin, sWt[6], gk4[0], dW2, 24576);
  e0[1] = ffma_s(e0[1], cv[0], cv[0]);
  e0[2] = ffma_s(e0[2], cv[0], cv[0]);
  e0[3] = ffma_s(e0[3], cv[0], cv[0]);
  e0[4] = ffma_s(e0[4], cv[0], cv[0]);
  e0[5] = ffma_s(e0[5], cv[0], cv[0]);
  e0[6] = ffma_s(e0[6], cv[0], cv[0]);
  e0[7] = ffma_s(e0[7], cv[0], cv[0]);
  SCHED_FENCE();
  asm volatile("buffer_load_dwordx4 a[0:3], %0, %1, %2 offen offset:0 sc1" :: "v"(va), "s"(ra), "s"(sa0 + 0) : "memory");
  e0[0] = __builtin_amdgcn_rcpf(e0[0]);
  e0[1] = __builtin_amdgcn_rcpf(e0[1]);
  e0[2] = __builtin_amdgcn_rcpf(e0[2]);
  e0[3] = __builtin_amdgcn_rcpf(e0[3]);
  e0[4] = __builtin_amdgcn_rcpf(e0[4]);
  e0[5] = __builtin_amdgcn_rcpf(e0[5]);
  e0[6] = __builtin_amdgcn_rcpf(e0[6]);
  SCHED_FENCE();
  asm volatile("buffer_load_dwordx4 a[4:7], %0, %1, %2 offen offset:256 sc1" :: "v"(va), "s"(ra), "s"(sa0 + 0) : "memory");
  e0[7] = __builtin_amdgcn_rcpf(e0[7]);
  e1[0] = fmul_s(e1[0], e0[0]);
  e1[1] = fmul_s(e1[1], e0[1]);
  e1[2] = fmul_s(e1[2], e0[2]);
  e1[3] = fmul_s(e1[3], e0[3]);
  e1[4] = fmul_s(e1[4], e0[4]);
  e1[5] = fmul_s(e1[5], e0[5]);
  SCHED_FENCE();
  asm volatile("buffer_load_dwordx4 a[8:11], %0, %1, %2 offen offset:2048 sc1" :: "v"(va), "s"(ra), "s"(sa0 + 0) : "memory");
  e1[6] = fmul_s(e1[6], e0[6]);
  e1[7] = fmul_s(e1[7], e0[7]);
  pk[0] = pack_bf16(e1[0], e1[1]);
  pk[1] = pack_bf16(e1[2], e1[3]);
  pk[2] = pack_bf16(e1[4], e1[5]);
  pk[3] = pack_bf16(e1[6], e1[7]);
  __builtin_amdgcn_raw_buffer_store_b128((u32x4){pk[0], pk[1], pk[2], pk[3]}, ract, vlin, sactB + 3072, 16);
  SCHED_FENCE();
  asm volatile("buffer_load_dwordx4 a[12:15], %0, %1, %2 offen offset:2304 sc1" :: "v"(va), "s"(ra), "s"(sa0 + 0) : "memory");
  e0[0] = fmul_s(acc[3][1][0], acc[3][1][0]);
  e0[1] = fmul_s(acc[3][1][1], acc[3][1][1]);
  e0[2] = fmul_s(acc[3][1][2], acc[3][1][2]);
  e0[3] = fmul_s(acc[3][1][3], acc[3][1][3]);
  e0[4] = fmul_s(acc[3][1][4], acc[3][1][4]);
  e0[5] = fmul_s(acc[3][1][5], acc[3][1][5]);
  e0[6] = fmul_s(acc[3][1][6], acc[3][1][6]);
  SCHED_FENCE();
  asm volatile("buffer_load_dwordx4 a[16:19], %0, %1, %2 offen offset:0 sc1" :: "v"(va), "s"(ra), "s"(sa0 + 8192) : "memory");
  e0[7] = fmul_s(acc[3][1][7], acc[3][1][7]);
  e0[0] = ffma_s(e0[0], ca1[1], ca0[1]);
  e0[1] = ffma_s(e0[1], ca1[1], ca0[1]);
  e0[2] = ffma_s(e0[2], ca1[1], ca0[1]);
  e0[3] = ffma_s(e0[3], ca1[1], ca0[1]);
  e0[4] = ffma_s(e0[4], ca1[1], ca0[1]);
  e0[5] = ffma_s(e0[5], ca1[1], ca0[1]);
  SCHED_FENCE();
  asm volatile("buffer_load_dwordx4 a[20:23], %0, %1, %2 offen offset:256 sc1" :: "v"(va), "s"(ra), "s"(sa0 + 8192) : "memory");
  e0[6] = ffma_s(e0[6], ca1[1], ca0[1]);
  e0[7] = ffma_s(e0[7], ca1[1], ca0[1]);
  e0[0] = fmul_s(e0[0], acc[3][1][0]);
  e0[1] = fmul_s(e0[1], acc[3][1][1]);
  e0[2] = fmul_s(e0[2], acc[3][1][2]);
  e0[3] = fmul_s(e0[3], acc[3][1][3]);
  e0[4] = fmul_s(e0[4], acc[3][1][4]);
  SCHED_FENCE();
  asm volatile("buffer_load_dwordx4 a[24:27], %0, %1, %2 offen offset:2048 sc1" :: "v"(va), "s"(ra), "s"(sa0 + 8192) : "memory");
  e0[5] = fmul_s(e0[5], acc[3][1][5]);
  e0[6] = fmul_s(e0[6], acc[3][1][6]);
  e0[7] = fmul_s(e0[7], acc[3][1][7]);
  e0[0] = __builtin_amdgcn_exp2f(e0[0]);
  e0[1] = __builtin_amdgcn_exp2f(e0[1]);
  e0[2] = __builtin_amdgcn_exp2f(e0[2]);
  e0[3] = __builtin_amdgcn_exp2f(e0[3]);
  SCHED_FENCE();
  asm volatile("buffer_load_dwordx4 a[28:31], %0, %1, %2 offen offset:2304 sc1" :: "v"(va), "s"(ra), "s"(sa0 + 8192) : "memory");
  e0[4] = __builtin_amdgcn_exp2f(e0[4]);
  e0[5] = __builtin_amdgcn_exp2f(e0[5]);
  e0[6] = __builtin_amdgcn_exp2f(e0[6]);
  e0[7] = __builtin_amdgcn_exp2f(e0[7]);
  e1[0] = fmul_s(acc[3][1][0], acc[3][1][8]);
  e1[1] = fmul_s(acc[3][1][1], acc[3][1][9]);
  e1[2] = fmul_s(acc[3][1][2], acc[3][1][10]);
  SCHED_FENCE();
  GLDS3(rw2, vlin, sWt[0], gk4[1], dW2, 28672);
  e1[3] = fmul_s(acc[3][1][3], acc[3][1][11]);
  e1[4] = fmul_s(acc[3][1][4], acc[3][1][12]);
  e1[5] = fmul_s(acc[3][1][5], acc[3][1][13]);
  e1[6] = fmul_s(acc[3][1][6], acc[3][1][14]);
  e1[7] = fmul_s(acc[3][1][7], acc[3][1][15]);
  e0[0] = ffma_s(e0[0], cv[1], cv[1]);
  e0[1] = ffma_s(e0[1], cv[1], cv[1]);
  SCHED_FENCE();
  GLDS3(rw2, vlin, sWt[1], gk4[1], dW2, 32768);
  e0[2] = ffma_s(e0[2], cv[1], cv[1]);
  e0[3] = ffma_s(e0[3], cv[1], cv[1]);
  e0[4] = ffma_s(e0[4], cv[1], cv[1]);
  e0[5] = ffma_s(e0[5], cv[1], cv[1]);
  e0[6] = ffma_s(e0[6], cv[1], cv[1]);
  e0[7] = ffma_s(e0[7], cv[1], cv[1]);
  e0[0] = __builtin_amdgcn_rcpf(e0[0]);
  SCHED_FENCE();
  GLDS3(rw2, vlin, sWt[2], gk4[1], dW2, 36864);
  e0[1] = __builtin_amdgcn_rcpf(e0[1]);
  e0[2] = __builtin_amdgcn_rcpf(e0[2]);
  e0[3] = __builtin_amdgcn_rcpf(e0[3]);
  e0[4] = __builtin_amdgcn_rcpf(e0[4]);
  e0[5] = __builtin_amdgcn_rcpf(e0[5]);
  e0[6] = __builtin_amdgcn_rcpf(e0[6]);
  e0[7] = __builtin_amdgcn_rcpf(e0[7]);
  SCHED_FENCE();
  GLDS3(rw2, vlin, sWt[3], gk4[1], dW2, 40960);
  e1[0] = fmul_s(e1[0], e0[0]);
  e1[1] = fmul_s(e1[1], e0[1]);
  e1[2] = fmul_s(e1[2], e0[2]);
  e1[3] = fmul_s(e1[3], e0[3]);
  e1[4] = fmul_s(e1[4], e0[4]);
  e1[5] = fmul_s(e1[5], e0[5]);
  e1[6] = fmul_s(e1[6], e0[6]);
  SCHED_FENCE();
  GLDS3(rw2, vlin, sWt[4], gk4[1], dW2, 45056);
  e1[7] = fmul_s(e1[7], e0[7]);
  pk[0] = pack_bf16(e1[0], e1[1]);
  pk[1] = pack_bf16(e1[2], e1[3]);
  pk[2] = pack_bf16(e1[4], e1[5]);
  pk[3] = pack_bf16(e1[6], e1[7]);
  __builtin_amdgcn_raw_buffer_store_b128((u32x4){pk[0], pk[1], pk[2], pk[3]}, ract, vlin, sactB + 297984, 16);
  SCHED_FENCE();
  GLDS3(rw2, vlin, sWt[5], gk4[1], dW2, 49152);
  SCHED_FENCE();
  GLDS3(rw2, vlin, sWt[6], gk4[1], dW2, 53248);
  SCHED_FENCE();
  asm volatile("buffer_load_dwordx4 a[32:35], %0, %1, %2 offen offset:0 sc1" :: "v"(va), "s"(ra), "s"(sa0 + 16384) : "memory");
  SCHED_FENCE();
  asm volatile("buffer_load_dwordx4 a[36:39], %0, %1, %2 offen offset:256 sc1" :: "v"(va), "s"(ra), "s"(sa0 + 16384) : "memory");
  SCHED_FENCE();
  asm volatile("buffer_load_dwordx4 a[40:43], %0, %1, %2 offen offset:2048 sc1" :: "v"(va), "s"(ra), "s"(sa0 + 16384) : "memory");
  SCHED_FENCE();
  asm volatile("buffer_load_dwordx4 a[44:47], %0, %1, %2 offen offset:2304 sc1" :: "v"(va), "s"(ra), "s"(sa0 + 16384) : "memory");
  SCHED_FENCE();
  asm volatile("buffer_load_dwordx4 a[48:51], %0, %1, %2 offen offset:0 sc1" :: "v"(va), "s"(ra), "s"(sa0 + 24576) : "memory");
  SCHED_FENCE();
  asm volatile("buffer_load_dwordx4 a[52:55], %0, %1, %2 offen offset:256 sc1" :: "v"(va), "s"(ra), "s"(sa0 + 24576) : "memory");
  SCHED_FENCE();
  asm volatile("buffer_load_dwordx4 a[56:59], %0, %1, %2 offen offset:2048 sc1" :: "v"(va), "s"(ra), "s"(sa0 + 24576) : "memory");
  SCHED_FENCE();
  asm volatile("buffer_load_dwordx4 a[60:63], %0, %1, %2 offen offset:2304 sc1" :: "v"(va), "s"(ra), "s"(sa0 + 24576) : "memory");
  SCHED_FENCE();
  }
  {
  const i32x4 rw = rw2;
  const unsigned dW = dW2;
  unsigned f2v;
#define G2_SIGNAL2() do { if (tid == 0) __hip_atomic_fetch_add((gu32*)cntB, 1u, __ATOMIC_RELAXED, __HIP_MEMORY_SCOPE_AGENT); } while (0)
#define G2_LOADF2() asm volatile("global_load_dword %0, %1, off sc1" : "=v"(f2v) : "v"(cntB) : "memory")
#define G2_CHECK2() do { asm volatile("" : "+v"(f2v)); if (__builtin_amdgcn_readfirstlane(f2v) < 8u) wait_count(cntB, 8u); } while (0)
  unsigned lb[3] = {lds0 + vlin, lds0 + 57344 + vlin, lds0 + 114688 + vlin};
  asm volatile("" : "+v"(lb[0]), "+v"(lb[1]), "+v"(lb[2]));
  const __amdgpu_buffer_rsrc_t rp = __builtin_amdgcn_make_buffer_rsrc((void*)part, 0, 1024 * 9216, 0x00020000);
  bf16x8 wf[12], tv[2][4];
  f32x4 acc[9][2];
  unsigned pk[4];
  const __amdgpu_buffer_rsrc_t ro = __builtin_amdgcn_make_buffer_rsrc((void*)out, 0, T_ * D_ * 2, 0x00020000);
  const __amdgpu_buffer_rsrc_t rm = __builtin_amdgcn_make_buffer_rsrc((void*)hmean, 0, T_ * 4, 0x00020000);
  u32x2 hv[3][2];
  unsigned mv[2];
  u32x4 rv[3][3];
  unsigned sR[3][3];
#pragma unroll
  for (int a = 0; a < 3; ++a)
#pragma unroll
    for (int i = 0; i < 3; ++i) {
      const unsigned base = ((grp * 4 + ksl) * 4 + ((ksl + 1 + i) & 3)) * 9216;
      sR[a][i] = (a < 2) ? base + (a * 4 + w) * 1024 : ((w == ksl) ? base + 8192 : 0x40000000u);
    }
#define G2_LOADP(a, i) rv[a][i] = __builtin_amdgcn_raw_buffer_load_b128(rp, vlin, sR[a][i], 16)
#define G2_LOADH(a, b) hv[a][b] = __builtin_amdgcn_raw_buffer_load_b64(rc, (g >> 1) * 512 + t16 * 16 + (g & 1) * 8, \
                                   ((mt2 * 4 + w) * 72 + nq * 18 + (a < 2 ? ksl * 4 + a * 2 : 16)) * 512 + b * 256, 16)
#define G2_LOADM(b) mv[b] = __builtin_amdgcn_raw_buffer_load_b32(rm, t16 * 4, (mt2 * 128 + w * 32 + b * 16) * 4, 16)
  wait_vm_barrier<10>();
  G2_SIGNAL2();
  wf[0] = lds_frag(lb[0] + 0);
  wf[1] = lds_frag(lb[0] + 4096);
  wf[2] = lds_frag(lb[0] + 8192);
  wf[3] = lds_frag(lb[0] + 12288);
  wf[4] = lds_frag(lb[0] + 16384);
  wf[5] = lds_frag(lb[0] + 20480);
  SCHED_FENCE();
  asm volatile("v_mfma_f32_16x16x32_bf16 %0, %1, a[0:3], 0" : "=&v"(acc[0][0]) : "v"(wf[0]));
  SCHED_FENCE();
  wf[6] = lds_frag(lb[0] + 24576);
  SCHED_FENCE();
  asm volatile("v_mfma_f32_16x16x32_bf16 %0, %1, a[4:7], 0" : "=&v"(acc[0][1]) : "v"(wf[0]));
  SCHED_FENCE();
  asm volatile("buffer_load_dwordx4 a[64:67], %0, %1, %2 offen offset:0 sc1" :: "v"(va), "s"(ra), "s"(sa0 + 32768) : "memory");
  SCHED_FENCE();
  asm volatile("v_mfma_f32_16x16x32_bf16 %0, %1, a[0:3], 0" : "=&v"(acc[1][0]) : "v"(wf[1]));
  SCHED_FENCE();
  wf[0] = lds_frag(lb[0] + 1024);
  SCHED_FENCE();
  asm volatile("v_mfma_f32_16x16x32_bf16 %0, %1, a[4:7], 0" : "=&v"(acc[1][1]) : "v"(wf[1]));
  SCHED_FENCE();
  asm volatile("buffer_load_dwordx4 a[68:71], %0, %1, %2 offen offset:256 sc1" :: "v"(va), "s"(ra), "s"(sa0 + 32768) : "memory");
  SCHED_FENCE();
  asm volatile("v_mfma_f32_16x16x32_bf16 %0, %1, a[0:3], 0" : "=&v"(acc[2][0]) : "v"(wf[2]));
  SCHED_FENCE();
  wf[1] = lds_frag(lb[0] + 5120);
  SCHED_FENCE();
  asm volatile("v_mfma_f32_16x16x32_bf16 %0, %1, a[4:7], 0" : "=&v"(acc[2][1]) : "v"(wf[2]));
  SCHED_FENCE();
  asm volatile("v_mfma_f32_16x16x32_bf16 %0, %1, a[0:3], 0" : "=&v"(acc[3][0]) : "v"(wf[3]));
  SCHED_FENCE();
  wf[2] = lds_frag(lb[0] + 9216);
  SCHED_FENCE();
  asm volatile("v_mfma_f32_16x16x32_bf16 %0, %1, a[4:7], 0" : "=&v"(acc[3][1]) : "v"(wf[3]));
  SCHED_FENCE();
  asm volatile("buffer_load_dwordx4 a[72:75], %0, %1, %2 offen offset:2048 sc1" :: "v"(va), "s"(ra), "s"(sa0 + 32768) : "memory");
  SCHED_FENCE();
  asm volatile("v_mfma_f32_16x16x32_bf16 %0, %1, a[0:3], 0" : "=&v"(acc[4][0]) : "v"(wf[4]));
  SCHED_FENCE();
  wf[3] = lds_frag(lb[0] + 13312);
  SCHED_FENCE();
  asm volatile("v_mfma_f32_16x16x32_bf16 %0, %1, a[4:7], 0" : "=&v"(acc[4][1]) : "v"(wf[4]));
  SCHED_FENCE();
  asm volatile("buffer_load_dwordx4 a[76:79], %0, %1, %2 offen offset:2304 sc1" :: "v"(va), "s"(ra), "s"(sa0 + 32768) : "memory");
  SCHED_FENCE();
  asm volatile("v_mfma_f32_16x16x32_bf16 %0, %1, a[0:3], 0" : "=&v"(acc[5][0]) : "v"(wf[5]));
  SCHED_FENCE();
  wf[4] = lds_frag(lb[0] + 17408);
  SCHED_FENCE();
  asm volatile("v_mfma_f32_16x16x32_bf16 %0, %1, a[4:7], 0" : "=&v"(acc[5][1]) : "v"(wf[5]));
  SCHED_FENCE();
  asm volatile("v_mfma_f32_16x16x32_bf16 %0, %1, a[0:3], 0" : "=&v"(acc[6][0]) : "v"(wf[6]));
  SCHED_FENCE();
  wf[5] = lds_frag(lb[0] + 21504);
  SCHED_FENCE();
  asm volatile("v_mfma_f32_16x16x32_bf16 %0, %1, a[4:7], 0" : "=&v"(acc[6][1]) : "v"(wf[6]));
  SCHED_FENCE();
  asm volatile("buffer_load_dwordx4 a[80:83], %0, %1, %2 offen offset:0 sc1" :: "v"(va), "s"(ra), "s"(sa0 + 36864) : "memory");
  SCHED_FENCE();
  asm volatile("v_mfma_f32_16x16x32_bf16 %0, %1, a[8:11], %0" : "+v"(acc[0][0]) : "v"(wf[0]));
  SCHED_FENCE();
  wf[6] = lds_frag(lb[0] + 25600);
  SCHED_FENCE();
  asm volatile("v_mfma_f32_16x16x32_bf16 %0, %1, a[12:15], %0" : "+v"(acc[0][1]) : "v"(wf[0]));
  SCHED_FENCE();
  asm volatile("buffer_load_dwordx4 a[84:87], %0, %1, %2 offen offset:256 sc1" :: "v"(va), "s"(ra), "s"(sa0 + 36864) : "memory");
  SCHED_FENCE();
  asm volatile("v_mfma_f32_16x16x32_bf16 %0, %1, a[8:11], %0" : "+v"(acc[1][0]) : "v"(wf[1]));
  SCHED_FENCE();
  wf[0] = lds_frag(lb[0] + 2048);
  SCHED_FENCE();
  asm volatile("v_mfma_f32_16x16x32_bf16 %0, %1, a[12:15], %0" : "+v"(acc[1][1]) : "v"(wf[1]));
  SCHED_FENCE();
  asm volatile("v_mfma_f32_16x16x32_bf16 %0, %1, a[8:11], %0" : "+v"(acc[2][0]) : "v"(wf[2]));
  SCHED_FENCE();
  wf[1] = lds_frag(lb[0] + 6144);
  SCHED_FENCE();
  asm volatile("v_mfma_f32_16x16x32_bf16 %0, %1, a[12:15], %0" : "+v"(acc[2][1]) : "v"(wf[2]));
  SCHED_FENCE();
  asm volatile("buffer_load_dwordx4 a[88:91], %0, %1, %2 offen offset:2048 sc1" :: "v"(va), "s"(ra), "s"(sa0 + 36864) : "memory");
  SCHED_FENCE();
  asm volatile("v_mfma_f32_16x16x32_bf16 %0, %1, a[8:11], %0" : "+v"(acc[3][0]) : "v"(wf[3]));
  SCHED_FENCE();
  wf[2] = lds_frag(lb[0] + 10240);
  SCHED_FENCE();
  asm volatile("v_mfma_f32_16x16x32_bf16 %0, %1, a[12:15], %0" : "+v"(acc[3][1]) : "v"(wf[3]));
  SCHED_FENCE();
  asm volatile("buffer_load_dwordx4 a[92:95], %0, %1, %2 offen offset:2304 sc1" :: "v"(va), "s"(ra), "s"(sa0 + 36864) : "memory");
  SCHED_FENCE();
  asm volatile("v_mfma_f32_16x16x32_bf16 %0, %1, a[8:11], %0" : "+v"(acc[4][0]) : "v"(wf[4]));
  SCHED_FENCE();
  wf[3] = lds_frag(lb[0] + 14336);
  SCHED_FENCE();
  asm volatile("v_mfma_f32_16x16x32_bf16 %0, %1, a[12:15], %0" : "+v"(acc[4][1]) : "v"(wf[4]));
  SCHED_FENCE();
  asm volatile("v_mfma_f32_16x16x32_bf16 %0, %1, a[8:11], %0" : "+v"(acc[5][0]) : "v"(wf[5]));
  SCHED_FENCE();
  wf[4] = lds_frag(lb[0] + 18432);
  SCHED_FENCE();
  asm volatile("v_mfma_f32_16x16x32_bf16 %0, %1, a[12:15], %0" : "+v"(acc[5][1]) : "v"(wf[5]));
  SCHED_FENCE();
  GLDS3(rw, vlin, sWt[0], gk4[2], dW, 57344);
  SCHED_FENCE();
  asm volatile("v_mfma_f32_16x16x32_bf16 %0, %1, a[8:11], %0" : "+v"(acc[6][0]) : "v"(wf[6]));
  SCHED_FENCE();
  wf[5] = lds_frag(lb[0] + 22528);
  SCHED_FENCE();
  asm volatile("v_mfma_f32_16x16x32_bf16 %0, %1, a[12:15], %0" : "+v"(acc[6][1]) : "v"(wf[6]));
  SCHED_FENCE();
  GLDS3(rw, vlin, sWt[1], gk4[2], dW, 61440);
  SCHED_FENCE();
  asm volatile("v_mfma_f32_16x16x32_bf16 %0, %1, a[16:19], %0" : "+v"(acc[0][0]) : "v"(wf[0]));
  SCHED_FENCE();
  wf[6] = lds_frag(lb[0] + 26624);
  SCHED_FENCE();
  asm volatile("v_mfma_f32_16x16x32_bf16 %0, %1, a[20:23], %0" : "+v"(acc[0][1]) : "v"(wf[0]));
  SCHED_FENCE();
  asm volatile("v_mfma_f32_16x16x32_bf16 %0, %1, a[16:19], %0" : "+v"(acc[1][0]) : "v"(wf[1]));
  SCHED_FENCE();
  wf[0] = lds_frag(lb[0] + 3072);
  SCHED_FENCE();
  asm volatile("v_mfma_f32_16x16x32_bf16 %0, %1, a[20:23], %0" : "+v"(acc[1][1]) : "v"(wf[1]));
  SCHED_FENCE();
  GLDS3(rw, vlin, sWt[2], gk4[2], dW, 65536);
  SCHED_FENCE();
  asm volatile("v_mfma_f32_16x16x32_bf16 %0, %1, a[16:19], %0" : "+v"(acc[2][0]) : "v"(wf[2]));
  SCHED_FENCE();
  wf[1] = lds_frag(lb[0] + 7168);
  SCHED_FENCE();
  asm volatile("v_mfma_f32_16x16x32_bf16 %0, %1, a[20:23], %0" : "+v"(acc[2][1]) : "v"(wf[2]));
  SCHED_FENCE();
  GLDS3(rw, vlin, sWt[3], gk4[2], dW, 69632);
  SCHED_FENCE();
  asm volatile("v_mfma_f32_16x16x32_bf16 %0, %1, a[16:19], %0" : "+v"(acc[3][0]) : "v"(wf[3]));
  SCHED_FENCE();
  wf[2] = lds_frag(lb[0] + 11264);
  SCHED_FENCE();
  asm volatile("v_mfma_f32_16x16x32_bf16 %0, %1, a[20:23], %0" : "+v"(acc[3][1]) : "v"(wf[3]));
  SCHED_FENCE();
  asm volatile("v_mfma_f32_16x16x32_bf16 %0, %1, a[16:19], %0" : "+v"(acc[4][0]) : "v"(wf[4]));
  SCHED_FENCE();
  wf[3] = lds_frag(lb[0] + 15360);
  SCHED_FENCE();
  asm volatile("v_mfma_f32_16x16x32_bf16 %0, %1, a[20:23], %0" : "+v"(acc[4][1]) : "v"(wf[4]));
  SCHED_FENCE();
  GLDS3(rw, vlin, sWt[4], gk4[2], dW, 73728);
  SCHED_FENCE();
  asm volatile("v_mfma_f32_16x16x32_bf16 %0, %1, a[16:19], %0" : "+v"(acc[5][0]) : "v"(wf[5]));
  SCHED_FENCE();
  wf[4] = lds_frag(lb[0] + 19456);
  SCHED_FENCE();
  asm volatile("v_mfma_f32_16x16x32_bf16 %0, %1, a[20:23], %0" : "+v"(acc[5][1]) : "v"(wf[5]));
  SCHED_FENCE();
  GLDS3(rw, vlin, sWt[5], gk4[2], dW, 77824);
  SCHED_FENCE();
  asm volatile("v_mfma_f32_16x16x32_bf16 %0, %1, a[16:19], %0" : "+v"(acc[6][0]) : "v"(wf[6]));
  SCHED_FENCE();
  wf[5] = lds_frag(lb[0] + 23552);
  SCHED_FENCE();
  asm volatile("v_mfma_f32_16x16x32_bf16 %0, %1, a[20:23], %0" : "+v"(acc[6][1]) : "v"(wf[6]));
  SCHED_FENCE();
  asm volatile("v_mfma_f32_16x16x32_bf16 %0, %1, a[24:27], %0" : "+v"(acc[0][0]) : "v"(wf[0]));
  SCHED_FENCE();
  wf[6] = lds_frag(lb[0] + 27648);
  SCHED_FENCE();
  asm volatile("v_mfma_f32_16x16x32_bf16 %0, %1, a[28:31], %0" : "+v"(acc[0][1]) : "v"(wf[0]));
  SCHED_FENCE();
  GLDS3(rw, vlin, sWt[6], gk4[2], dW, 81920);
  SCHED_FENCE();
  asm volatile("v_mfma_f32_16x16x32_bf16 %0, %1, a[24:27], %0" : "+v"(acc[1][0]) : "v"(wf[1]));
  SCHED_FENCE();
  wait_vm_barrier<15>();
  wf[0] = lds_frag(lb[0] + 28672);
  SCHED_FENCE();
  asm volatile("v_mfma_f32_16x16x32_bf16 %0, %1, a[28:31], %0" : "+v"(acc[1][1]) : "v"(wf[1]));
  SCHED_FENCE();
  asm volatile("buffer_load_dwordx4 a[96:99], %0, %1, %2 offen offset:0 sc1" :: "v"(va), "s"(ra), "s"(sa0 + 45056) : "memory");
  SCHED_FENCE();
  asm volatile("v_mfma_f32_16x16x32_bf16 %0, %1, a[24:27], %0" : "+v"(acc[2][0]) : "v"(wf[2]));
  SCHED_FENCE();
  wf[1] = lds_frag(lb[0] + 32768);
  SCHED_FENCE();
  asm volatile("v_mfma_f32_16x16x32_bf16 %0, %1, a[28:31], %0" : "+v"(acc[2][1]) : "v"(wf[2]));
  SCHED_FENCE();
  asm volatile("v_mfma_f32_16x16x32_bf16 %0, %1, a[24:27], %0" : "+v"(acc[3][0]) : "v"(wf[3]));
  SCHED_FENCE();
  wf[2] = lds_frag(lb[0] + 36864);
  SCHED_FENCE();
  asm volatile("v_mfma_f32_16x16x32_bf16 %0, %1, a[28:31], %0" : "+v"(acc[3][1]) : "v"(wf[3]));
  SCHED_FENCE();
  asm volatile("buffer_load_dwordx4 a[100:103], %0, %1, %2 offen offset:256 sc1" :: "v"(va), "s"(ra), "s"(sa0 + 45056) : "memory");
  SCHED_FENCE();
  asm volatile("v_mfma_f32_16x16x32_bf16 %0, %1, a[24:27], %0" : "+v"(acc[4][0]) : "v"(wf[4]));
  SCHED_FENCE();
  wf[3] = lds_frag(lb[0] + 40960);
  SCHED_FENCE();
  asm volatile("v_mfma_f32_16x16x32_bf16 %0, %1, a[28:31], %0" : "+v"(acc[4][1]) : "v"(wf[4]));
  SCHED_FENCE();
  asm volatile("buffer_load_dwordx4 a[104:107], %0, %1, %2 offen offset:2048 sc1" :: "v"(va), "s"(ra), "s"(sa0 + 45056) : "memory");
  SCHED_FENCE();
  asm volatile("v_mfma_f32_16x16x32_bf16 %0, %1, a[24:27], %0" : "+v"(acc[5][0]) : "v"(wf[5]));
  SCHED_FENCE();
  wf[4] = lds_frag(lb[0] + 45056);
  SCHED_FENCE();
  asm volatile("v_mfma_f32_16x16x32_bf16 %0, %1, a[28:31], %0" : "+v"(acc[5][1]) : "v"(wf[5]));
  SCHED_FENCE();
  asm volatile("v_mfma_f32_16x16x32_bf16 %0, %1, a[24:27], %0" : "+v"(acc[6][0]) : "v"(wf[6]));
  SCHED_FENCE();
  wf[5] = lds_frag(lb[0] + 49152);
  SCHED_FENCE();
  asm volatile("v_mfma_f32_16x16x32_bf16 %0, %1, a[28:31], %0" : "+v"(acc[6][1]) : "v"(wf[6]));
  SCHED_FENCE();
  asm volatile("buffer_load_dwordx4 a[108:111], %0, %1, %2 offen offset:2304 sc1" :: "v"(va), "s"(ra), "s"(sa0 + 45056) : "memory");
  SCHED_FENCE();
  asm volatile("v_mfma_f32_16x16x32_bf16 %0, %1, a[32:35], %0" : "+v"(acc[0][0]) : "v"(wf[0]));
  SCHED_FENCE();
  wf[6] = lds_frag(lb[0] + 53248);
  SCHED_FENCE();
  asm volatile("v_mfma_f32_16x16x32_bf16 %0, %1, a[36:39], %0" : "+v"(acc[0][1]) : "v"(wf[0]));
  SCHED_FENCE();
  asm volatile("buffer_load_dwordx4 a[112:115], %0, %1, %2 offen offset:0 sc1" :: "v"(va), "s"(ra), "s"(sa0 + 53248) : "memory");
  SCHED_FENCE();
  asm volatile("v_mfma_f32_16x16x32_bf16 %0, %1, a[32:35], %0" : "+v"(acc[1][0]) : "v"(wf[1]));
  SCHED_FENCE();
  wf[0] = lds_frag(lb[0] + 29696);
  SCHED_FENCE();
  asm volatile("v_mfma_f32_16x16x32_bf16 %0, %1, a[36:39], %0" : "+v"(acc[1][1]) : "v"(wf[1]));
  SCHED_FENCE();
  asm volatile("v_mfma_f32_16x16x32_bf16 %0, %1, a[32:35], %0" : "+v"(acc[2][0]) : "v"(wf[2]));
  SCHED_FENCE();
  wf[1] = lds_frag(lb[0] + 33792);
  SCHED_FENCE();
  asm volatile("v_mfma_f32_16x16x32_bf16 %0, %1, a[36:39], %0" : "+v"(acc[2][1]) : "v"(wf[2]));
  SCHED_FENCE();
  asm volatile("buffer_load_dwordx4 a[116:119], %0, %1, %2 offen offset:256 sc1" :: "v"(va), "s"(ra), "s"(sa0 + 53248) : "memory");
  SCHED_FENCE();
  asm volatile("v_mfma_f32_16x16x32_bf16 %0, %1, a[32:35], %0" : "+v"(acc[3][0]) : "v"(wf[3]));
  SCHED_FENCE();
  wf[2] = lds_frag(lb[0] + 37888);
  SCHED_FENCE();
  asm volatile("v_mfma_f32_16x16x32_bf16 %0, %1, a[36:39], %0" : "+v"(acc[3][1]) : "v"(wf[3]));
  SCHED_FENCE();
  asm volatile("buffer_load_dwordx4 a[120:123], %0, %1, %2 offen offset:2048 sc1" :: "v"(va), "s"(ra), "s"(sa0 + 53248) : "memory");
  SCHED_FENCE();
  asm volatile("v_mfma_f32_16x16x32_bf16 %0, %1, a[32:35], %0" : "+v"(acc[4][0]) : "v"(wf[4]));
  SCHED_FENCE();
  wf[3] = lds_frag(lb[0] + 41984);
  SCHED_FENCE();
  asm volatile("v_mfma_f32_16x16x32_bf16 %0, %1, a[36:39], %0" : "+v"(acc[4][1]) : "v"(wf[4]));
  SCHED_FENCE();
  asm volatile("v_mfma_f32_16x16x32_bf16 %0, %1, a[32:35], %0" : "+v"(acc[5][0]) : "v"(wf[5]));
  SCHED_FENCE();
  wf[4] = lds_frag(lb[0] + 46080);
  SCHED_FENCE();
  asm volatile("v_mfma_f32_16x16x32_bf16 %0, %1, a[36:39], %0" : "+v"(acc[5][1]) : "v"(wf[5]));
  SCHED_FENCE();
  asm volatile("buffer_load_dwordx4 a[124:127], %0, %1, %2 offen offset:2304 sc1" :: "v"(va), "s"(ra), "s"(sa0 + 53248) : "memory");
  SCHED_FENCE();
  asm volatile("v_mfma_f32_16x16x32_bf16 %0, %1, a[32:35], %0" : "+v"(acc[6][0]) : "v"(wf[6]));
  SCHED_FENCE();
  wf[5] = lds_frag(lb[0] + 50176);
  SCHED_FENCE();
  asm volatile("v_mfma_f32_16x16x32_bf16 %0, %1, a[36:39], %0" : "+v"(acc[6][1]) : "v"(wf[6]));
  SCHED_FENCE();
  GLDS3(rw, vlin, sWt[0], gk4[3], dW, 86016);
  SCHED_FENCE();
  asm volatile("v_mfma_f32_16x16x32_bf16 %0, %1, a[40:43], %0" : "+v"(acc[0][0]) : "v"(wf[0]));
  SCHED_FENCE();
  wf[6] = lds_frag(lb[0] + 54272);
  SCHED_FENCE();
  asm volatile("v_mfma_f32_16x16x32_bf16 %0, %1, a[44:47], %0" : "+v"(acc[0][1]) : "v"(wf[0]));
  SCHED_FENCE();
  asm volatile("v_mfma_f32_16x16x32_bf16 %0, %1, a[40:43], %0" : "+v"(acc[1][0]) : "v"(wf[1]));
  SCHED_FENCE();
  wf[0] = lds_frag(lb[0] + 30720);
  SCHED_FENCE();
  asm volatile("v_mfma_f32_16x16x32_bf16 %0, %1, a[44:47], %0" : "+v"(acc[1][1]) : "v"(wf[1]));
  SCHED_FENCE();
  GLDS3(rw, vlin, sWt[1], gk4[3], dW, 90112);
  SCHED_FENCE();
  asm volatile("v_mfma_f32_16x16x32_bf16 %0, %1, a[40:43], %0" : "+v"(acc[2][0]) : "v"(wf[2]));
  SCHED_FENCE();
  wf[1] = lds_frag(lb[0] + 34816);
  SCHED_FENCE();
  asm volatile("v_mfma_f32_16x16x32_bf16 %0, %1, a[44:47], %0" : "+v"(acc[2][1]) : "v"(wf[2]));
  SCHED_FENCE();
  GLDS3(rw, vlin, sWt[2], gk4[3], dW, 94208);
  SCHED_FENCE();
  asm volatile("v_mfma_f32_16x16x32_bf16 %0, %1, a[40:43], %0" : "+v"(acc[3][0]) : "v"(wf[3]));
  SCHED_FENCE();
  wf[2] = lds_frag(lb[0] + 38912);
  SCHED_FENCE();
  asm volatile("v_mfma_f32_16x16x32_bf16 %0, %1, a[44:47], %0" : "+v"(acc[3][1]) : "v"(wf[3]));
  SCHED_FENCE();
  asm volatile("v_mfma_f32_16x16x32_bf16 %0, %1, a[40:43], %0" : "+v"(acc[4][0]) : "v"(wf[4]));
  SCHED_FENCE();
  wf[3] = lds_frag(lb[0] + 43008);
  SCHED_FENCE();
  asm volatile("v_mfma_f32_16x16x32_bf16 %0, %1, a[44:47], %0" : "+v"(acc[4][1]) : "v"(wf[4]));
  SCHED_FENCE();
  GLDS3(rw, vlin, sWt[3], gk4[3], dW, 98304);
  SCHED_FENCE();
  asm volatile("v_mfma_f32_16x16x32_bf16 %0, %1, a[40:43], %0" : "+v"(acc[5][0]) : "v"(wf[5]));
  SCHED_FENCE();
  wf[4] = lds_frag(lb[0] + 47104);
  SCHED_FENCE();
  asm volatile("v_mfma_f32_16x16x32_bf16 %0, %1, a[44:47], %0" : "+v"(acc[5][1]) : "v"(wf[5]));
  SCHED_FENCE();
  GLDS3(rw, vlin, sWt[4], gk4[3], dW, 102400);
  SCHED_FENCE();
  asm volatile("v_mfma_f32_16x16x32_bf16 %0, %1, a[40:43], %0" : "+v"(acc[6][0]) : "v"(wf[6]));
  SCHED_FENCE();
  wf[5] = lds_frag(lb[0] + 51200);
  SCHED_FENCE();
  asm volatile("v_mfma_f32_16x16x32_bf16 %0, %1, a[44:47], %0" : "+v"(acc[6][1]) : "v"(wf[6]));
  SCHED_FENCE();
  asm volatile("v_mfma_f32_16x16x32_bf16 %0, %1, a[48:51], %0" : "+v"(acc[0][0]) : "v"(wf[0]));
  SCHED_FENCE();
  wf[6] = lds_frag(lb[0] + 55296);
  SCHED_FENCE();
  asm volatile("v_mfma_f32_16x16x32_bf16 %0, %1, a[52:55], %0" : "+v"(acc[0][1]) : "v"(wf[0]));
  SCHED_FENCE();
  GLDS3(rw, vlin, sWt[5], gk4[3], dW, 106496);
  SCHED_FENCE();
  asm volatile("v_mfma_f32_16x16x32_bf16 %0, %1, a[48:51], %0" : "+v"(acc[1][0]) : "v"(wf[1]));
  SCHED_FENCE();
  wf[0] = lds_frag(lb[0] + 31744);
  SCHED_FENCE();
  asm volatile("v_mfma_f32_16x16x32_bf16 %0, %1, a[52:55], %0" : "+v"(acc[1][1]) : "v"(wf[1]));
  SCHED_FENCE();
  GLDS3(rw, vlin, sWt[6], gk4[3], dW, 110592);
  SCHED_FENCE();
  asm volatile("v_mfma_f32_16x16x32_bf16 %0, %1, a[48:51], %0" : "+v"(acc[2][0]) : "v"(wf[2]));
  SCHED_FENCE();
  wf[1] = lds_frag(lb[0] + 35840);
  SCHED_FENCE();
  asm volatile("v_mfma_f32_16x16x32_bf16 %0, %1, a[52:55], %0" : "+v"(acc[2][1]) : "v"(wf[2]));
  SCHED_FENCE();
  asm volatile("v_mfma_f32_16x16x32_bf16 %0, %1, a[48:51], %0" : "+v"(acc[3][0]) : "v"(wf[3]));
  SCHED_FENCE();
  wf[2] = lds_frag(lb[0] + 39936);
  SCHED_FENCE();
  asm volatile("v_mfma_f32_16x16x32_bf16 %0, %1, a[52:55], %0" : "+v"(acc[3][1]) : "v"(wf[3]));
  SCHED_FENCE();
  asm volatile("buffer_load_dwordx4 a[128:131], %0, %1, %2 offen offset:0 sc1" :: "v"(va), "s"(ra), "s"(sa0 + 61440) : "memory");
  SCHED_FENCE();
  asm volatile("v_mfma_f32_16x16x32_bf16 %0, %1, a[48:51], %0" : "+v"(acc[4][0]) : "v"(wf[4]));
  SCHED_FENCE();
  wf[3] = lds_frag(lb[0] + 44032);
  SCHED_FENCE();
  asm volatile("v_mfma_f32_16x16x32_bf16 %0, %1, a[52:55], %0" : "+v"(acc[4][1]) : "v"(wf[4]));
  SCHED_FENCE();
  asm volatile("buffer_load_dwordx4 a[132:135], %0, %1, %2 offen offset:256 sc1" :: "v"(va), "s"(ra), "s"(sa0 + 61440) : "memory");
  SCHED_FENCE();
  asm volatile("v_mfma_f32_16x16x32_bf16 %0, %1, a[48:51], %0" : "+v"(acc[5][0]) : "v"(wf[5]));
  SCHED_FENCE();
  wf[4] = lds_frag(lb[0] + 48128);
  SCHED_FENCE();
  asm volatile("v_mfma_f32_16x16x32_bf16 %0, %1, a[52:55], %0" : "+v"(acc[5][1]) : "v"(wf[5]));
  SCHED_FENCE();
  asm volatile("v_mfma_f32_16x16x32_bf16 %0, %1, a[48:51], %0" : "+v"(acc[6][0]) : "v"(wf[6]));
  SCHED_FENCE();
  wf[5] = lds_frag(lb[0] + 52224);
  SCHED_FENCE();
  asm volatile("v_mfma_f32_16x16x32_bf16 %0, %1, a[52:55], %0" : "+v"(acc[6][1]) : "v"(wf[6]));
  SCHED_FENCE();
  asm volatile("buffer_load_dwordx4 a[136:139], %0, %1, %2 offen offset:2048 sc1" :: "v"(va), "s"(ra), "s"(sa0 + 61440) : "memory");
  SCHED_FENCE();
  asm volatile("v_mfma_f32_16x16x32_bf16 %0, %1, a[56:59], %0" : "+v"(acc[0][0]) : "v"(wf[0]));
  SCHED_FENCE();
  wf[6] = lds_frag(lb[0] + 56320);
  SCHED_FENCE();
  asm volatile("v_mfma_f32_16x16x32_bf16 %0, %1, a[60:63], %0" : "+v"(acc[0][1]) : "v"(wf[0]));
  SCHED_FENCE();
  asm volatile("buffer_load_dwordx4 a[140:143], %0, %1, %2 offen offset:2304 sc1" :: "v"(va), "s"(ra), "s"(sa0 + 61440) : "memory");
  SCHED_FENCE();
  asm volatile("v_mfma_f32_16x16x32_bf16 %0, %1, a[56:59], %0" : "+v"(acc[1][0]) : "v"(wf[1]));
  SCHED_FENCE();
  wait_vm_barrier<19>();
  wf[0] = lds_frag(lb[1] + 0);
  SCHED_FENCE();
  asm volatile("v_mfma_f32_16x16x32_bf16 %0, %1, a[60:63], %0" : "+v"(acc[1][1]) : "v"(wf[1]));
  SCHED_FENCE();
  asm volatile("v_mfma_f32_16x16x32_bf16 %0, %1, a[56:59], %0" : "+v"(acc[2][0]) : "v"(wf[2]));
  SCHED_FENCE();
  wf[1] = lds_frag(lb[1] + 4096);
  SCHED_FENCE();
  asm volatile("v_mfma_f32_16x16x32_bf16 %0, %1, a[60:63], %0" : "+v"(acc[2][1]) : "v"(wf[2]));
  SCHED_FENCE();
  asm volatile("buffer_load_dwordx4 a[144:147], %0, %1, %2 offen offset:0 sc1" :: "v"(va), "s"(ra), "s"(sa0 + 69632) : "memory");
  SCHED_FENCE();
  asm volatile("v_mfma_f32_16x16x32_bf16 %0, %1, a[56:59], %0" : "+v"(acc[3][0]) : "v"(wf[3]));
  SCHED_FENCE();
  wf[2] = lds_frag(lb[1] + 8192);
  SCHED_FENCE();
  asm volatile("v_mfma_f32_16x16x32_bf16 %0, %1, a[60:63], %0" : "+v"(acc[3][1]) : "v"(wf[3]));
  SCHED_FENCE();
  asm volatile("buffer_load_dwordx4 a[148:151], %0, %1, %2 offen offset:256 sc1" :: "v"(va), "s"(ra), "s"(sa0 + 69632) : "memory");
  SCHED_FENCE();
  asm volatile("v_mfma_f32_16x16x32_bf16 %0, %1, a[56:59], %0" : "+v"(acc[4][0]) : "v"(wf[4]));
  SCHED_FENCE();
  wf[3] = lds_frag(lb[1] + 12288);
  SCHED_FENCE();
  asm volatile("v_mfma_f32_16x16x32_bf16 %0, %1, a[60:63], %0" : "+v"(acc[4][1]) : "v"(wf[4]));
  SCHED_FENCE();
  asm volatile("v_mfma_f32_16x16x32_bf16 %0, %1, a[56:59], %0" : "+v"(acc[5][0]) : "v"(wf[5]));
  SCHED_FENCE();
  wf[4] = lds_frag(lb[1] + 16384);
  SCHED_FENCE();
  asm volatile("v_mfma_f32_16x16x32_bf16 %0, %1, a[60:63], %0" : "+v"(acc[5][1]) : "v"(wf[5]));
  SCHED_FENCE();
  asm volatile("buffer_load_dwordx4 a[152:155], %0, %1, %2 offen offset:2048 sc1" :: "v"(va), "s"(ra), "s"(sa0 + 69632) : "memory");
  SCHED_FENCE();
  asm volatile("v_mfma_f32_16x16x32_bf16 %0, %1, a[56:59], %0" : "+v"(acc[6][0]) : "v"(wf[6]));
  SCHED_FENCE();
  wf[5] = lds_frag(lb[1] + 20480);
  SCHED_FENCE();
  asm volatile("v_mfma_f32_16x16x32_bf16 %0, %1, a[60:63], %0" : "+v"(acc[6][1]) : "v"(wf[6]));
  SCHED_FENCE();
  asm volatile("buffer_load_dwordx4 a[156:159], %0, %1, %2 offen offset:2304 sc1" :: "v"(va), "s"(ra), "s"(sa0 + 69632) : "memory");
  SCHED_FENCE();
  asm volatile("v_mfma_f32_16x16x32_bf16 %0, %1, a[64:67], %0" : "+v"(acc[0][0]) : "v"(wf[0]));
  SCHED_FENCE();
  wf[6] = lds_frag(lb[1] + 24576);
  SCHED_FENCE();
  asm volatile("v_mfma_f32_16x16x32_bf16 %0, %1, a[68:71], %0" : "+v"(acc[0][1]) : "v"(wf[0]));
  SCHED_FENCE();
  asm volatile("v_mfma_f32_16x16x32_bf16 %0, %1, a[64:67], %0" : "+v"(acc[1][0]) : "v"(wf[1]));
  SCHED_FENCE();
  wf[0] = lds_frag(lb[1] + 1024);
  SCHED_FENCE();
  asm volatile("v_mfma_f32_16x16x32_bf16 %0, %1, a[68:71], %0" : "+v"(acc[1][1]) : "v"(wf[1]));
  SCHED_FENCE();
  GLDS3(rw, vlin, sWt[0], gk4[4], dW, 114688);
  SCHED_FENCE();
  asm volatile("v_mfma_f32_16x16x32_bf16 %0, %1, a[64:67], %0" : "+v"(acc[2][0]) : "v"(wf[2]));
  SCHED_FENCE();
  wf[1] = lds_frag(lb[1] + 5120);
  SCHED_FENCE();
  asm volatile("v_mfma_f32_16x16x32_bf16 %0, %1, a[68:71], %0" : "+v"(acc[2][1]) : "v"(wf[2]));
  SCHED_FENCE();
  GLDS3(rw, vlin, sWt[1], gk4[4], dW, 118784);
  SCHED_FENCE();
  asm volatile("v_mfma_f32_16x16x32_bf16 %0, %1, a[64:67], %0" : "+v"(acc[3][0]) : "v"(wf[3]));
  SCHED_FENCE();
  wf[2] = lds_frag(lb[1] + 9216);
  SCHED_FENCE();
  asm volatile("v_mfma_f32_16x16x32_bf16 %0, %1, a[68:71], %0" : "+v"(acc[3][1]) : "v"(wf[3]));
  SCHED_FENCE();
  asm volatile("v_mfma_f32_16x16x32_bf16 %0, %1, a[64:67], %0" : "+v"(acc[4][0]) : "v"(wf[4]));
  SCHED_FENCE();
  wf[3] = lds_frag(lb[1] + 13312);
  G2_LOADF2();
  SCHED_FENCE();
  asm volatile("v_mfma_f32_16x16x32_bf16 %0, %1, a[68:71], %0" : "+v"(acc[4][1]) : "v"(wf[4]));
  SCHED_FENCE();
  GLDS3(rw, vlin, sWt[2], gk4[4], dW, 122880);
  SCHED_FENCE();
  asm volatile("v_mfma_f32_16x16x32_bf16 %0, %1, a[64:67], %0" : "+v"(acc[5][0]) : "v"(wf[5]));
  SCHED_FENCE();
  wf[4] = lds_frag(lb[1] + 17408);
  SCHED_FENCE();
  asm volatile("v_mfma_f32_16x16x32_bf16 %0, %1, a[68:71], %0" : "+v"(acc[5][1]) : "v"(wf[5]));
  SCHED_FENCE();
  GLDS3(rw, vlin, sWt[3], gk4[4], dW, 126976);
  SCHED_FENCE();
  asm volatile("v_mfma_f32_16x16x32_bf16 %0, %1, a[64:67], %0" : "+v"(acc[6][0]) : "v"(wf[6]));
  SCHED_FENCE();
  wf[5] = lds_frag(lb[1] + 21504);
  SCHED_FENCE();
  asm volatile("v_mfma_f32_16x16x32_bf16 %0, %1, a[68:71], %0" : "+v"(acc[6][1]) : "v"(wf[6]));
  SCHED_FENCE();
  asm volatile("v_mfma_f32_16x16x32_bf16 %0, %1, a[72:75], %0" : "+v"(acc[0][0]) : "v"(wf[0]));
  SCHED_FENCE();
  wf[6] = lds_frag(lb[1] + 25600);
  SCHED_FENCE();
  asm volatile("v_mfma_f32_16x16x32_bf16 %0, %1, a[76:79], %0" : "+v"(acc[0][1]) : "v"(wf[0]));
  SCHED_FENCE();
  GLDS3(rw, vlin, sWt[4], gk4[4], dW, 131072);
  SCHED_FENCE();
  asm volatile("v_mfma_f32_16x16x32_bf16 %0, %1, a[72:75], %0" : "+v"(acc[1][0]) : "v"(wf[1]));
  SCHED_FENCE();
  wf[0] = lds_frag(lb[1] + 2048);
  SCHED_FENCE();
  asm volatile("v_mfma_f32_16x16x32_bf16 %0, %1, a[76:79], %0" : "+v"(acc[1][1]) : "v"(wf[1]));
  SCHED_FENCE();
  GLDS3(rw, vlin, sWt[5], gk4[4], dW, 135168);
  SCHED_FENCE();
  asm volatile("v_mfma_f32_16x16x32_bf16 %0, %1, a[72:75], %0" : "+v"(acc[2][0]) : "v"(wf[2]));
  SCHED_FENCE();
  wf[1] = lds_frag(lb[1] + 6144);
  SCHED_FENCE();
  asm volatile("v_mfma_f32_16x16x32_bf16 %0, %1, a[76:79], %0" : "+v"(acc[2][1]) : "v"(wf[2]));
  SCHED_FENCE();
  asm volatile("v_mfma_f32_16x16x32_bf16 %0, %1, a[72:75], %0" : "+v"(acc[3][0]) : "v"(wf[3]));
  SCHED_FENCE();
  wf[2] = lds_frag(lb[1] + 10240);
  SCHED_FENCE();
  asm volatile("v_mfma_f32_16x16x32_bf16 %0, %1, a[76:79], %0" : "+v"(acc[3][1]) : "v"(wf[3]));
  SCHED_FENCE();
  GLDS3(rw, vlin, sWt[6], gk4[4], dW, 139264);
  SCHED_FENCE();
  asm volatile("v_mfma_f32_16x16x32_bf16 %0, %1, a[72:75], %0" : "+v"(acc[4][0]) : "v"(wf[4]));
  SCHED_FENCE();
  wf[3] = lds_frag(lb[1] + 14336);
  SCHED_FENCE();
  asm volatile("v_mfma_f32_16x16x32_bf16 %0, %1, a[76:79], %0" : "+v"(acc[4][1]) : "v"(wf[4]));
  SCHED_FENCE();
  GLDS3(rw, vlin, sWt[0], gk4[5], dW, 143360);
  SCHED_FENCE();
  asm volatile("v_mfma_f32_16x16x32_bf16 %0, %1, a[72:75], %0" : "+v"(acc[5][0]) : "v"(wf[5]));
  SCHED_FENCE();
  wf[4] = lds_frag(lb[1] + 18432);
  SCHED_FENCE();
  asm volatile("v_mfma_f32_16x16x32_bf16 %0, %1, a[76:79], %0" : "+v"(acc[5][1]) : "v"(wf[5]));
  SCHED_FENCE();
  asm volatile("v_mfma_f32_16x16x32_bf16 %0, %1, a[72:75], %0" : "+v"(acc[6][0]) : "v"(wf[6]));
  SCHED_FENCE();
  wf[5] = lds_frag(lb[1] + 22528);
  SCHED_FENCE();
  asm volatile("v_mfma_f32_16x16x32_bf16 %0, %1, a[76:79], %0" : "+v"(acc[6][1]) : "v"(wf[6]));
  SCHED_FENCE();
  GLDS3(rw, vlin, sWt[1], gk4[5], dW, 147456);
  SCHED_FENCE();
  asm volatile("v_mfma_f32_16x16x32_bf16 %0, %1, a[80:83], %0" : "+v"(acc[0][0]) : "v"(wf[0]));
  SCHED_FENCE();
  wf[6] = lds_frag(lb[1] + 26624);
  SCHED_FENCE();
  asm volatile("v_mfma_f32_16x16x32_bf16 %0, %1, a[84:87], %0" : "+v"(acc[0][1]) : "v"(wf[0]));
  SCHED_FENCE();
  GLDS3(rw, vlin, sWt[2], gk4[5], dW, 151552);
  SCHED_FENCE();
  asm volatile("v_mfma_f32_16x16x32_bf16 %0, %1, a[80:83], %0" : "+v"(acc[1][0]) : "v"(wf[1]));
  SCHED_FENCE();
  wf[0] = lds_frag(lb[1] + 3072);
  SCHED_FENCE();
  asm volatile("v_mfma_f32_16x16x32_bf16 %0, %1, a[84:87], %0" : "+v"(acc[1][1]) : "v"(wf[1]));
  SCHED_FENCE();
  asm volatile("v_mfma_f32_16x16x32_bf16 %0, %1, a[80:83], %0" : "+v"(acc[2][0]) : "v"(wf[2]));
  SCHED_FENCE();
  wf[1] = lds_frag(lb[1] + 7168);
  SCHED_FENCE();
  asm volatile("v_mfma_f32_16x16x32_bf16 %0, %1, a[84:87], %0" : "+v"(acc[2][1]) : "v"(wf[2]));
  SCHED_FENCE();
  GLDS3(rw, vlin, sWt[3], gk4[5], dW, 155648);
  SCHED_FENCE();
  asm volatile("v_mfma_f32_16x16x32_bf16 %0, %1, a[80:83], %0" : "+v"(acc[3][0]) : "v"(wf[3]));
  SCHED_FENCE();
  wf[2] = lds_frag(lb[1] + 11264);
  SCHED_FENCE();
  asm volatile("v_mfma_f32_16x16x32_bf16 %0, %1, a[84:87], %0" : "+v"(acc[3][1]) : "v"(wf[3]));
  SCHED_FENCE();
  GLDS3(rw, vlin, sWt[4], gk4[5], dW, 159744);
  SCHED_FENCE();
  asm volatile("v_mfma_f32_16x16x32_bf16 %0, %1, a[80:83], %0" : "+v"(acc[4][0]) : "v"(wf[4]));
  SCHED_FENCE();
  wf[3] = lds_frag(lb[1] + 15360);
  SCHED_FENCE();
  asm volatile("v_mfma_f32_16x16x32_bf16 %0, %1, a[84:87], %0" : "+v"(acc[4][1]) : "v"(wf[4]));
  SCHED_FENCE();
  asm volatile("v_mfma_f32_16x16x32_bf16 %0, %1, a[80:83], %0" : "+v"(acc[5][0]) : "v"(wf[5]));
  SCHED_FENCE();
  wf[4] = lds_frag(lb[1] + 19456);
  SCHED_FENCE();
  asm volatile("v_mfma_f32_16x16x32_bf16 %0, %1, a[84:87], %0" : "+v"(acc[5][1]) : "v"(wf[5]));
  SCHED_FENCE();
  GLDS3(rw, vlin, sWt[5], gk4[5], dW, 0);
  SCHED_FENCE();
  asm volatile("v_mfma_f32_16x16x32_bf16 %0, %1, a[80:83], %0" : "+v"(acc[6][0]) : "v"(wf[6]));
  SCHED_FENCE();
  wf[5] = lds_frag(lb[1] + 23552);
  SCHED_FENCE();
  asm volatile("v_mfma_f32_16x16x32_bf16 %0, %1, a[84:87], %0" : "+v"(acc[6][1]) : "v"(wf[6]));
  SCHED_FENCE();
  GLDS3(rw, vlin, sWt[6], gk4[5], dW, 4096);
  SCHED_FENCE();
  asm volatile("v_mfma_f32_16x16x32_bf16 %0, %1, a[88:91], %0" : "+v"(acc[0][0]) : "v"(wf[0]));
  SCHED_FENCE();
  wf[6] = lds_frag(lb[1] + 27648);
  SCHED_FENCE();
  asm volatile("v_mfma_f32_16x16x32_bf16 %0, %1, a[92:95], %0" : "+v"(acc[0][1]) : "v"(wf[0]));
  SCHED_FENCE();
  asm volatile("v_mfma_f32_16x16x32_bf16 %0, %1, a[88:91], %0" : "+v"(acc[1][0]) : "v"(wf[1]));
  SCHED_FENCE();
  wait_vm_barrier<12>();
  G2_CHECK2();
  wf[0] = lds_frag(lb[1] + 28672);
  SCHED_FENCE();
  asm volatile("v_mfma_f32_16x16x32_bf16 %0, %1, a[92:95], %0" : "+v"(acc[1][1]) : "v"(wf[1]));
  SCHED_FENCE();
  asm volatile("buffer_load_dwordx4 a[160:163], %0, %1, %2 offen offset:0 sc1" :: "v"(va), "s"(ra), "s"(sa0 + 4096) : "memory");
  SCHED_FENCE();
  asm volatile("v_mfma_f32_16x16x32_bf16 %0, %1, a[88:91], %0" : "+v"(acc[2][0]) : "v"(wf[2]));
  SCHED_FENCE();
  wf[1] = lds_frag(lb[1] + 32768);
  SCHED_FENCE();
  asm volatile("v_mfma_f32_16x16x32_bf16 %0, %1, a[92:95], %0" : "+v"(acc[2][1]) : "v"(wf[2]));
  SCHED_FENCE();
  asm volatile("buffer_load_dwordx4 a[164:167], %0, %1, %2 offen offset:256 sc1" :: "v"(va), "s"(ra), "s"(sa0 + 4096) : "memory");
  SCHED_FENCE();
  asm volatile("v_mfma_f32_16x16x32_bf16 %0, %1, a[88:91], %0" : "+v"(acc[3][0]) : "v"(wf[3]));
  SCHED_FENCE();
  wf[2] = lds_frag(lb[1] + 36864);
  SCHED_FENCE();
  asm volatile("v_mfma_f32_16x16x32_bf16 %0, %1, a[92:95], %0" : "+v"(acc[3][1]) : "v"(wf[3]));
  SCHED_FENCE();
  asm volatile("v_mfma_f32_16x16x32_bf16 %0, %1, a[88:91], %0" : "+v"(acc[4][0]) : "v"(wf[4]));
  SCHED_FENCE();
  wf[3] = lds_frag(lb[1] + 40960);
  SCHED_FENCE();
  asm volatile("v_mfma_f32_16x16x32_bf16 %0, %1, a[92:95], %0" : "+v"(acc[4][1]) : "v"(wf[4]));
  SCHED_FENCE();
  asm volatile("buffer_load_dwordx4 a[168:171], %0, %1, %2 offen offset:2048 sc1" :: "v"(va), "s"(ra), "s"(sa0 + 4096) : "memory");
  SCHED_FENCE();
  asm volatile("v_mfma_f32_16x16x32_bf16 %0, %1, a[88:91], %0" : "+v"(acc[5][0]) : "v"(wf[5]));
  SCHED_FENCE();
  wf[4] = lds_frag(lb[1] + 45056);
  SCHED_FENCE();
  asm volatile("v_mfma_f32_16x16x32_bf16 %0, %1, a[92:95], %0" : "+v"(acc[5][1]) : "v"(wf[5]));
  SCHED_FENCE();
  asm volatile("buffer_load_dwordx4 a[172:175], %0, %1, %2 offen offset:2304 sc1" :: "v"(va), "s"(ra), "s"(sa0 + 4096) : "memory");
  SCHED_FENCE();
  asm volatile("v_mfma_f32_16x16x32_bf16 %0, %1, a[88:91], %0" : "+v"(acc[6][0]) : "v"(wf[6]));
  SCHED_FENCE();
  wf[5] = lds_frag(lb[1] + 49152);
  SCHED_FENCE();
  asm volatile("v_mfma_f32_16x16x32_bf16 %0, %1, a[92:95], %0" : "+v"(acc[6][1]) : "v"(wf[6]));
  SCHED_FENCE();
  asm volatile("v_mfma_f32_16x16x32_bf16 %0, %1, a[96:99], %0" : "+v"(acc[0][0]) : "v"(wf[0]));
  SCHED_FENCE();
  wf[6] = lds_frag(lb[1] + 53248);
  SCHED_FENCE();
  asm volatile("v_mfma_f32_16x16x32_bf16 %0, %1, a[100:103], %0" : "+v"(acc[0][1]) : "v"(wf[0]));
  SCHED_FENCE();
  asm volatile("buffer_load_dwordx4 a[176:179], %0, %1, %2 offen offset:0 sc1" :: "v"(va), "s"(ra), "s"(sa0 + 12288) : "memory");
  SCHED_FENCE();
  asm volatile("v_mfma_f32_16x16x32_bf16 %0, %1, a[96:99], %0" : "+v"(acc[1][0]) : "v"(wf[1]));
  SCHED_FENCE();
  wf[0] = lds_frag(lb[1] + 29696);
  SCHED_FENCE();
  asm volatile("v_mfma_f32_16x16x32_bf16 %0, %1, a[100:103], %0" : "+v"(acc[1][1]) : "v"(wf[1]));
  SCHED_FENCE();
  asm volatile("buffer_load_dwordx4 a[180:183], %0, %1, %2 offen offset:256 sc1" :: "v"(va), "s"(ra), "s"(sa0 + 12288) : "memory");
  SCHED_FENCE();
  asm volatile("v_mfma_f32_16x16x32_bf16 %0, %1, a[96:99], %0" : "+v"(acc[2][0]) : "v"(wf[2]));
  SCHED_FENCE();
  wf[1] = lds_frag(lb[1] + 33792);
  SCHED_FENCE();
  asm volatile("v_mfma_f32_16x16x32_bf16 %0, %1, a[100:103], %0" : "+v"(acc[2][1]) : "v"(wf[2]));
  SCHED_FENCE();
  asm volatile("v_mfma_f32_16x16x32_bf16 %0, %1, a[96:99], %0" : "+v"(acc[3][0]) : "v"(wf[3]));
  SCHED_FENCE();
  wf[2] = lds_frag(lb[1] + 37888);
  SCHED_FENCE();
  asm volatile("v_mfma_f32_16x16x32_bf16 %0, %1, a[100:103], %0" : "+v"(acc[3][1]) : "v"(wf[3]));
  SCHED_FENCE();
  asm volatile("buffer_load_dwordx4 a[184:187], %0, %1, %2 offen offset:2048 sc1" :: "v"(va), "s"(ra), "s"(sa0 + 12288) : "memory");
  SCHED_FENCE();
  asm volatile("v_mfma_f32_16x16x32_bf16 %0, %1, a[96:99], %0" : "+v"(acc[4][0]) : "v"(wf[4]));
  SCHED_FENCE();
  wf[3] = lds_frag(lb[1] + 41984);
  SCHED_FENCE();
  asm volatile("v_mfma_f32_16x16x32_bf16 %0, %1, a[100:103], %0" : "+v"(acc[4][1]) : "v"(wf[4]));
  SCHED_FENCE();
  asm volatile("buffer_load_dwordx4 a[188:191], %0, %1, %2 offen offset:2304 sc1" :: "v"(va), "s"(ra), "s"(sa0 + 12288) : "memory");
  SCHED_FENCE();
  asm volatile("v_mfma_f32_16x16x32_bf16 %0, %1, a[96:99], %0" : "+v"(acc[5][0]) : "v"(wf[5]));
  SCHED_FENCE();
  wf[4] = lds_frag(lb[1] + 46080);
  SCHED_FENCE();
  asm volatile("v_mfma_f32_16x16x32_bf16 %0, %1, a[100:103], %0" : "+v"(acc[5][1]) : "v"(wf[5]));
  SCHED_FENCE();
  asm volatile("v_mfma_f32_16x16x32_bf16 %0, %1, a[96:99], %0" : "+v"(acc[6][0]) : "v"(wf[6]));
  SCHED_FENCE();
  wf[5] = lds_frag(lb[1] + 50176);
  SCHED_FENCE();
  asm volatile("v_mfma_f32_16x16x32_bf16 %0, %1, a[100:103], %0" : "+v"(acc[6][1]) : "v"(wf[6]));
  SCHED_FENCE();
  asm volatile("buffer_load_dwordx4 a[192:195], %0, %1, %2 offen offset:0 sc1" :: "v"(va), "s"(ra), "s"(sa0 + 20480) : "memory");
  SCHED_FENCE();
  asm volatile("v_mfma_f32_16x16x32_bf16 %0, %1, a[104:107], %0" : "+v"(acc[0][0]) : "v"(wf[0]));
  SCHED_FENCE();
  wf[6] = lds_frag(lb[1] + 54272);
  SCHED_FENCE();
  asm volatile("v_mfma_f32_16x16x32_bf16 %0, %1, a[108:111], %0" : "+v"(acc[0][1]) : "v"(wf[0]));
  SCHED_FENCE();
  asm volatile("buffer_load_dwordx4 a[196:199], %0, %1, %2 offen offset:256 sc1" :: "v"(va), "s"(ra), "s"(sa0 + 20480) : "memory");
  SCHED_FENCE();
  asm volatile("v_mfma_f32_16x16x32_bf16 %0, %1, a[104:107], %0" : "+v"(acc[1][0]) : "v"(wf[1]));
  SCHED_FENCE();
  wf[0] = lds_frag(lb[1] + 30720);
  SCHED_FENCE();
  asm volatile("v_mfma_f32_16x16x32_bf16 %0, %1, a[108:111], %0" : "+v"(acc[1][1]) : "v"(wf[1]));
  SCHED_FENCE();
  asm volatile("v_mfma_f32_16x16x32_bf16 %0, %1, a[104:107], %0" : "+v"(acc[2][0]) : "v"(wf[2]));
  SCHED_FENCE();
  wf[1] = lds_frag(lb[1] + 34816);
  SCHED_FENCE();
  asm volatile("v_mfma_f32_16x16x32_bf16 %0, %1, a[108:111], %0" : "+v"(acc[2][1]) : "v"(wf[2]));
  SCHED_FENCE();
  asm volatile("buffer_load_dwordx4 a[200:203], %0, %1, %2 offen offset:2048 sc1" :: "v"(va), "s"(ra), "s"(sa0 + 20480) : "memory");
  SCHED_FENCE();
  asm volatile("v_mfma_f32_16x16x32_bf16 %0, %1, a[104:107], %0" : "+v"(acc[3][0]) : "v"(wf[3]));
  SCHED_FENCE();
  wf[2] = lds_frag(lb[1] + 38912);
  SCHED_FENCE();
  asm volatile("v_mfma_f32_16x16x32_bf16 %0, %1, a[108:111], %0" : "+v"(acc[3][1]) : "v"(wf[3]));
  SCHED_FENCE();
  asm volatile("buffer_load_dwordx4 a[204:207], %0, %1, %2 offen offset:2304 sc1" :: "v"(va), "s"(ra), "s"(sa0 + 20480) : "memory");
  SCHED_FENCE();
  asm volatile("v_mfma_f32_16x16x32_bf16 %0, %1, a[104:107], %0" : "+v"(acc[4][0]) : "v"(wf[4]));
  SCHED_FENCE();
  wf[3] = lds_frag(lb[1] + 43008);
  SCHED_FENCE();
  asm volatile("v_mfma_f32_16x16x32_bf16 %0, %1, a[108:111], %0" : "+v"(acc[4][1]) : "v"(wf[4]));
  SCHED_FENCE();
  asm volatile("v_mfma_f32_16x16x32_bf16 %0, %1, a[104:107], %0" : "+v"(acc[5][0]) : "v"(wf[5]));
  SCHED_FENCE();
  wf[4] = lds_frag(lb[1] + 47104);
  SCHED_FENCE();
  asm volatile("v_mfma_f32_16x16x32_bf16 %0, %1, a[108:111], %0" : "+v"(acc[5][1]) : "v"(wf[5]));
  SCHED_FENCE();
  asm volatile("buffer_load_dwordx4 a[208:211], %0, %1, %2 offen offset:0 sc1" :: "v"(va), "s"(ra), "s"(sa0 + 28672) : "memory");
  SCHED_FENCE();
  asm volatile("v_mfma_f32_16x16x32_bf16 %0, %1, a[104:107], %0" : "+v"(acc[6][0]) : "v"(wf[6]));
  SCHED_FENCE();
  wf[5] = lds_frag(lb[1] + 51200);
  SCHED_FENCE();
  asm volatile("v_mfma_f32_16x16x32_bf16 %0, %1, a[108:111], %0" : "+v"(acc[6][1]) : "v"(wf[6]));
  SCHED_FENCE();
  asm volatile("buffer_load_dwordx4 a[212:215], %0, %1, %2 offen offset:256 sc1" :: "v"(va), "s"(ra), "s"(sa0 + 28672) : "memory");
  SCHED_FENCE();
  asm volatile("v_mfma_f32_16x16x32_bf16 %0, %1, a[112:115], %0" : "+v"(acc[0][0]) : "v"(wf[0]));
  SCHED_FENCE();
  wf[6] = lds_frag(lb[1] + 55296);
  SCHED_FENCE();
  asm volatile("v_mfma_f32_16x16x32_bf16 %0, %1, a[116:119], %0" : "+v"(acc[0][1]) : "v"(wf[0]));
  SCHED_FENCE();
  asm volatile("v_mfma_f32_16x16x32_bf16 %0, %1, a[112:115], %0" : "+v"(acc[1][0]) : "v"(wf[1]));
  SCHED_FENCE();
  wf[0] = lds_frag(lb[1] + 31744);
  SCHED_FENCE();
  asm volatile("v_mfma_f32_16x16x32_bf16 %0, %1, a[116:119], %0" : "+v"(acc[1][1]) : "v"(wf[1]));
  SCHED_FENCE();
  asm volatile("buffer_load_dwordx4 a[216:219], %0, %1, %2 offen offset:2048 sc1" :: "v"(va), "s"(ra), "s"(sa0 + 28672) : "memory");
  SCHED_FENCE();
  asm volatile("v_mfma_f32_16x16x32_bf16 %0, %1, a[112:115], %0" : "+v"(acc[2][0]) : "v"(wf[2]));
  SCHED_FENCE();
  wf[1] = lds_frag(lb[1] + 35840);
  SCHED_FENCE();
  asm volatile("v_mfma_f32_16x16x32_bf16 %0, %1, a[116:119], %0" : "+v"(acc[2][1]) : "v"(wf[2]));
  SCHED_FENCE();
  asm volatile("buffer_load_dwordx4 a[220:223], %0, %1, %2 offen offset:2304 sc1" :: "v"(va), "s"(ra), "s"(sa0 + 28672) : "memory");
  SCHED_FENCE();
  asm volatile("v_mfma_f32_16x16x32_bf16 %0, %1, a[112:115], %0" : "+v"(acc[3][0]) : "v"(wf[3]));
  SCHED_FENCE();
  wf[2] = lds_frag(lb[1] + 39936);
  SCHED_FENCE();
  asm volatile("v_mfma_f32_16x16x32_bf16 %0, %1, a[116:119], %0" : "+v"(acc[3][1]) : "v"(wf[3]));
  SCHED_FENCE();
  asm volatile("v_mfma_f32_16x16x32_bf16 %0, %1, a[112:115], %0" : "+v"(acc[4][0]) : "v"(wf[4]));
  SCHED_FENCE();
  wf[3] = lds_frag(lb[1] + 44032);
  SCHED_FENCE();
  asm volatile("v_mfma_f32_16x16x32_bf16 %0, %1, a[116:119], %0" : "+v"(acc[4][1]) : "v"(wf[4]));
  SCHED_FENCE();
  GLDS3(rw, vlin, sWt[0], gk4[6], dW, 8192);
  SCHED_FENCE();
  asm volatile("v_mfma_f32_16x16x32_bf16 %0, %1, a[112:115], %0" : "+v"(acc[5][0]) : "v"(wf[5]));
  SCHED_FENCE();
  wf[4] = lds_frag(lb[1] + 48128);
  SCHED_FENCE();
  asm volatile("v_mfma_f32_16x16x32_bf16 %0, %1, a[116:119], %0" : "+v"(acc[5][1]) : "v"(wf[5]));
  SCHED_FENCE();
  GLDS3(rw, vlin, sWt[1], gk4[6], dW, 12288);
  SCHED_FENCE();
  asm volatile("v_mfma_f32_16x16x32_bf16 %0, %1, a[112:115], %0" : "+v"(acc[6][0]) : "v"(wf[6]));
  SCHED_FENCE();
  wf[5] = lds_frag(lb[1] + 52224);
  SCHED_FENCE();
  asm volatile("v_mfma_f32_16x16x32_bf16 %0, %1, a[116:119], %0" : "+v"(acc[6][1]) : "v"(wf[6]));
  SCHED_FENCE();
  asm volatile("v_mfma_f32_16x16x32_bf16 %0, %1, a[120:123], %0" : "+v"(acc[0][0]) : "v"(wf[0]));
  SCHED_FENCE();
  wf[6] = lds_frag(lb[1] + 56320);
  SCHED_FENCE();
  asm volatile("v_mfma_f32_16x16x32_bf16 %0, %1, a[124:127], %0" : "+v"(acc[0][1]) : "v"(wf[0]));
  SCHED_FENCE();
  GLDS3(rw, vlin, sWt[2], gk4[6], dW, 16384);
  SCHED_FENCE();
  asm volatile("v_mfma_f32_16x16x32_bf16 %0, %1, a[120:123], %0" : "+v"(acc[1][0]) : "v"(wf[1]));
  SCHED_FENCE();
  wait_vm_barrier<26>();
  wf[0] = lds_frag(lb[2] + 0);
  SCHED_FENCE();
  asm volatile("v_mfma_f32_16x16x32_bf16 %0, %1, a[124:127], %0" : "+v"(acc[1][1]) : "v"(wf[1]));
  SCHED_FENCE();
  GLDS3(rw, vlin, sWt[3], gk4[6], dW, 20480);
  SCHED_FENCE();
  asm volatile("v_mfma_f32_16x16x32_bf16 %0, %1, a[120:123], %0" : "+v"(acc[2][0]) : "v"(wf[2]));
  SCHED_FENCE();
  wf[1] = lds_frag(lb[2] + 4096);
  SCHED_FENCE();
  asm volatile("v_mfma_f32_16x16x32_bf16 %0, %1, a[124:127], %0" : "+v"(acc[2][1]) : "v"(wf[2]));
  SCHED_FENCE();
  asm volatile("v_mfma_f32_16x16x32_bf16 %0, %1, a[120:123], %0" : "+v"(acc[3][0]) : "v"(wf[3]));
  SCHED_FENCE();
  wf[2] = lds_frag(lb[2] + 8192);
  SCHED_FENCE();
  asm volatile("v_mfma_f32_16x16x32_bf16 %0, %1, a[124:127], %0" : "+v"(acc[3][1]) : "v"(wf[3]));
  SCHED_FENCE();
  GLDS3(rw, vlin, sWt[4], gk4[6], dW, 24576);
  SCHED_FENCE();
  asm volatile("v_mfma_f32_16x16x32_bf16 %0, %1, a[120:123], %0" : "+v"(acc[4][0]) : "v"(wf[4]));
  SCHED_FENCE();
  wf[3] = lds_frag(lb[2] + 12288);
  SCHED_FENCE();
  asm volatile("v_mfma_f32_16x16x32_bf16 %0, %1, a[124:127], %0" : "+v"(acc[4][1]) : "v"(wf[4]));
  SCHED_FENCE();
  GLDS3(rw, vlin, sWt[5], gk4[6], dW, 28672);
  SCHED_FENCE();
  asm volatile("v_mfma_f32_16x16x32_bf16 %0, %1, a[120:123], %0" : "+v"(acc[5][0]) : "v"(wf[5]));
  SCHED_FENCE();
  wf[4] = lds_frag(lb[2] + 16384);
  SCHED_FENCE();
  asm volatile("v_mfma_f32_16x16x32_bf16 %0, %1, a[124:127], %0" : "+v"(acc[5][1]) : "v"(wf[5]));
  SCHED_FENCE();
  asm volatile("v_mfma_f32_16x16x32_bf16 %0, %1, a[120:123], %0" : "+v"(acc[6][0]) : "v"(wf[6]));
  SCHED_FENCE();
  wf[5] = lds_frag(lb[2] + 20480);
  SCHED_FENCE();
  asm volatile("v_mfma_f32_16x16x32_bf16 %0, %1, a[124:127], %0" : "+v"(acc[6][1]) : "v"(wf[6]));
  SCHED_FENCE();
  GLDS3(rw, vlin, sWt[6], gk4[6], dW, 32768);
  SCHED_FENCE();
  asm volatile("v_mfma_f32_16x16x32_bf16 %0, %1, a[128:131], %0" : "+v"(acc[0][0]) : "v"(wf[0]));
  SCHED_FENCE();
  wf[6] = lds_frag(lb[2] + 24576);
  SCHED_FENCE();
  asm volatile("v_mfma_f32_16x16x32_bf16 %0, %1, a[132:135], %0" : "+v"(acc[0][1]) : "v"(wf[0]));
  SCHED_FENCE();
  asm volatile("buffer_load_dwordx4 a[224:227], %0, %1, %2 offen offset:0 sc1" :: "v"(va), "s"(ra), "s"(sa0 + 40960) : "memory");
  SCHED_FENCE();
  asm volatile("v_mfma_f32_16x16x32_bf16 %0, %1, a[128:131], %0" : "+v"(acc[1][0]) : "v"(wf[1]));
  SCHED_FENCE();
  wf[0] = lds_frag(lb[2] + 1024);
  SCHED_FENCE();
  asm volatile("v_mfma_f32_16x16x32_bf16 %0, %1, a[132:135], %0" : "+v"(acc[1][1]) : "v"(wf[1]));
  SCHED_FENCE();
  asm volatile("v_mfma_f32_16x16x32_bf16 %0, %1, a[128:131], %0" : "+v"(acc[2][0]) : "v"(wf[2]));
  SCHED_FENCE();
  wf[1] = lds_frag(lb[2] + 5120);
  SCHED_FENCE();
  asm volatile("v_mfma_f32_16x16x32_bf16 %0, %1, a[132:135], %0" : "+v"(acc[2][1]) : "v"(wf[2]));
  SCHED_FENCE();
  asm volatile("buffer_load_dwordx4 a[228:231], %0, %1, %2 offen offset:256 sc1" :: "v"(va), "s"(ra), "s"(sa0 + 40960) : "memory");
  SCHED_FENCE();
  asm volatile("v_mfma_f32_16x16x32_bf16 %0, %1, a[128:131], %0" : "+v"(acc[3][0]) : "v"(wf[3]));
  SCHED_FENCE();
  wf[2] = lds_frag(lb[2] + 9216);
  SCHED_FENCE();
  asm volatile("v_mfma_f32_16x16x32_bf16 %0, %1, a[132:135], %0" : "+v"(acc[3][1]) : "v"(wf[3]));
  SCHED_FENCE();
  asm volatile("buffer_load_dwordx4 a[232:235], %0, %1, %2 offen offset:2048 sc1" :: "v"(va), "s"(ra), "s"(sa0 + 40960) : "memory");
  SCHED_FENCE();
  asm volatile("v_mfma_f32_16x16x32_bf16 %0, %1, a[128:131], %0" : "+v"(acc[4][0]) : "v"(wf[4]));
  SCHED_FENCE();
  wf[3] = lds_frag(lb[2] + 13312);
  SCHED_FENCE();
  asm volatile("v_mfma_f32_16x16x32_bf16 %0, %1, a[132:135], %0" : "+v"(acc[4][1]) : "v"(wf[4]));
  SCHED_FENCE();
  asm volatile("v_mfma_f32_16x16x32_bf16 %0, %1, a[128:131], %0" : "+v"(acc[5][0]) : "v"(wf[5]));
  SCHED_FENCE();
  wf[4] = lds_frag(lb[2] + 17408);
  SCHED_FENCE();
  asm volatile("v_mfma_f32_16x16x32_bf16 %0, %1, a[132:135], %0" : "+v"(acc[5][1]) : "v"(wf[5]));
  SCHED_FENCE();
  asm volatile("buffer_load_dwordx4 a[236:239], %0, %1, %2 offen offset:2304 sc1" :: "v"(va), "s"(ra), "s"(sa0 + 40960) : "memory");
  SCHED_FENCE();
  asm volatile("v_mfma_f32_16x16x32_bf16 %0, %1, a[128:131], %0" : "+v"(acc[6][0]) : "v"(wf[6]));
  SCHED_FENCE();
  wf[5] = lds_frag(lb[2] + 21504);
  SCHED_FENCE();
  asm volatile("v_mfma_f32_16x16x32_bf16 %0, %1, a[132:135], %0" : "+v"(acc[6][1]) : "v"(wf[6]));
  SCHED_FENCE();
  asm volatile("buffer_load_dwordx4 a[240:243], %0, %1, %2 offen offset:0 sc1" :: "v"(va), "s"(ra), "s"(sa0 + 49152) : "memory");
  SCHED_FENCE();
  asm volatile("v_mfma_f32_16x16x32_bf16 %0, %1, a[136:139], %0" : "+v"(acc[0][0]) : "v"(wf[0]));
  SCHED_FENCE();
  wf[6] = lds_frag(lb[2] + 25600);
  SCHED_FENCE();
  asm volatile("v_mfma_f32_16x16x32_bf16 %0, %1, a[140:143], %0" : "+v"(acc[0][1]) : "v"(wf[0]));
  SCHED_FENCE();
  asm volatile("v_mfma_f32_16x16x32_bf16 %0, %1, a[136:139], %0" : "+v"(acc[1][0]) : "v"(wf[1]));
  SCHED_FENCE();
  wf[0] = lds_frag(lb[2] + 2048);
  SCHED_FENCE();
  asm volatile("v_mfma_f32_16x16x32_bf16 %0, %1, a[140:143], %0" : "+v"(acc[1][1]) : "v"(wf[1]));
  SCHED_FENCE();
  asm volatile("buffer_load_dwordx4 a[244:247], %0, %1, %2 offen offset:256 sc1" :: "v"(va), "s"(ra), "s"(sa0 + 49152) : "memory");
  SCHED_FENCE();
  asm volatile("v_mfma_f32_16x16x32_bf16 %0, %1, a[136:139], %0" : "+v"(acc[2][0]) : "v"(wf[2]));
  SCHED_FENCE();
  wf[1] = lds_frag(lb[2] + 6144);
  SCHED_FENCE();
  asm volatile("v_mfma_f32_16x16x32_bf16 %0, %1, a[140:143], %0" : "+v"(acc[2][1]) : "v"(wf[2]));
  SCHED_FENCE();
  asm volatile("buffer_load_dwordx4 a[248:251], %0, %1, %2 offen offset:2048 sc1" :: "v"(va), "s"(ra), "s"(sa0 + 49152) : "memory");
  SCHED_FENCE();
  asm volatile("v_mfma_f32_16x16x32_bf16 %0, %1, a[136:139], %0" : "+v"(acc[3][0]) : "v"(wf[3]));
  SCHED_FENCE();
  wf[2] = lds_frag(lb[2] + 10240);
  SCHED_FENCE();
  asm volatile("v_mfma_f32_16x16x32_bf16 %0, %1, a[140:143], %0" : "+v"(acc[3][1]) : "v"(wf[3]));
  SCHED_FENCE();
  asm volatile("v_mfma_f32_16x16x32_bf16 %0, %1, a[136:139], %0" : "+v"(acc[4][0]) : "v"(wf[4]));
  SCHED_FENCE();
  wf[3] = lds_frag(lb[2] + 14336);
  SCHED_FENCE();
  asm volatile("v_mfma_f32_16x16x32_bf16 %0, %1, a[140:143], %0" : "+v"(acc[4][1]) : "v"(wf[4]));
  SCHED_FENCE();
  asm volatile("buffer_load_dwordx4 a[252:255], %0, %1, %2 offen offset:2304 sc1" :: "v"(va), "s"(ra), "s"(sa0 + 49152) : "memory");
  SCHED_FENCE();
  asm volatile("v_mfma_f32_16x16x32_bf16 %0, %1, a[136:139], %0" : "+v"(acc[5][0]) : "v"(wf[5]));
  SCHED_FENCE();
  wf[4] = lds_frag(lb[2] + 18432);
  SCHED_FENCE();
  asm volatile("v_mfma_f32_16x16x32_bf16 %0, %1, a[140:143], %0" : "+v"(acc[5][1]) : "v"(wf[5]));
  SCHED_FENCE();
  GLDS3(rw, vlin, sWt[0], gk4[7], dW, 36864);
  SCHED_FENCE();
  asm volatile("v_mfma_f32_16x16x32_bf16 %0, %1, a[136:139], %0" : "+v"(acc[6][0]) : "v"(wf[6]));
  SCHED_FENCE();
  wf[5] = lds_frag(lb[2] + 22528);
  SCHED_FENCE();
  asm volatile("v_mfma_f32_16x16x32_bf16 %0, %1, a[140:143], %0" : "+v"(acc[6][1]) : "v"(wf[6]));
  SCHED_FENCE();
  asm volatile("v_mfma_f32_16x16x32_bf16 %0, %1, a[144:147], %0" : "+v"(acc[0][0]) : "v"(wf[0]));
  SCHED_FENCE();
  wf[6] = lds_frag(lb[2] + 26624);
  SCHED_FENCE();
  asm volatile("v_mfma_f32_16x16x32_bf16 %0, %1, a[148:151], %0" : "+v"(acc[0][1]) : "v"(wf[0]));
  SCHED_FENCE();
  GLDS3(rw, vlin, sWt[1], gk4[7], dW, 40960);
  SCHED_FENCE();
  asm volatile("v_mfma_f32_16x16x32_bf16 %0, %1, a[144:147], %0" : "+v"(acc[1][0]) : "v"(wf[1]));
  SCHED_FENCE();
  wf[0] = lds_frag(lb[2] + 3072);
  SCHED_FENCE();
  asm volatile("v_mfma_f32_16x16x32_bf16 %0, %1, a[148:151], %0" : "+v"(acc[1][1]) : "v"(wf[1]));
  SCHED_FENCE();
  GLDS3(rw, vlin, sWt[2], gk4[7], dW, 45056);
  SCHED_FENCE();
  asm volatile("v_mfma_f32_16x16x32_bf16 %0, %1, a[144:147], %0" : "+v"(acc[2][0]) : "v"(wf[2]));
  SCHED_FENCE();
  wf[1] = lds_frag(lb[2] + 7168);
  SCHED_FENCE();
  asm volatile("v_mfma_f32_16x16x32_bf16 %0, %1, a[148:151], %0" : "+v"(acc[2][1]) : "v"(wf[2]));
  SCHED_FENCE();
  asm volatile("v_mfma_f32_16x16x32_bf16 %0, %1, a[144:147], %0" : "+v"(acc[3][0]) : "v"(wf[3]));
  SCHED_FENCE();
  wf[2] = lds_frag(lb[2] + 11264);
  SCHED_FENCE();
  asm volatile("v_mfma_f32_16x16x32_bf16 %0, %1, a[148:151], %0" : "+v"(acc[3][1]) : "v"(wf[3]));
  SCHED_FENCE();
  GLDS3(rw, vlin, sWt[3], gk4[7], dW, 49152);
  SCHED_FENCE();
  asm volatile("v_mfma_f32_16x16x32_bf16 %0, %1, a[144:147], %0" : "+v"(acc[4][0]) : "v"(wf[4]));
  SCHED_FENCE();
  wf[3] = lds_frag(lb[2] + 15360);
  SCHED_FENCE();
  asm volatile("v_mfma_f32_16x16x32_bf16 %0, %1, a[148:151], %0" : "+v"(acc[4][1]) : "v"(wf[4]));
  SCHED_FENCE();
  GLDS3(rw, vlin, sWt[4], gk4[7], dW, 53248);
  SCHED_FENCE();
  asm volatile("v_mfma_f32_16x16x32_bf16 %0, %1, a[144:147], %0" : "+v"(acc[5][0]) : "v"(wf[5]));
  SCHED_FENCE();
  wf[4] = lds_frag(lb[2] + 19456);
  SCHED_FENCE();
  asm volatile("v_mfma_f32_16x16x32_bf16 %0, %1, a[148:151], %0" : "+v"(acc[5][1]) : "v"(wf[5]));
  SCHED_FENCE();
  asm volatile("v_mfma_f32_16x16x32_bf16 %0, %1, a[144:147], %0" : "+v"(acc[6][0]) : "v"(wf[6]));
  SCHED_FENCE();
  wf[5] = lds_frag(lb[2] + 23552);
  SCHED_FENCE();
  asm volatile("v_mfma_f32_16x16x32_bf16 %0, %1, a[148:151], %0" : "+v"(acc[6][1]) : "v"(wf[6]));
  SCHED_FENCE();
  GLDS3(rw, vlin, sWt[5], gk4[7], dW, 57344);
  SCHED_FENCE();
  asm volatile("v_mfma_f32_16x16x32_bf16 %0, %1, a[152:155], %0" : "+v"(acc[0][0]) : "v"(wf[0]));
  SCHED_FENCE();
  wf[6] = lds_frag(lb[2] + 27648);
  SCHED_FENCE();
  asm volatile("v_mfma_f32_16x16x32_bf16 %0, %1, a[156:159], %0" : "+v"(acc[0][1]) : "v"(wf[0]));
  SCHED_FENCE();
  GLDS3(rw, vlin, sWt[6], gk4[7], dW, 61440);
  SCHED_FENCE();
  asm volatile("v_mfma_f32_16x16x32_bf16 %0, %1, a[152:155], %0" : "+v"(acc[1][0]) : "v"(wf[1]));
  SCHED_FENCE();
  wait_vm_barrier<30>();
  wf[0] = lds_frag(lb[2] + 28672);
  SCHED_FENCE();
  asm volatile("v_mfma_f32_16x16x32_bf16 %0, %1, a[156:159], %0" : "+v"(acc[1][1]) : "v"(wf[1]));
  SCHED_FENCE();
  asm volatile("v_mfma_f32_16x16x32_bf16 %0, %1, a[152:155], %0" : "+v"(acc[2][0]) : "v"(wf[2]));
  SCHED_FENCE();
  wf[1] = lds_frag(lb[2] + 32768);
  SCHED_FENCE();
  asm volatile("v_mfma_f32_16x16x32_bf16 %0, %1, a[156:159], %0" : "+v"(acc[2][1]) : "v"(wf[2]));
  SCHED_FENCE();
  asm volatile("buffer_load_dwordx4 %0, %1, %2, %3 offen offset:0 sc1" : "=v"(tv[0][0]) : "v"(va), "s"(ra), "s"(sa0 + 57344) : "memory");
  SCHED_FENCE();
  asm volatile("v_mfma_f32_16x16x32_bf16 %0, %1, a[152:155], %0" : "+v"(acc[3][0]) : "v"(wf[3]));
  SCHED_FENCE();
  wf[2] = lds_frag(lb[2] + 36864);
  SCHED_FENCE();
  asm volatile("v_mfma_f32_16x16x32_bf16 %0, %1, a[156:159], %0" : "+v"(acc[3][1]) : "v"(wf[3]));
  SCHED_FENCE();
  asm volatile("buffer_load_dwordx4 %0, %1, %2, %3 offen offset:256 sc1" : "=v"(tv[1][0]) : "v"(va), "s"(ra), "s"(sa0 + 57344) : "memory");
  SCHED_FENCE();
  asm volatile("v_mfma_f32_16x16x32_bf16 %0, %1, a[152:155], %0" : "+v"(acc[4][0]) : "v"(wf[4]));
  SCHED_FENCE();
  wf[3] = lds_frag(lb[2] + 40960);
  SCHED_FENCE();
  asm volatile("v_mfma_f32_16x16x32_bf16 %0, %1, a[156:159], %0" : "+v"(acc[4][1]) : "v"(wf[4]));
  SCHED_FENCE();
  asm volatile("v_mfma_f32_16x16x32_bf16 %0, %1, a[152:155], %0" : "+v"(acc[5][0]) : "v"(wf[5]));
  SCHED_FENCE();
  wf[4] = lds_frag(lb[2] + 45056);
  SCHED_FENCE();
  asm volatile("v_mfma_f32_16x16x32_bf16 %0, %1, a[156:159], %0" : "+v"(acc[5][1]) : "v"(wf[5]));
  SCHED_FENCE();
  asm volatile("buffer_load_dwordx4 %0, %1, %2, %3 offen offset:2048 sc1" : "=v"(tv[0][1]) : "v"(va), "s"(ra), "s"(sa0 + 57344) : "memory");
  SCHED_FENCE();
  asm volatile("v_mfma_f32_16x16x32_bf16 %0, %1, a[152:155], %0" : "+v"(acc[6][0]) : "v"(wf[6]));
  SCHED_FENCE();
  wf[5] = lds_frag(lb[0] + 0);
  SCHED_FENCE();
  asm volatile("v_mfma_f32_16x16x32_bf16 %0, %1, a[156:159], %0" : "+v"(acc[6][1]) : "v"(wf[6]));
  SCHED_FENCE();
  asm volatile("buffer_load_dwordx4 %0, %1, %2, %3 offen offset:2304 sc1" : "=v"(tv[1][1]) : "v"(va), "s"(ra), "s"(sa0 + 57344) : "memory");
  SCHED_FENCE();
  asm volatile("v_mfma_f32_16x16x32_bf16 %0, %1, a[160:163], %0" : "+v"(acc[0][0]) : "v"(wf[0]));
  SCHED_FENCE();
  wf[6] = lds_frag(lb[0] + 4096);
  SCHED_FENCE();
  asm volatile("v_mfma_f32_16x16x32_bf16 %0, %1, a[164:167], %0" : "+v"(acc[0][1]) : "v"(wf[0]));
  SCHED_FENCE();
  asm volatile("v_mfma_f32_16x16x32_bf16 %0, %1, a[160:163], %0" : "+v"(acc[1][0]) : "v"(wf[1]));
  SCHED_FENCE();
  wf[0] = lds_frag(lb[2] + 29696);
  SCHED_FENCE();
  asm volatile("v_mfma_f32_16x16x32_bf16 %0, %1, a[164:167], %0" : "+v"(acc[1][1]) : "v"(wf[1]));
  SCHED_FENCE();
  asm volatile("buffer_load_dwordx4 %0, %1, %2, %3 offen offset:0 sc1" : "=v"(tv[0][2]) : "v"(va), "s"(ra), "s"(sa0 + 65536) : "memory");
  SCHED_FENCE();
  asm volatile("v_mfma_f32_16x16x32_bf16 %0, %1, a[160:163], %0" : "+v"(acc[2][0]) : "v"(wf[2]));
  SCHED_FENCE();
  wf[1] = lds_frag(lb[2] + 33792);
  SCHED_FENCE();
  asm volatile("v_mfma_f32_16x16x32_bf16 %0, %1, a[164:167], %0" : "+v"(acc[2][1]) : "v"(wf[2]));
  SCHED_FENCE();
  asm volatile("buffer_load_dwordx4 %0, %1, %2, %3 offen offset:256 sc1" : "=v"(tv[1][2]) : "v"(va), "s"(ra), "s"(sa0 + 65536) : "memory");
  SCHED_FENCE();
  asm volatile("v_mfma_f32_16x16x32_bf16 %0, %1, a[160:163], %0" : "+v"(acc[3][0]) : "v"(wf[3]));
  SCHED_FENCE();
  wf[2] = lds_frag(lb[2] + 37888);
  SCHED_FENCE();
  asm volatile("v_mfma_f32_16x16x32_bf16 %0, %1, a[164:167], %0" : "+v"(acc[3][1]) : "v"(wf[3]));
  SCHED_FENCE();
  asm volatile("v_mfma_f32_16x16x32_bf16 %0, %1, a[160:163], %0" : "+v"(acc[4][0]) : "v"(wf[4]));
  SCHED_FENCE();
  wf[3] = lds_frag(lb[2] + 41984);
  SCHED_FENCE();
  asm volatile("v_mfma_f32_16x16x32_bf16 %0, %1, a[164:167], %0" : "+v"(acc[4][1]) : "v"(wf[4]));
  SCHED_FENCE();
  asm volatile("buffer_load_dwordx4 %0, %1, %2, %3 offen offset:2048 sc1" : "=v"(tv[0][3]) : "v"(va), "s"(ra), "s"(sa0 + 65536) : "memory");
  SCHED_FENCE();
  asm volatile("v_mfma_f32_16x16x32_bf16 %0, %1, a[160:163], %0" : "+v"(acc[5][0]) : "v"(wf[5]));
  SCHED_FENCE();
  wf[4] = lds_frag(lb[2] + 46080);
  SCHED_FENCE();
  asm volatile("v_mfma_f32_16x16x32_bf16 %0, %1, a[164:167], %0" : "+v"(acc[5][1]) : "v"(wf[5]));
  SCHED_FENCE();
  asm volatile("buffer_load_dwordx4 %0, %1, %2, %3 offen offset:2304 sc1" : "=v"(tv[1][3]) : "v"(va), "s"(ra), "s"(sa0 + 65536) : "memory");
  SCHED_FENCE();
  asm volatile("v_mfma_f32_16x16x32_bf16 %0, %1, a[160:163], %0" : "+v"(acc[6][0]) : "v"(wf[6]));
  SCHED_FENCE();
  wf[5] = lds_frag(lb[0] + 1024);
  SCHED_FENCE();
  asm volatile("v_mfma_f32_16x16x32_bf16 %0, %1, a[164:167], %0" : "+v"(acc[6][1]) : "v"(wf[6]));
  SCHED_FENCE();
  asm volatile("v_mfma_f32_16x16x32_bf16 %0, %1, a[168:171], %0" : "+v"(acc[0][0]) : "v"(wf[0]));
  SCHED_FENCE();
  wf[6] = lds_frag(lb[0] + 5120);
  SCHED_FENCE();
  asm volatile("v_mfma_f32_16x16x32_bf16 %0, %1, a[172:175], %0" : "+v"(acc[0][1]) : "v"(wf[0]));
  SCHED_FENCE();
  GLDS3(rw, vlin, sWt[0], gk4[8], dW, 65536);
  SCHED_FENCE();
  asm volatile("v_mfma_f32_16x16x32_bf16 %0, %1, a[168:171], %0" : "+v"(acc[1][0]) : "v"(wf[1]));
  SCHED_FENCE();
  wf[0] = lds_frag(lb[2] + 30720);
  SCHED_FENCE();
  asm volatile("v_mfma_f32_16x16x32_bf16 %0, %1, a[172:175], %0" : "+v"(acc[1][1]) : "v"(wf[1]));
  SCHED_FENCE();
  GLDS3(rw, vlin, sWt[1], gk4[8], dW, 69632);
  SCHED_FENCE();
  asm volatile("v_mfma_f32_16x16x32_bf16 %0, %1, a[168:171], %0" : "+v"(acc[2][0]) : "v"(wf[2]));
  SCHED_FENCE();
  wf[1] = lds_frag(lb[2] + 34816);
  SCHED_FENCE();
  asm volatile("v_mfma_f32_16x16x32_bf16 %0, %1, a[172:175], %0" : "+v"(acc[2][1]) : "v"(wf[2]));
  SCHED_FENCE();
  asm volatile("v_mfma_f32_16x16x32_bf16 %0, %1, a[168:171], %0" : "+v"(acc[3][0]) : "v"(wf[3]));
  SCHED_FENCE();
  wf[2] = lds_frag(lb[2] + 38912);
  SCHED_FENCE();
  asm volatile("v_mfma_f32_16x16x32_bf16 %0, %1, a[172:175], %0" : "+v"(acc[3][1]) : "v"(wf[3]));
  SCHED_FENCE();
  GLDS3(rw, vlin, sWt[2], gk4[8], dW, 73728);
  SCHED_FENCE();
  asm volatile("v_mfma_f32_16x16x32_bf16 %0, %1, a[168:171], %0" : "+v"(acc[4][0]) : "v"(wf[4]));
  SCHED_FENCE();
  wf[3] = lds_frag(lb[2] + 43008);
  SCHED_FENCE();
  asm volatile("v_mfma_f32_16x16x32_bf16 %0, %1, a[172:175], %0" : "+v"(acc[4][1]) : "v"(wf[4]));
  SCHED_FENCE();
  GLDS3(rw, vlin, sWt[3], gk4[8], dW, 77824);
  SCHED_FENCE();
  asm volatile("v_mfma_f32_16x16x32_bf16 %0, %1, a[168:171], %0" : "+v"(acc[5][0]) : "v"(wf[5]));
  SCHED_FENCE();
  wf[4] = lds_frag(lb[2] + 47104);
  SCHED_FENCE();
  asm volatile("v_mfma_f32_16x16x32_bf16 %0, %1, a[172:175], %0" : "+v"(acc[5][1]) : "v"(wf[5]));
  SCHED_FENCE();
  asm volatile("v_mfma_f32_16x16x32_bf16 %0, %1, a[168:171], %0" : "+v"(acc[6][0]) : "v"(wf[6]));
  SCHED_FENCE();
  wf[5] = lds_frag(lb[0] + 2048);
  SCHED_FENCE();
  asm volatile("v_mfma_f32_16x16x32_bf16 %0, %1, a[172:175], %0" : "+v"(acc[6][1]) : "v"(wf[6]));
  SCHED_FENCE();
  GLDS3(rw, vlin, sWt[4], gk4[8], dW, 81920);
  SCHED_FENCE();
  asm volatile("v_mfma_f32_16x16x32_bf16 %0, %1, a[176:179], %0" : "+v"(acc[0][0]) : "v"(wf[0]));
  SCHED_FENCE();
  wf[6] = lds_frag(lb[0] + 6144);
  SCHED_FENCE();
  asm volatile("v_mfma_f32_16x16x32_bf16 %0, %1, a[180:183], %0" : "+v"(acc[0][1]) : "v"(wf[0]));
  SCHED_FENCE();
  GLDS3(rw, vlin, sWt[5], gk4[8], dW, 86016);
  SCHED_FENCE();
  asm volatile("v_mfma_f32_16x16x32_bf16 %0, %1, a[176:179], %0" : "+v"(acc[1][0]) : "v"(wf[1]));
  SCHED_FENCE();
  wf[0] = lds_frag(lb[2] + 31744);
  SCHED_FENCE();
  asm volatile("v_mfma_f32_16x16x32_bf16 %0, %1, a[180:183], %0" : "+v"(acc[1][1]) : "v"(wf[1]));
  SCHED_FENCE();
  asm volatile("v_mfma_f32_16x16x32_bf16 %0, %1, a[176:179], %0" : "+v"(acc[2][0]) : "v"(wf[2]));
  SCHED_FENCE();
  wf[1] = lds_frag(lb[2] + 35840);
  SCHED_FENCE();
  asm volatile("v_mfma_f32_16x16x32_bf16 %0, %1, a[180:183], %0" : "+v"(acc[2][1]) : "v"(wf[2]));
  SCHED_FENCE();
  GLDS3(rw, vlin, sWt[6], gk4[8], dW, 90112);
  SCHED_FENCE();
  asm volatile("v_mfma_f32_16x16x32_bf16 %0, %1, a[176:179], %0" : "+v"(acc[3][0]) : "v"(wf[3]));
  SCHED_FENCE();
  wf[2] = lds_frag(lb[2] + 39936);
  SCHED_FENCE();
  asm volatile("v_mfma_f32_16x16x32_bf16 %0, %1, a[180:183], %0" : "+v"(acc[3][1]) : "v"(wf[3]));
  SCHED_FENCE();
  GLDS3(rw, vlin, sWt[7], gk4[0], dW, 94208);
  SCHED_FENCE();
  asm volatile("v_mfma_f32_16x16x32_bf16 %0, %1, a[176:179], %0" : "+v"(acc[4][0]) : "v"(wf[4]));
  SCHED_FENCE();
  wf[3] = lds_frag(lb[2] + 44032);
  SCHED_FENCE();
  asm volatile("v_mfma_f32_16x16x32_bf16 %0, %1, a[180:183], %0" : "+v"(acc[4][1]) : "v"(wf[4]));
  SCHED_FENCE();
  asm volatile("v_mfma_f32_16x16x32_bf16 %0, %1, a[176:179], %0" : "+v"(acc[5][0]) : "v"(wf[5]));
  SCHED_FENCE();
  wf[4] = lds_frag(lb[2] + 48128);
  SCHED_FENCE();
  asm volatile("v_mfma_f32_16x16x32_bf16 %0, %1, a[180:183], %0" : "+v"(acc[5][1]) : "v"(wf[5]));
  SCHED_FENCE();
  GLDS3(rw, vlin, sWt[8], gk4[0], dW, 98304);
  SCHED_FENCE();
  asm volatile("v_mfma_f32_16x16x32_bf16 %0, %1, a[176:179], %0" : "+v"(acc[6][0]) : "v"(wf[6]));
  SCHED_FENCE();
  wf[5] = lds_frag(lb[0] + 3072);
  SCHED_FENCE();
  asm volatile("v_mfma_f32_16x16x32_bf16 %0, %1, a[180:183], %0" : "+v"(acc[6][1]) : "v"(wf[6]));
  SCHED_FENCE();
  GLDS3(rw, vlin, sWt[7], gk4[1], dW, 102400);
  SCHED_FENCE();
  asm volatile("v_mfma_f32_16x16x32_bf16 %0, %1, a[184:187], %0" : "+v"(acc[0][0]) : "v"(wf[0]));
  SCHED_FENCE();
  wf[6] = lds_frag(lb[0] + 7168);
  SCHED_FENCE();
  asm volatile("v_mfma_f32_16x16x32_bf16 %0, %1, a[188:191], %0" : "+v"(acc[0][1]) : "v"(wf[0]));
  SCHED_FENCE();
  asm volatile("v_mfma_f32_16x16x32_bf16 %0, %1, a[184:187], %0" : "+v"(acc[1][0]) : "v"(wf[1]));
  SCHED_FENCE();
  wait_vm_barrier<33>();
  wf[0] = lds_frag(lb[0] + 8192);
  SCHED_FENCE();
  asm volatile("v_mfma_f32_16x16x32_bf16 %0, %1, a[188:191], %0" : "+v"(acc[1][1]) : "v"(wf[1]));
  SCHED_FENCE();
  GLDS3(rw, vlin, sWt[8], gk4[1], dW, 106496);
  SCHED_FENCE();
  asm volatile("v_mfma_f32_16x16x32_bf16 %0, %1, a[184:187], %0" : "+v"(acc[2][0]) : "v"(wf[2]));
  SCHED_FENCE();
  wf[1] = lds_frag(lb[0] + 12288);
  SCHED_FENCE();
  asm volatile("v_mfma_f32_16x16x32_bf16 %0, %1, a[188:191], %0" : "+v"(acc[2][1]) : "v"(wf[2]));
  SCHED_FENCE();
  GLDS3(rw, vlin, sWt[7], gk4[2], dW, 110592);
  SCHED_FENCE();
  asm volatile("v_mfma_f32_16x16x32_bf16 %0, %1, a[184:187], %0" : "+v"(acc[3][0]) : "v"(wf[3]));
  SCHED_FENCE();
  wf[2] = lds_frag(lb[0] + 16384);
  SCHED_FENCE();
  asm volatile("v_mfma_f32_16x16x32_bf16 %0, %1, a[188:191], %0" : "+v"(acc[3][1]) : "v"(wf[3]));
  SCHED_FENCE();
  asm volatile("v_mfma_f32_16x16x32_bf16 %0, %1, a[184:187], %0" : "+v"(acc[4][0]) : "v"(wf[4]));
  SCHED_FENCE();
  wf[3] = lds_frag(lb[0] + 20480);
  SCHED_FENCE();
  asm volatile("v_mfma_f32_16x16x32_bf16 %0, %1, a[188:191], %0" : "+v"(acc[4][1]) : "v"(wf[4]));
  SCHED_FENCE();
  GLDS3(rw, vlin, sWt[8], gk4[2], dW, 114688);
  SCHED_FENCE();
  asm volatile("v_mfma_f32_16x16x32_bf16 %0, %1, a[184:187], %0" : "+v"(acc[5][0]) : "v"(wf[5]));
  SCHED_FENCE();
  wf[4] = lds_frag(lb[0] + 24576);
  SCHED_FENCE();
  asm volatile("v_mfma_f32_16x16x32_bf16 %0, %1, a[188:191], %0" : "+v"(acc[5][1]) : "v"(wf[5]));
  SCHED_FENCE();
  GLDS3(rw, vlin, sWt[7], gk4[3], dW, 118784);
  SCHED_FENCE();
  asm volatile("v_mfma_f32_16x16x32_bf16 %0, %1, a[184:187], %0" : "+v"(acc[6][0]) : "v"(wf[6]));
  SCHED_FENCE();
  wf[5] = lds_frag(lb[0] + 28672);
  SCHED_FENCE();
  asm volatile("v_mfma_f32_16x16x32_bf16 %0, %1, a[188:191], %0" : "+v"(acc[6][1]) : "v"(wf[6]));
  SCHED_FENCE();
  asm volatile("v_mfma_f32_16x16x32_bf16 %0, %1, a[192:195], %0" : "+v"(acc[0][0]) : "v"(wf[0]));
  SCHED_FENCE();
  wf[6] = lds_frag(lb[0] + 32768);
  SCHED_FENCE();
  asm volatile("v_mfma_f32_16x16x32_bf16 %0, %1, a[196:199], %0" : "+v"(acc[0][1]) : "v"(wf[0]));
  SCHED_FENCE();
  GLDS3(rw, vlin, sWt[8], gk4[3], dW, 122880);
  SCHED_FENCE();
  asm volatile("v_mfma_f32_16x16x32_bf16 %0, %1, a[192:195], %0" : "+v"(acc[1][0]) : "v"(wf[1]));
  SCHED_FENCE();
  wf[0] = lds_frag(lb[0] + 9216);
  SCHED_FENCE();
  asm volatile("v_mfma_f32_16x16x32_bf16 %0, %1, a[196:199], %0" : "+v"(acc[1][1]) : "v"(wf[1]));
  SCHED_FENCE();
  GLDS3(rw, vlin, sWt[7], gk4[4], dW, 126976);
  SCHED_FENCE();
  asm volatile("v_mfma_f32_16x16x32_bf16 %0, %1, a[192:195], %0" : "+v"(acc[2][0]) : "v"(wf[2]));
  SCHED_FENCE();
  wf[1] = lds_frag(lb[0] + 13312);
  SCHED_FENCE();
  asm volatile("v_mfma_f32_16x16x32_bf16 %0, %1, a[196:199], %0" : "+v"(acc[2][1]) : "v"(wf[2]));
  SCHED_FENCE();
  asm volatile("v_mfma_f32_16x16x32_bf16 %0, %1, a[192:195], %0" : "+v"(acc[3][0]) : "v"(wf[3]));
  SCHED_FENCE();
  wf[2] = lds_frag(lb[0] + 17408);
  SCHED_FENCE();
  asm volatile("v_mfma_f32_16x16x32_bf16 %0, %1, a[196:199], %0" : "+v"(acc[3][1]) : "v"(wf[3]));
  SCHED_FENCE();
  GLDS3(rw, vlin, sWt[8], gk4[4], dW, 131072);
  SCHED_FENCE();
  asm volatile("v_mfma_f32_16x16x32_bf16 %0, %1, a[192:195], %0" : "+v"(acc[4][0]) : "v"(wf[4]));
  SCHED_FENCE();
  wf[3] = lds_frag(lb[0] + 21504);
  SCHED_FENCE();
  asm volatile("v_mfma_f32_16x16x32_bf16 %0, %1, a[196:199], %0" : "+v"(acc[4][1]) : "v"(wf[4]));
  SCHED_FENCE();
  GLDS3(rw, vlin, sWt[7], gk4[5], dW, 135168);
  SCHED_FENCE();
  asm volatile("v_mfma_f32_16x16x32_bf16 %0, %1, a[192:195], %0" : "+v"(acc[5][0]) : "v"(wf[5]));
  SCHED_FENCE();
  wf[4] = lds_frag(lb[0] + 25600);
  SCHED_FENCE();
  asm volatile("v_mfma_f32_16x16x32_bf16 %0, %1, a[196:199], %0" : "+v"(acc[5][1]) : "v"(wf[5]));
  SCHED_FENCE();
  asm volatile("v_mfma_f32_16x16x32_bf16 %0, %1, a[192:195], %0" : "+v"(acc[6][0]) : "v"(wf[6]));
  SCHED_FENCE();
  wf[5] = lds_frag(lb[0] + 29696);
  SCHED_FENCE();
  asm volatile("v_mfma_f32_16x16x32_bf16 %0, %1, a[196:199], %0" : "+v"(acc[6][1]) : "v"(wf[6]));
  SCHED_FENCE();
  GLDS3(rw, vlin, sWt[8], gk4[5], dW, 139264);
  SCHED_FENCE();
  asm volatile("v_mfma_f32_16x16x32_bf16 %0, %1, a[200:203], %0" : "+v"(acc[0][0]) : "v"(wf[0]));
  SCHED_FENCE();
  wf[6] = lds_frag(lb[0] + 33792);
  SCHED_FENCE();
  asm volatile("v_mfma_f32_16x16x32_bf16 %0, %1, a[204:207], %0" : "+v"(acc[0][1]) : "v"(wf[0]));
  SCHED_FENCE();
  asm volatile("v_mfma_f32_16x16x32_bf16 %0, %1, a[200:203], %0" : "+v"(acc[1][0]) : "v"(wf[1]));
  SCHED_FENCE();
  wf[0] = lds_frag(lb[0] + 10240);
  SCHED_FENCE();
  asm volatile("v_mfma_f32_16x16x32_bf16 %0, %1, a[204:207], %0" : "+v"(acc[1][1]) : "v"(wf[1]));
  SCHED_FENCE();
  asm volatile("v_mfma_f32_16x16x32_bf16 %0, %1, a[200:203], %0" : "+v"(acc[2][0]) : "v"(wf[2]));
  SCHED_FENCE();
  wf[1] = lds_frag(lb[0] + 14336);
  SCHED_FENCE();
  asm volatile("v_mfma_f32_16x16x32_bf16 %0, %1, a[204:207], %0" : "+v"(acc[2][1]) : "v"(wf[2]));
  SCHED_FENCE();
  asm volatile("v_mfma_f32_16x16x32_bf16 %0, %1, a[200:203], %0" : "+v"(acc[3][0]) : "v"(wf[3]));
  SCHED_FENCE();
  wf[2] = lds_frag(lb[0] + 18432);
  SCHED_FENCE();
  asm volatile("v_mfma_f32_16x16x32_bf16 %0, %1, a[204:207], %0" : "+v"(acc[3][1]) : "v"(wf[3]));
  SCHED_FENCE();
  asm volatile("v_mfma_f32_16x16x32_bf16 %0, %1, a[200:203], %0" : "+v"(acc[4][0]) : "v"(wf[4]));
  SCHED_FENCE();
  wf[3] = lds_frag(lb[0] + 22528);
  SCHED_FENCE();
  asm volatile("v_mfma_f32_16x16x32_bf16 %0, %1, a[204:207], %0" : "+v"(acc[4][1]) : "v"(wf[4]));
  SCHED_FENCE();
  asm volatile("v_mfma_f32_16x16x32_bf16 %0, %1, a[200:203], %0" : "+v"(acc[5][0]) : "v"(wf[5]));
  SCHED_FENCE();
  wf[4] = lds_frag(lb[0] + 26624);
  SCHED_FENCE();
  asm volatile("v_mfma_f32_16x16x32_bf16 %0, %1, a[204:207], %0" : "+v"(acc[5][1]) : "v"(wf[5]));
  SCHED_FENCE();
  asm volatile("v_mfma_f32_16x16x32_bf16 %0, %1, a[200:203], %0" : "+v"(acc[6][0]) : "v"(wf[6]));
  SCHED_FENCE();
  wf[5] = lds_frag(lb[0] + 30720);
  SCHED_FENCE();
  asm volatile("v_mfma_f32_16x16x32_bf16 %0, %1, a[204:207], %0" : "+v"(acc[6][1]) : "v"(wf[6]));
  SCHED_FENCE();
  asm volatile("v_mfma_f32_16x16x32_bf16 %0, %1, a[208:211], %0" : "+v"(acc[0][0]) : "v"(wf[0]));
  SCHED_FENCE();
  wf[6] = lds_frag(lb[0] + 34816);
  SCHED_FENCE();
  asm volatile("v_mfma_f32_16x16x32_bf16 %0, %1, a[212:215], %0" : "+v"(acc[0][1]) : "v"(wf[0]));
  SCHED_FENCE();
  asm volatile("v_mfma_f32_16x16x32_bf16 %0, %1, a[208:211], %0" : "+v"(acc[1][0]) : "v"(wf[1]));
  SCHED_FENCE();
  wf[0] = lds_frag(lb[0] + 11264);
  SCHED_FENCE();
  asm volatile("v_mfma_f32_16x16x32_bf16 %0, %1, a[212:215], %0" : "+v"(acc[1][1]) : "v"(wf[1]));
  SCHED_FENCE();
  asm volatile("v_mfma_f32_16x16x32_bf16 %0, %1, a[208:211], %0" : "+v"(acc[2][0]) : "v"(wf[2]));
  SCHED_FENCE();
  wf[1] = lds_frag(lb[0] + 15360);
  SCHED_FENCE();
  asm volatile("v_mfma_f32_16x16x32_bf16 %0, %1, a[212:215], %0" : "+v"(acc[2][1]) : "v"(wf[2]));
  SCHED_FENCE();
  asm volatile("v_mfma_f32_16x16x32_bf16 %0, %1, a[208:211], %0" : "+v"(acc[3][0]) : "v"(wf[3]));
  SCHED_FENCE();
  wf[2] = lds_frag(lb[0] + 19456);
  SCHED_FENCE();
  asm volatile("v_mfma_f32_16x16x32_bf16 %0, %1, a[212:215], %0" : "+v"(acc[3][1]) : "v"(wf[3]));
  SCHED_FENCE();
  asm volatile("v_mfma_f32_16x16x32_bf16 %0, %1, a[208:211], %0" : "+v"(acc[4][0]) : "v"(wf[4]));
  SCHED_FENCE();
  wf[3] = lds_frag(lb[0] + 23552);
  SCHED_FENCE();
  asm volatile("v_mfma_f32_16x16x32_bf16 %0, %1, a[212:215], %0" : "+v"(acc[4][1]) : "v"(wf[4]));
  SCHED_FENCE();
  asm volatile("v_mfma_f32_16x16x32_bf16 %0, %1, a[208:211], %0" : "+v"(acc[5][0]) : "v"(wf[5]));
  SCHED_FENCE();
  wf[4] = lds_frag(lb[0] + 27648);
  SCHED_FENCE();
  asm volatile("v_mfma_f32_16x16x32_bf16 %0, %1, a[212:215], %0" : "+v"(acc[5][1]) : "v"(wf[5]));
  SCHED_FENCE();
  asm volatile("v_mfma_f32_16x16x32_bf16 %0, %1, a[208:211], %0" : "+v"(acc[6][0]) : "v"(wf[6]));
  SCHED_FENCE();
  wf[5] = lds_frag(lb[0] + 31744);
  SCHED_FENCE();
  asm volatile("v_mfma_f32_16x16x32_bf16 %0, %1, a[212:215], %0" : "+v"(acc[6][1]) : "v"(wf[6]));
  SCHED_FENCE();
  asm volatile("v_mfma_f32_16x16x32_bf16 %0, %1, a[216:219], %0" : "+v"(acc[0][0]) : "v"(wf[0]));
  SCHED_FENCE();
  wf[6] = lds_frag(lb[0] + 35840);
  SCHED_FENCE();
  asm volatile("v_mfma_f32_16x16x32_bf16 %0, %1, a[220:223], %0" : "+v"(acc[0][1]) : "v"(wf[0]));
  SCHED_FENCE();
  asm volatile("v_mfma_f32_16x16x32_bf16 %0, %1, a[216:219], %0" : "+v"(acc[1][0]) : "v"(wf[1]));
  SCHED_FENCE();
  wait_vm_barrier<27>();
  wf[0] = lds_frag(lb[0] + 36864);
  SCHED_FENCE();
  asm volatile("v_mfma_f32_16x16x32_bf16 %0, %1, a[220:223], %0" : "+v"(acc[1][1]) : "v"(wf[1]));
  SCHED_FENCE();
  GLDS3(rw, vlin, sWt[7], gk4[6], dW, 0);
  SCHED_FENCE();
  asm volatile("v_mfma_f32_16x16x32_bf16 %0, %1, a[216:219], %0" : "+v"(acc[2][0]) : "v"(wf[2]));
  SCHED_FENCE();
  wf[1] = lds_frag(lb[0] + 40960);
  SCHED_FENCE();
  asm volatile("v_mfma_f32_16x16x32_bf16 %0, %1, a[220:223], %0" : "+v"(acc[2][1]) : "v"(wf[2]));
  SCHED_FENCE();
  GLDS3(rw, vlin, sWt[8], gk4[6], dW, 4096);
  SCHED_FENCE();
  asm volatile("v_mfma_f32_16x16x32_bf16 %0, %1, a[216:219], %0" : "+v"(acc[3][0]) : "v"(wf[3]));
  SCHED_FENCE();
  wf[2] = lds_frag(lb[0] + 45056);
  SCHED_FENCE();
  asm volatile("v_mfma_f32_16x16x32_bf16 %0, %1, a[220:223], %0" : "+v"(acc[3][1]) : "v"(wf[3]));
  SCHED_FENCE();
  asm volatile("v_mfma_f32_16x16x32_bf16 %0, %1, a[216:219], %0" : "+v"(acc[4][0]) : "v"(wf[4]));
  SCHED_FENCE();
  wf[3] = lds_frag(lb[0] + 49152);
  SCHED_FENCE();
  asm volatile("v_mfma_f32_16x16x32_bf16 %0, %1, a[220:223], %0" : "+v"(acc[4][1]) : "v"(wf[4]));
  SCHED_FENCE();
  GLDS3(rw, vlin, sWt[7], gk4[7], dW, 143360);
  SCHED_FENCE();
  asm volatile("v_mfma_f32_16x16x32_bf16 %0, %1, a[216:219], %0" : "+v"(acc[5][0]) : "v"(wf[5]));
  SCHED_FENCE();
  wf[4] = lds_frag(lb[0] + 53248);
  SCHED_FENCE();
  asm volatile("v_mfma_f32_16x16x32_bf16 %0, %1, a[220:223], %0" : "+v"(acc[5][1]) : "v"(wf[5]));
  SCHED_FENCE();
  GLDS3(rw, vlin, sWt[8], gk4[7], dW, 147456);
  SCHED_FENCE();
  asm volatile("v_mfma_f32_16x16x32_bf16 %0, %1, a[216:219], %0" : "+v"(acc[6][0]) : "v"(wf[6]));
  SCHED_FENCE();
  wf[5] = lds_frag(lb[1] + 0);
  SCHED_FENCE();
  asm volatile("v_mfma_f32_16x16x32_bf16 %0, %1, a[220:223], %0" : "+v"(acc[6][1]) : "v"(wf[6]));
  SCHED_FENCE();
  asm volatile("v_mfma_f32_16x16x32_bf16 %0, %1, a[224:227], %0" : "+v"(acc[0][0]) : "v"(wf[0]));
  SCHED_FENCE();
  wf[6] = lds_frag(lb[1] + 4096);
  SCHED_FENCE();
  asm volatile("v_mfma_f32_16x16x32_bf16 %0, %1, a[228:231], %0" : "+v"(acc[0][1]) : "v"(wf[0]));
  SCHED_FENCE();
  GLDS3(rw, vlin, sWt[7], gk4[8], dW, 151552);
  SCHED_FENCE();
  asm volatile("v_mfma_f32_16x16x32_bf16 %0, %1, a[224:227], %0" : "+v"(acc[1][0]) : "v"(wf[1]));
  SCHED_FENCE();
  wf[0] = lds_frag(lb[0] + 37888);
  SCHED_FENCE();
  asm volatile("v_mfma_f32_16x16x32_bf16 %0, %1, a[228:231], %0" : "+v"(acc[1][1]) : "v"(wf[1]));
  SCHED_FENCE();
  GLDS3(rw, vlin, sWt[8], gk4[8], dW, 155648);
  SCHED_FENCE();
  asm volatile("v_mfma_f32_16x16x32_bf16 %0, %1, a[224:227], %0" : "+v"(acc[2][0]) : "v"(wf[2]));
  SCHED_FENCE();
  wf[1] = lds_frag(lb[0] + 41984);
  SCHED_FENCE();
  asm volatile("v_mfma_f32_16x16x32_bf16 %0, %1, a[228:231], %0" : "+v"(acc[2][1]) : "v"(wf[2]));
  SCHED_FENCE();
  asm volatile("v_mfma_f32_16x16x32_bf16 %0, %1, a[224:227], %0" : "+v"(acc[3][0]) : "v"(wf[3]));
  SCHED_FENCE();
  wf[2] = lds_frag(lb[0] + 46080);
  SCHED_FENCE();
  asm volatile("v_mfma_f32_16x16x32_bf16 %0, %1, a[228:231], %0" : "+v"(acc[3][1]) : "v"(wf[3]));
  SCHED_FENCE();
  asm volatile("v_mfma_f32_16x16x32_bf16 %0, %1, a[224:227], %0" : "+v"(acc[4][0]) : "v"(wf[4]));
  SCHED_FENCE();
  wf[3] = lds_frag(lb[0] + 50176);
  SCHED_FENCE();
  asm volatile("v_mfma_f32_16x16x32_bf16 %0, %1, a[228:231], %0" : "+v"(acc[4][1]) : "v"(wf[4]));
  SCHED_FENCE();
  asm volatile("v_mfma_f32_16x16x32_bf16 %0, %1, a[224:227], %0" : "+v"(acc[5][0]) : "v"(wf[5]));
  SCHED_FENCE();
  wf[4] = lds_frag(lb[0] + 54272);
  SCHED_FENCE();
  asm volatile("v_mfma_f32_16x16x32_bf16 %0, %1, a[228:231], %0" : "+v"(acc[5][1]) : "v"(wf[5]));
  SCHED_FENCE();
  asm volatile("v_mfma_f32_16x16x32_bf16 %0, %1, a[224:227], %0" : "+v"(acc[6][0]) : "v"(wf[6]));
  SCHED_FENCE();
  wf[5] = lds_frag(lb[1] + 1024);
  SCHED_FENCE();
  asm volatile("v_mfma_f32_16x16x32_bf16 %0, %1, a[228:231], %0" : "+v"(acc[6][1]) : "v"(wf[6]));
  SCHED_FENCE();
  asm volatile("v_mfma_f32_16x16x32_bf16 %0, %1, a[232:235], %0" : "+v"(acc[0][0]) : "v"(wf[0]));
  SCHED_FENCE();
  wf[6] = lds_frag(lb[1] + 5120);
  SCHED_FENCE();
  asm volatile("v_mfma_f32_16x16x32_bf16 %0, %1, a[236:239], %0" : "+v"(acc[0][1]) : "v"(wf[0]));
  SCHED_FENCE();
  asm volatile("v_mfma_f32_16x16x32_bf16 %0, %1, a[232:235], %0" : "+v"(acc[1][0]) : "v"(wf[1]));
  SCHED_FENCE();
  wf[0] = lds_frag(lb[0] + 38912);
  SCHED_FENCE();
  asm volatile("v_mfma_f32_16x16x32_bf16 %0, %1, a[236:239], %0" : "+v"(acc[1][1]) : "v"(wf[1]));
  SCHED_FENCE();
  asm volatile("v_mfma_f32_16x16x32_bf16 %0, %1, a[232:235], %0" : "+v"(acc[2][0]) : "v"(wf[2]));
  SCHED_FENCE();
  wf[1] = lds_frag(lb[0] + 43008);
  SCHED_FENCE();
  asm volatile("v_mfma_f32_16x16x32_bf16 %0, %1, a[236:239], %0" : "+v"(acc[2][1]) : "v"(wf[2]));
  SCHED_FENCE();
  asm volatile("v_mfma_f32_16x16x32_bf16 %0, %1, a[232:235], %0" : "+v"(acc[3][0]) : "v"(wf[3]));
  SCHED_FENCE();
  wf[2] = lds_frag(lb[0] + 47104);
  SCHED_FENCE();
  asm volatile("v_mfma_f32_16x16x32_bf16 %0, %1, a[236:239], %0" : "+v"(acc[3][1]) : "v"(wf[3]));
  SCHED_FENCE();
  asm volatile("v_mfma_f32_16x16x32_bf16 %0, %1, a[232:235], %0" : "+v"(acc[4][0]) : "v"(wf[4]));
  SCHED_FENCE();
  wf[3] = lds_frag(lb[0] + 51200);
  SCHED_FENCE();
  asm volatile("v_mfma_f32_16x16x32_bf16 %0, %1, a[236:239], %0" : "+v"(acc[4][1]) : "v"(wf[4]));
  SCHED_FENCE();
  asm volatile("v_mfma_f32_16x16x32_bf16 %0, %1, a[232:235], %0" : "+v"(acc[5][0]) : "v"(wf[5]));
  SCHED_FENCE();
  wf[4] = lds_frag(lb[0] + 55296);
  SCHED_FENCE();
  asm volatile("v_mfma_f32_16x16x32_bf16 %0, %1, a[236:239], %0" : "+v"(acc[5][1]) : "v"(wf[5]));
  SCHED_FENCE();
  asm volatile("v_mfma_f32_16x16x32_bf16 %0, %1, a[232:235], %0" : "+v"(acc[6][0]) : "v"(wf[6]));
  SCHED_FENCE();
  wf[5] = lds_frag(lb[1] + 2048);
  SCHED_FENCE();
  asm volatile("v_mfma_f32_16x16x32_bf16 %0, %1, a[236:239], %0" : "+v"(acc[6][1]) : "v"(wf[6]));
  SCHED_FENCE();
  asm volatile("v_mfma_f32_16x16x32_bf16 %0, %1, a[240:243], %0" : "+v"(acc[0][0]) : "v"(wf[0]));
  SCHED_FENCE();
  wf[6] = lds_frag(lb[1] + 6144);
  SCHED_FENCE();
  asm volatile("v_mfma_f32_16x16x32_bf16 %0, %1, a[244:247], %0" : "+v"(acc[0][1]) : "v"(wf[0]));
  SCHED_FENCE();
  asm volatile("v_mfma_f32_16x16x32_bf16 %0, %1, a[240:243], %0" : "+v"(acc[1][0]) : "v"(wf[1]));
  SCHED_FENCE();
  wf[0] = lds_frag(lb[0] + 39936);
  SCHED_FENCE();
  asm volatile("v_mfma_f32_16x16x32_bf16 %0, %1, a[244:247], %0" : "+v"(acc[1][1]) : "v"(wf[1]));
  SCHED_FENCE();
  asm volatile("v_mfma_f32_16x16x32_bf16 %0, %1, a[240:243], %0" : "+v"(acc[2][0]) : "v"(wf[2]));
  SCHED_FENCE();
  wf[1] = lds_frag(lb[0] + 44032);
  SCHED_FENCE();
  asm volatile("v_mfma_f32_16x16x32_bf16 %0, %1, a[244:247], %0" : "+v"(acc[2][1]) : "v"(wf[2]));
  SCHED_FENCE();
  asm volatile("v_mfma_f32_16x16x32_bf16 %0, %1, a[240:243], %0" : "+v"(acc[3][0]) : "v"(wf[3]));
  SCHED_FENCE();
  wf[2] = lds_frag(lb[0] + 48128);
  SCHED_FENCE();
  asm volatile("v_mfma_f32_16x16x32_bf16 %0, %1, a[244:247], %0" : "+v"(acc[3][1]) : "v"(wf[3]));
  SCHED_FENCE();
  asm volatile("v_mfma_f32_16x16x32_bf16 %0, %1, a[240:243], %0" : "+v"(acc[4][0]) : "v"(wf[4]));
  SCHED_FENCE();
  wf[3] = lds_frag(lb[0] + 52224);
  SCHED_FENCE();
  asm volatile("v_mfma_f32_16x16x32_bf16 %0, %1, a[244:247], %0" : "+v"(acc[4][1]) : "v"(wf[4]));
  SCHED_FENCE();
  asm volatile("v_mfma_f32_16x16x32_bf16 %0, %1, a[240:243], %0" : "+v"(acc[5][0]) : "v"(wf[5]));
  SCHED_FENCE();
  wf[4] = lds_frag(lb[0] + 56320);
  SCHED_FENCE();
  asm volatile("v_mfma_f32_16x16x32_bf16 %0, %1, a[244:247], %0" : "+v"(acc[5][1]) : "v"(wf[5]));
  SCHED_FENCE();
  asm volatile("v_mfma_f32_16x16x32_bf16 %0, %1, a[240:243], %0" : "+v"(acc[6][0]) : "v"(wf[6]));
  SCHED_FENCE();
  wf[5] = lds_frag(lb[1] + 3072);
  SCHED_FENCE();
  asm volatile("v_mfma_f32_16x16x32_bf16 %0, %1, a[244:247], %0" : "+v"(acc[6][1]) : "v"(wf[6]));
  SCHED_FENCE();
  asm volatile("v_mfma_f32_16x16x32_bf16 %0, %1, a[248:251], %0" : "+v"(acc[0][0]) : "v"(wf[0]));
  SCHED_FENCE();
  wf[6] = lds_frag(lb[1] + 7168);
  SCHED_FENCE();
  asm volatile("v_mfma_f32_16x16x32_bf16 %0, %1, a[252:255], %0" : "+v"(acc[0][1]) : "v"(wf[0]));
  SCHED_FENCE();
  asm volatile("v_mfma_f32_16x16x32_bf16 %0, %1, a[248:251], %0" : "+v"(acc[1][0]) : "v"(wf[1]));
  SCHED_FENCE();
  wait_vm_barrier<18>();
  asm volatile("" : "+v"(tv[0][0]), "+v"(tv[0][1]), "+v"(tv[0][2]), "+v"(tv[0][3]), "+v"(tv[1][0]), "+v"(tv[1][1]), "+v"(tv[1][2]), "+v"(tv[1][3]));
  wf[0] = lds_frag(lb[1] + 8192);
  SCHED_FENCE();
  asm volatile("v_mfma_f32_16x16x32_bf16 %0, %1, a[252:255], %0" : "+v"(acc[1][1]) : "v"(wf[1]));
  SCHED_FENCE();
  asm volatile("v_mfma_f32_16x16x32_bf16 %0, %1, a[248:251], %0" : "+v"(acc[2][0]) : "v"(wf[2]));
  SCHED_FENCE();
  wf[1] = lds_frag(lb[1] + 12288);
  SCHED_FENCE();
  asm volatile("v_mfma_f32_16x16x32_bf16 %0, %1, a[252:255], %0" : "+v"(acc[2][1]) : "v"(wf[2]));
  SCHED_FENCE();
  asm volatile("v_mfma_f32_16x16x32_bf16 %0, %1, a[248:251], %0" : "+v"(acc[3][0]) : "v"(wf[3]));
  SCHED_FENCE();
  wf[2] = lds_frag(lb[1] + 16384);
  SCHED_FENCE();
  asm volatile("v_mfma_f32_16x16x32_bf16 %0, %1, a[252:255], %0" : "+v"(acc[3][1]) : "v"(wf[3]));
  SCHED_FENCE();
  asm volatile("v_mfma_f32_16x16x32_bf16 %0, %1, a[248:251], %0" : "+v"(acc[4][0]) : "v"(wf[4]));
  SCHED_FENCE();
  wf[3] = lds_frag(lb[1] + 20480);
  SCHED_FENCE();
  asm volatile("v_mfma_f32_16x16x32_bf16 %0, %1, a[252:255], %0" : "+v"(acc[4][1]) : "v"(wf[4]));
  SCHED_FENCE();
  asm volatile("v_mfma_f32_16x16x32_bf16 %0, %1, a[248:251], %0" : "+v"(acc[5][0]) : "v"(wf[5]));
  SCHED_FENCE();
  wf[4] = lds_frag(lb[1] + 24576);
  SCHED_FENCE();
  asm volatile("v_mfma_f32_16x16x32_bf16 %0, %1, a[252:255], %0" : "+v"(acc[5][1]) : "v"(wf[5]));
  SCHED_FENCE();
  asm volatile("v_mfma_f32_16x16x32_bf16 %0, %1, a[248:251], %0" : "+v"(acc[6][0]) : "v"(wf[6]));
  SCHED_FENCE();
  wf[5] = lds_frag(lb[1] + 28672);
  SCHED_FENCE();
  asm volatile("v_mfma_f32_16x16x32_bf16 %0, %1, a[252:255], %0" : "+v"(acc[6][1]) : "v"(wf[6]));
  SCHED_FENCE();
  asm volatile("v_mfma_f32_16x16x32_bf16 %0, %1, %2, %0" : "+v"(acc[0][0]) : "v"(wf[0]), "v"(tv[0][0]));
  SCHED_FENCE();
  wf[6] = lds_frag(lb[1] + 32768);
  SCHED_FENCE();
  asm volatile("v_mfma_f32_16x16x32_bf16 %0, %1, %2, %0" : "+v"(acc[0][1]) : "v"(wf[0]), "v"(tv[1][0]));
  SCHED_FENCE();
  asm volatile("v_mfma_f32_16x16x32_bf16 %0, %1, %2, %0" : "+v"(acc[1][0]) : "v"(wf[1]), "v"(tv[0][0]));
  SCHED_FENCE();
  wf[0] = lds_frag(lb[1] + 9216);
  SCHED_FENCE();
  asm volatile("v_mfma_f32_16x16x32_bf16 %0, %1, %2, %0" : "+v"(acc[1][1]) : "v"(wf[1]), "v"(tv[1][0]));
  SCHED_FENCE();
  asm volatile("v_mfma_f32_16x16x32_bf16 %0, %1, %2, %0" : "+v"(acc[2][0]) : "v"(wf[2]), "v"(tv[0][0]));
  SCHED_FENCE();
  wf[1] = lds_frag(lb[1] + 13312);
  SCHED_FENCE();
  asm volatile("v_mfma_f32_16x16x32_bf16 %0, %1, %2, %0" : "+v"(acc[2][1]) : "v"(wf[2]), "v"(tv[1][0]));
  SCHED_FENCE();
  asm volatile("v_mfma_f32_16x16x32_bf16 %0, %1, %2, %0" : "+v"(acc[3][0]) : "v"(wf[3]), "v"(tv[0][0]));
  SCHED_FENCE();
  wf[2] = lds_frag(lb[1] + 17408);
  SCHED_FENCE();
  asm volatile("v_mfma_f32_16x16x32_bf16 %0, %1, %2, %0" : "+v"(acc[3][1]) : "v"(wf[3]), "v"(tv[1][0]));
  SCHED_FENCE();
  asm volatile("v_mfma_f32_16x16x32_bf16 %0, %1, %2, %0" : "+v"(acc[4][0]) : "v"(wf[4]), "v"(tv[0][0]));
  SCHED_FENCE();
  wf[3] = lds_frag(lb[1] + 21504);
  SCHED_FENCE();
  asm volatile("v_mfma_f32_16x16x32_bf16 %0, %1, %2, %0" : "+v"(acc[4][1]) : "v"(wf[4]), "v"(tv[1][0]));
  SCHED_FENCE();
  asm volatile("v_mfma_f32_16x16x32_bf16 %0, %1, %2, %0" : "+v"(acc[5][0]) : "v"(wf[5]), "v"(tv[0][0]));
  SCHED_FENCE();
  wf[4] = lds_frag(lb[1] + 25600);
  SCHED_FENCE();
  asm volatile("v_mfma_f32_16x16x32_bf16 %0, %1, %2, %0" : "+v"(acc[5][1]) : "v"(wf[5]), "v"(tv[1][0]));
  SCHED_FENCE();
  asm volatile("v_mfma_f32_16x16x32_bf16 %0, %1, %2, %0" : "+v"(acc[6][0]) : "v"(wf[6]), "v"(tv[0][0]));
  SCHED_FENCE();
  wf[5] = lds_frag(lb[1] + 29696);
  SCHED_FENCE();
  asm volatile("v_mfma_f32_16x16x32_bf16 %0, %1, %2, %0" : "+v"(acc[6][1]) : "v"(wf[6]), "v"(tv[1][0]));
  SCHED_FENCE();
  asm volatile("v_mfma_f32_16x16x32_bf16 %0, %1, %2, %0" : "+v"(acc[0][0]) : "v"(wf[0]), "v"(tv[0][1]));
  SCHED_FENCE();
  wf[6] = lds_frag(lb[1] + 33792);
  SCHED_FENCE();
  asm volatile("v_mfma_f32_16x16x32_bf16 %0, %1, %2, %0" : "+v"(acc[0][1]) : "v"(wf[0]), "v"(tv[1][1]));
  SCHED_FENCE();
  asm volatile("v_mfma_f32_16x16x32_bf16 %0, %1, %2, %0" : "+v"(acc[1][0]) : "v"(wf[1]), "v"(tv[0][1]));
  SCHED_FENCE();
  wf[0] = lds_frag(lb[1] + 10240);
  SCHED_FENCE();
  asm volatile("v_mfma_f32_16x16x32_bf16 %0, %1, %2, %0" : "+v"(acc[1][1]) : "v"(wf[1]), "v"(tv[1][1]));
  SCHED_FENCE();
  asm volatile("v_mfma_f32_16x16x32_bf16 %0, %1, %2, %0" : "+v"(acc[2][0]) : "v"(wf[2]), "v"(tv[0][1]));
  SCHED_FENCE();
  wf[1] = lds_frag(lb[1] + 14336);
  SCHED_FENCE();
  asm volatile("v_mfma_f32_16x16x32_bf16 %0, %1, %2, %0" : "+v"(acc[2][1]) : "v"(wf[2]), "v"(tv[1][1]));
  SCHED_FENCE();
  asm volatile("v_mfma_f32_16x16x32_bf16 %0, %1, %2, %0" : "+v"(acc[3][0]) : "v"(wf[3]), "v"(tv[0][1]));
  SCHED_FENCE();
  wf[2] = lds_frag(lb[1] + 18432);
  SCHED_FENCE();
  asm volatile("v_mfma_f32_16x16x32_bf16 %0, %1, %2, %0" : "+v"(acc[3][1]) : "v"(wf[3]), "v"(tv[1][1]));
  SCHED_FENCE();
  asm volatile("v_mfma_f32_16x16x32_bf16 %0, %1, %2, %0" : "+v"(acc[4][0]) : "v"(wf[4]), "v"(tv[0][1]));
  SCHED_FENCE();
  wf[3] = lds_frag(lb[1] + 22528);
  SCHED_FENCE();
  asm volatile("v_mfma_f32_16x16x32_bf16 %0, %1, %2, %0" : "+v"(acc[4][1]) : "v"(wf[4]), "v"(tv[1][1]));
  SCHED_FENCE();
  asm volatile("v_mfma_f32_16x16x32_bf16 %0, %1, %2, %0" : "+v"(acc[5][0]) : "v"(wf[5]), "v"(tv[0][1]));
  SCHED_FENCE();
  wf[4] = lds_frag(lb[1] + 26624);
  SCHED_FENCE();
  asm volatile("v_mfma_f32_16x16x32_bf16 %0, %1, %2, %0" : "+v"(acc[5][1]) : "v"(wf[5]), "v"(tv[1][1]));
  SCHED_FENCE();
  asm volatile("v_mfma_f32_16x16x32_bf16 %0, %1, %2, %0" : "+v"(acc[6][0]) : "v"(wf[6]), "v"(tv[0][1]));
  SCHED_FENCE();
  wf[5] = lds_frag(lb[1] + 30720);
  SCHED_FENCE();
  asm volatile("v_mfma_f32_16x16x32_bf16 %0, %1, %2, %0" : "+v"(acc[6][1]) : "v"(wf[6]), "v"(tv[1][1]));
  SCHED_FENCE();
  asm volatile("v_mfma_f32_16x16x32_bf16 %0, %1, %2, %0" : "+v"(acc[0][0]) : "v"(wf[0]), "v"(tv[0][2]));
  SCHED_FENCE();
  wf[6] = lds_frag(lb[1] + 34816);
  SCHED_FENCE();
  asm volatile("v_mfma_f32_16x16x32_bf16 %0, %1, %2, %0" : "+v"(acc[0][1]) : "v"(wf[0]), "v"(tv[1][2]));
  SCHED_FENCE();
  asm volatile("v_mfma_f32_16x16x32_bf16 %0, %1, %2, %0" : "+v"(acc[1][0]) : "v"(wf[1]), "v"(tv[0][2]));
  SCHED_FENCE();
  wf[0] = lds_frag(lb[1] + 11264);
  SCHED_FENCE();
  asm volatile("v_mfma_f32_16x16x32_bf16 %0, %1, %2, %0" : "+v"(acc[1][1]) : "v"(wf[1]), "v"(tv[1][2]));
  SCHED_FENCE();
  asm volatile("v_mfma_f32_16x16x32_bf16 %0, %1, %2, %0" : "+v"(acc[2][0]) : "v"(wf[2]), "v"(tv[0][2]));
  SCHED_FENCE();
  wf[1] = lds_frag(lb[1] + 15360);
  SCHED_FENCE();
  asm volatile("v_mfma_f32_16x16x32_bf16 %0, %1, %2, %0" : "+v"(acc[2][1]) : "v"(wf[2]), "v"(tv[1][2]));
  SCHED_FENCE();
  asm volatile("v_mfma_f32_16x16x32_bf16 %0, %1, %2, %0" : "+v"(acc[3][0]) : "v"(wf[3]), "v"(tv[0][2]));
  SCHED_FENCE();
  wf[2] = lds_frag(lb[1] + 19456);
  SCHED_FENCE();
  asm volatile("v_mfma_f32_16x16x32_bf16 %0, %1, %2, %0" : "+v"(acc[3][1]) : "v"(wf[3]), "v"(tv[1][2]));
  SCHED_FENCE();
  asm volatile("v_mfma_f32_16x16x32_bf16 %0, %1, %2, %0" : "+v"(acc[4][0]) : "v"(wf[4]), "v"(tv[0][2]));
  SCHED_FENCE();
  wf[3] = lds_frag(lb[1] + 23552);
  SCHED_FENCE();
  asm volatile("v_mfma_f32_16x16x32_bf16 %0, %1, %2, %0" : "+v"(acc[4][1]) : "v"(wf[4]), "v"(tv[1][2]));
  SCHED_FENCE();
  asm volatile("v_mfma_f32_16x16x32_bf16 %0, %1, %2, %0" : "+v"(acc[5][0]) : "v"(wf[5]), "v"(tv[0][2]));
  SCHED_FENCE();
  wf[4] = lds_frag(lb[1] + 27648);
  SCHED_FENCE();
  asm volatile("v_mfma_f32_16x16x32_bf16 %0, %1, %2, %0" : "+v"(acc[5][1]) : "v"(wf[5]), "v"(tv[1][2]));
  SCHED_FENCE();
  asm volatile("v_mfma_f32_16x16x32_bf16 %0, %1, %2, %0" : "+v"(acc[6][0]) : "v"(wf[6]), "v"(tv[0][2]));
  SCHED_FENCE();
  wf[5] = lds_frag(lb[1] + 31744);
  SCHED_FENCE();
  asm volatile("v_mfma_f32_16x16x32_bf16 %0, %1, %2, %0" : "+v"(acc[6][1]) : "v"(wf[6]), "v"(tv[1][2]));
  SCHED_FENCE();
  asm volatile("v_mfma_f32_16x16x32_bf16 %0, %1, %2, %0" : "+v"(acc[0][0]) : "v"(wf[0]), "v"(tv[0][3]));
  SCHED_FENCE();
  wf[6] = lds_frag(lb[1] + 35840);
  SCHED_FENCE();
  asm volatile("v_mfma_f32_16x16x32_bf16 %0, %1, %2, %0" : "+v"(acc[0][1]) : "v"(wf[0]), "v"(tv[1][3]));
  SCHED_FENCE();
  asm volatile("v_mfma_f32_16x16x32_bf16 %0, %1, %2, %0" : "+v"(acc[1][0]) : "v"(wf[1]), "v"(tv[0][3]));
  SCHED_FENCE();
  wait_vm_barrier<12>();
  wf[0] = lds_frag(lb[1] + 36864);
  SCHED_FENCE();
  asm volatile("v_mfma_f32_16x16x32_bf16 %0, %1, %2, %0" : "+v"(acc[1][1]) : "v"(wf[1]), "v"(tv[1][3]));
  SCHED_FENCE();
  asm volatile("v_mfma_f32_16x16x32_bf16 %0, %1, %2, %0" : "+v"(acc[2][0]) : "v"(wf[2]), "v"(tv[0][3]));
  SCHED_FENCE();
  wf[1] = lds_frag(lb[1] + 40960);
  SCHED_FENCE();
  asm volatile("v_mfma_f32_16x16x32_bf16 %0, %1, %2, %0" : "+v"(acc[2][1]) : "v"(wf[2]), "v"(tv[1][3]));
  SCHED_FENCE();
  asm volatile("v_mfma_f32_16x16x32_bf16 %0, %1, %2, %0" : "+v"(acc[3][0]) : "v"(wf[3]), "v"(tv[0][3]));
  SCHED_FENCE();
  wf[2] = lds_frag(lb[1] + 37888);
  SCHED_FENCE();
  asm volatile("v_mfma_f32_16x16x32_bf16 %0, %1, %2, %0" : "+v"(acc[3][1]) : "v"(wf[3]), "v"(tv[1][3]));
  SCHED_FENCE();
  asm volatile("v_mfma_f32_16x16x32_bf16 %0, %1, %2, %0" : "+v"(acc[4][0]) : "v"(wf[4]), "v"(tv[0][3]));
  SCHED_FENCE();
  wf[3] = lds_frag(lb[1] + 41984);
  SCHED_FENCE();
  asm volatile("v_mfma_f32_16x16x32_bf16 %0, %1, %2, %0" : "+v"(acc[4][1]) : "v"(wf[4]), "v"(tv[1][3]));
  SCHED_FENCE();
  asm volatile("v_mfma_f32_16x16x32_bf16 %0, %1, %2, %0" : "+v"(acc[5][0]) : "v"(wf[5]), "v"(tv[0][3]));
  SCHED_FENCE();
  wf[4] = lds_frag(lb[1] + 38912);
  SCHED_FENCE();
  asm volatile("v_mfma_f32_16x16x32_bf16 %0, %1, %2, %0" : "+v"(acc[5][1]) : "v"(wf[5]), "v"(tv[1][3]));
  SCHED_FENCE();
  asm volatile("v_mfma_f32_16x16x32_bf16 %0, %1, %2, %0" : "+v"(acc[6][0]) : "v"(wf[6]), "v"(tv[0][3]));
  SCHED_FENCE();
  wf[5] = lds_frag(lb[1] + 43008);
  SCHED_FENCE();
  asm volatile("v_mfma_f32_16x16x32_bf16 %0, %1, %2, %0" : "+v"(acc[6][1]) : "v"(wf[6]), "v"(tv[1][3]));
  SCHED_FENCE();
  asm volatile("v_mfma_f32_16x16x32_bf16 %0, %1, a[0:3], 0" : "=&v"(acc[7][0]) : "v"(wf[0]));
  SCHED_FENCE();
  wf[6] = lds_frag(lb[1] + 39936);
  SCHED_FENCE();
  asm volatile("v_mfma_f32_16x16x32_bf16 %0, %1, a[4:7], 0" : "=&v"(acc[7][1]) : "v"(wf[0]));
  SCHED_FENCE();
  asm volatile("v_mfma_f32_16x16x32_bf16 %0, %1, a[0:3], 0" : "=&v"(acc[8][0]) : "v"(wf[1]));
  SCHED_FENCE();
  wf[0] = lds_frag(lb[1] + 44032);
  SCHED_FENCE();
  asm volatile("v_mfma_f32_16x16x32_bf16 %0, %1, a[4:7], 0" : "=&v"(acc[8][1]) : "v"(wf[1]));
  SCHED_FENCE();
  asm volatile("v_mfma_f32_16x16x32_bf16 %0, %1, a[8:11], %0" : "+v"(acc[7][0]) : "v"(wf[2]));
  SCHED_FENCE();
  wf[1] = lds_frag(lb[1] + 45056);
  SCHED_FENCE();
  asm volatile("v_mfma_f32_16x16x32_bf16 %0, %1, a[12:15], %0" : "+v"(acc[7][1]) : "v"(wf[2]));
  SCHED_FENCE();
  asm volatile("v_mfma_f32_16x16x32_bf16 %0, %1, a[8:11], %0" : "+v"(acc[8][0]) : "v"(wf[3]));
  SCHED_FENCE();
  wf[2] = lds_frag(lb[1] + 49152);
  SCHED_FENCE();
  asm volatile("v_mfma_f32_16x16x32_bf16 %0, %1, a[12:15], %0" : "+v"(acc[8][1]) : "v"(wf[3]));
  SCHED_FENCE();
  asm volatile("v_mfma_f32_16x16x32_bf16 %0, %1, a[16:19], %0" : "+v"(acc[7][0]) : "v"(wf[4]));
  SCHED_FENCE();
  wf[3] = lds_frag(lb[1] + 46080);
  pk[0] = pack_bf16(acc[0][0][0], acc[0][0][1]); pk[1] = pack_bf16(acc[0][0][2], acc[0][0][3]);
  SCHED_FENCE();
  asm volatile("v_mfma_f32_16x16x32_bf16 %0, %1, a[20:23], %0" : "+v"(acc[7][1]) : "v"(wf[4]));
  SCHED_FENCE();
  pk[2] = pack_bf16(acc[0][1][0], acc[0][1][1]); pk[3] = pack_bf16(acc[0][1][2], acc[0][1][3]);
  SCHED_FENCE();
  asm volatile("v_mfma_f32_16x16x32_bf16 %0, %1, a[16:19], %0" : "+v"(acc[8][0]) : "v"(wf[5]));
  SCHED_FENCE();
  wf[4] = lds_frag(lb[1] + 50176);
  __builtin_amdgcn_raw_buffer_store_b128((u32x4){pk[0], pk[1], pk[2], pk[3]}, rp, vlin, sP2[0], 16);
  SCHED_FENCE();
  asm volatile("v_mfma_f32_16x16x32_bf16 %0, %1, a[20:23], %0" : "+v"(acc[8][1]) : "v"(wf[5]));
  SCHED_FENCE();
  pk[0] = pack_bf16(acc[1][0][0], acc[1][0][1]); pk[1] = pack_bf16(acc[1][0][2], acc[1][0][3]);
  SCHED_FENCE();
  asm volatile("v_mfma_f32_16x16x32_bf16 %0, %1, a[24:27], %0" : "+v"(acc[7][0]) : "v"(wf[6]));
  SCHED_FENCE();
  wf[5] = lds_frag(lb[1] + 47104);
  pk[2] = pack_bf16(acc[1][1][0], acc[1][1][1]); pk[3] = pack_bf16(acc[1][1][2], acc[1][1][3]);
  SCHED_FENCE();
  asm volatile("v_mfma_f32_16x16x32_bf16 %0, %1, a[28:31], %0" : "+v"(acc[7][1]) : "v"(wf[6]));
  SCHED_FENCE();
  __builtin_amdgcn_raw_buffer_store_b128((u32x4){pk[0], pk[1], pk[2], pk[3]}, rp, vlin, sP2[1], 16);
  SCHED_FENCE();
  asm volatile("v_mfma_f32_16x16x32_bf16 %0, %1, a[24:27], %0" : "+v"(acc[8][0]) : "v"(wf[0]));
  SCHED_FENCE();
  wf[6] = lds_frag(lb[1] + 51200);
  pk[0] = pack_bf16(acc[2][0][0], acc[2][0][1]); pk[1] = pack_bf16(acc[2][0][2], acc[2][0][3]);
  SCHED_FENCE();
  asm volatile("v_mfma_f32_16x16x32_bf16 %0, %1, a[28:31], %0" : "+v"(acc[8][1]) : "v"(wf[0]));
  SCHED_FENCE();
  pk[2] = pack_bf16(acc[2][1][0], acc[2][1][1]); pk[3] = pack_bf16(acc[2][1][2], acc[2][1][3]);
  SCHED_FENCE();
  asm volatile("v_mfma_f32_16x16x32_bf16 %0, %1, a[32:35], %0" : "+v"(acc[7][0]) : "v"(wf[1]));
  SCHED_FENCE();
  wf[0] = lds_frag(lb[1] + 48128);
  __builtin_amdgcn_raw_buffer_store_b128((u32x4){pk[0], pk[1], pk[2], pk[3]}, rp, vlin, sP2[2], 16);
  SCHED_FENCE();
  asm volatile("v_mfma_f32_16x16x32_bf16 %0, %1, a[36:39], %0" : "+v"(acc[7][1]) : "v"(wf[1]));
  SCHED_FENCE();
  pk[0] = pack_bf16(acc[3][0][0], acc[3][0][1]); pk[1] = pack_bf16(acc[3][0][2], acc[3][0][3]);
  SCHED_FENCE();
  asm volatile("v_mfma_f32_16x16x32_bf16 %0, %1, a[32:35], %0" : "+v"(acc[8][0]) : "v"(wf[2]));
  SCHED_FENCE();
  wf[1] = lds_frag(lb[1] + 52224);
  pk[2] = pack_bf16(acc[3][1][0], acc[3][1][1]); pk[3] = pack_bf16(acc[3][1][2], acc[3][1][3]);
  SCHED_FENCE();
  asm volatile("v_mfma_f32_16x16x32_bf16 %0, %1, a[36:39], %0" : "+v"(acc[8][1]) : "v"(wf[2]));
  SCHED_FENCE();
  __builtin_amdgcn_raw_buffer_store_b128((u32x4){pk[0], pk[1], pk[2], pk[3]}, rp, vlin, sP2[3], 16);
  SCHED_FENCE();
  asm volatile("v_mfma_f32_16x16x32_bf16 %0, %1, a[40:43], %0" : "+v"(acc[7][0]) : "v"(wf[3]));
  SCHED_FENCE();
  wf[2] = lds_frag(lb[1] + 53248);
  pk[0] = pack_bf16(acc[4][0][0], acc[4][0][1]); pk[1] = pack_bf16(acc[4][0][2], acc[4][0][3]);
  SCHED_FENCE();
  asm volatile("v_mfma_f32_16x16x32_bf16 %0, %1, a[44:47], %0" : "+v"(acc[7][1]) : "v"(wf[3]));
  SCHED_FENCE();
  pk[2] = pack_bf16(acc[4][1][0], acc[4][1][1]); pk[3] = pack_bf16(acc[4][1][2], acc[4][1][3]);
  SCHED_FENCE();
  asm volatile("v_mfma_f32_16x16x32_bf16 %0, %1, a[40:43], %0" : "+v"(acc[8][0]) : "v"(wf[4]));
  SCHED_FENCE();
  wf[3] = lds_frag(lb[2] + 0);
  __builtin_amdgcn_raw_buffer_store_b128((u32x4){pk[0], pk[1], pk[2], pk[3]}, rp, vlin, sP2[4], 16);
  SCHED_FENCE();
  asm volatile("v_mfma_f32_16x16x32_bf16 %0, %1, a[44:47], %0" : "+v"(acc[8][1]) : "v"(wf[4]));
  SCHED_FENCE();
  pk[0] = pack_bf16(acc[5][0][0], acc[5][0][1]); pk[1] = pack_bf16(acc[5][0][2], acc[5][0][3]);
  SCHED_FENCE();
  asm volatile("v_mfma_f32_16x16x32_bf16 %0, %1, a[48:51], %0" : "+v"(acc[7][0]) : "v"(wf[5]));
  SCHED_FENCE();
  wf[4] = lds_frag(lb[1] + 54272);
  pk[2] = pack_bf16(acc[5][1][0], acc[5][1][1]); pk[3] = pack_bf16(acc[5][1][2], acc[5][1][3]);
  SCHED_FENCE();
  asm volatile("v_mfma_f32_16x16x32_bf16 %0, %1, a[52:55], %0" : "+v"(acc[7][1]) : "v"(wf[5]));
  SCHED_FENCE();
  __builtin_amdgcn_raw_buffer_store_b128((u32x4){pk[0], pk[1], pk[2], pk[3]}, rp, vlin, sP2[5], 16);
  SCHED_FENCE();
  asm volatile("v_mfma_f32_16x16x32_bf16 %0, %1, a[48:51], %0" : "+v"(acc[8][0]) : "v"(wf[6]));
  SCHED_FENCE();
  wf[5] = lds_frag(lb[2] + 1024);
  pk[0] = pack_bf16(acc[6][0][0], acc[6][0][1]); pk[1] = pack_bf16(acc[6][0][2], acc[6][0][3]);
  SCHED_FENCE();
  asm volatile("v_mfma_f32_16x16x32_bf16 %0, %1, a[52:55], %0" : "+v"(acc[8][1]) : "v"(wf[6]));
  SCHED_FENCE();
  pk[2] = pack_bf16(acc[6][1][0], acc[6][1][1]); pk[3] = pack_bf16(acc[6][1][2], acc[6][1][3]);
  SCHED_FENCE();
  asm volatile("v_mfma_f32_16x16x32_bf16 %0, %1, a[56:59], %0" : "+v"(acc[7][0]) : "v"(wf[0]));
  SCHED_FENCE();
  wf[6] = lds_frag(lb[1] + 55296);
  __builtin_amdgcn_raw_buffer_store_b128((u32x4){pk[0], pk[1], pk[2], pk[3]}, rp, vlin, sP2[6], 16);
  SCHED_FENCE();
  asm volatile("v_mfma_f32_16x16x32_bf16 %0, %1, a[60:63], %0" : "+v"(acc[7][1]) : "v"(wf[0]));
  SCHED_FENCE();
  asm volatile("v_mfma_f32_16x16x32_bf16 %0, %1, a[56:59], %0" : "+v"(acc[8][0]) : "v"(wf[1]));
  SCHED_FENCE();
  wf[0] = lds_frag(lb[2] + 2048);
  SCHED_FENCE();
  asm volatile("v_mfma_f32_16x16x32_bf16 %0, %1, a[60:63], %0" : "+v"(acc[8][1]) : "v"(wf[1]));
  SCHED_FENCE();
  asm volatile("v_mfma_f32_16x16x32_bf16 %0, %1, a[64:67], %0" : "+v"(acc[7][0]) : "v"(wf[2]));
  SCHED_FENCE();
  wf[1] = lds_frag(lb[1] + 56320);
  SCHED_FENCE();
  asm volatile("v_mfma_f32_16x16x32_bf16 %0, %1, a[68:71], %0" : "+v"(acc[7][1]) : "v"(wf[2]));
  SCHED_FENCE();
  asm volatile("v_mfma_f32_16x16x32_bf16 %0, %1, a[64:67], %0" : "+v"(acc[8][0]) : "v"(wf[3]));
  SCHED_FENCE();
  wf[2] = lds_frag(lb[2] + 3072);
  SCHED_FENCE();
  asm volatile("v_mfma_f32_16x16x32_bf16 %0, %1, a[68:71], %0" : "+v"(acc[8][1]) : "v"(wf[3]));
  SCHED_FENCE();
  asm volatile("v_mfma_f32_16x16x32_bf16 %0, %1, a[72:75], %0" : "+v"(acc[7][0]) : "v"(wf[4]));
  SCHED_FENCE();
  wait_vm_barrier<13>();
  wf[3] = lds_frag(lb[2] + 4096);
  SCHED_FENCE();
  asm volatile("v_mfma_f32_16x16x32_bf16 %0, %1, a[76:79], %0" : "+v"(acc[7][1]) : "v"(wf[4]));
  SCHED_FENCE();
  asm volatile("v_mfma_f32_16x16x32_bf16 %0, %1, a[72:75], %0" : "+v"(acc[8][0]) : "v"(wf[5]));
  SCHED_FENCE();
  wf[4] = lds_frag(lb[2] + 8192);
  SCHED_FENCE();
  asm volatile("v_mfma_f32_16x16x32_bf16 %0, %1, a[76:79], %0" : "+v"(acc[8][1]) : "v"(wf[5]));
  SCHED_FENCE();
  asm volatile("v_mfma_f32_16x16x32_bf16 %0, %1, a[80:83], %0" : "+v"(acc[7][0]) : "v"(wf[6]));
  SCHED_FENCE();
  wf[5] = lds_frag(lb[2] + 5120);
  G2_LOADH(0, 0);
  SCHED_FENCE();
  asm volatile("v_mfma_f32_16x16x32_bf16 %0, %1, a[84:87], %0" : "+v"(acc[7][1]) : "v"(wf[6]));
  SCHED_FENCE();
  asm volatile("v_mfma_f32_16x16x32_bf16 %0, %1, a[80:83], %0" : "+v"(acc[8][0]) : "v"(wf[0]));
  SCHED_FENCE();
  wf[6] = lds_frag(lb[2] + 9216);
  G2_LOADH(0, 1);
  SCHED_FENCE();
  asm volatile("v_mfma_f32_16x16x32_bf16 %0, %1, a[84:87], %0" : "+v"(acc[8][1]) : "v"(wf[0]));
  SCHED_FENCE();
  asm volatile("v_mfma_f32_16x16x32_bf16 %0, %1, a[88:91], %0" : "+v"(acc[7][0]) : "v"(wf[1]));
  SCHED_FENCE();
  wf[0] = lds_frag(lb[2] + 6144);
  G2_LOADH(1, 0);
  SCHED_FENCE();
  asm volatile("v_mfma_f32_16x16x32_bf16 %0, %1, a[92:95], %0" : "+v"(acc[7][1]) : "v"(wf[1]));
  SCHED_FENCE();
  asm volatile("v_mfma_f32_16x16x32_bf16 %0, %1, a[88:91], %0" : "+v"(acc[8][0]) : "v"(wf[2]));
  SCHED_FENCE();
  wf[1] = lds_frag(lb[2] + 10240);
  G2_LOADH(1, 1);
  SCHED_FENCE();
  asm volatile("v_mfma_f32_16x16x32_bf16 %0, %1, a[92:95], %0" : "+v"(acc[8][1]) : "v"(wf[2]));
  SCHED_FENCE();
  asm volatile("v_mfma_f32_16x16x32_bf16 %0, %1, a[96:99], %0" : "+v"(acc[7][0]) : "v"(wf[3]));
  SCHED_FENCE();
  wf[2] = lds_frag(lb[2] + 7168);
  G2_LOADH(2, 0);
  SCHED_FENCE();
  asm volatile("v_mfma_f32_16x16x32_bf16 %0, %1, a[100:103], %0" : "+v"(acc[7][1]) : "v"(wf[3]));
  SCHED_FENCE();
  asm volatile("v_mfma_f32_16x16x32_bf16 %0, %1, a[96:99], %0" : "+v"(acc[8][0]) : "v"(wf[4]));
  SCHED_FENCE();
  wf[3] = lds_frag(lb[2] + 11264);
  G2_LOADH(2, 1);
  SCHED_FENCE();
  asm volatile("v_mfma_f32_16x16x32_bf16 %0, %1, a[100:103], %0" : "+v"(acc[8][1]) : "v"(wf[4]));
  SCHED_FENCE();
  asm volatile("v_mfma_f32_16x16x32_bf16 %0, %1, a[104:107], %0" : "+v"(acc[7][0]) : "v"(wf[5]));
  SCHED_FENCE();
  wf[4] = lds_frag(lb[2] + 12288);
  G2_LOADM(0);
  SCHED_FENCE();
  asm volatile("v_mfma_f32_16x16x32_bf16 %0, %1, a[108:111], %0" : "+v"(acc[7][1]) : "v"(wf[5]));
  SCHED_FENCE();
  asm volatile("v_mfma_f32_16x16x32_bf16 %0, %1, a[104:107], %0" : "+v"(acc[8][0]) : "v"(wf[6]));
  SCHED_FENCE();
  wf[5] = lds_frag(lb[2] + 16384);
  G2_LOADM(1);
  SCHED_FENCE();
  asm volatile("v_mfma_f32_16x16x32_bf16 %0, %1, a[108:111], %0" : "+v"(acc[8][1]) : "v"(wf[6]));
  SCHED_FENCE();
  asm volatile("v_mfma_f32_16x16x32_bf16 %0, %1, a[112:115], %0" : "+v"(acc[7][0]) : "v"(wf[0]));
  SCHED_FENCE();
  wf[6] = lds_frag(lb[2] + 13312);
  SCHED_FENCE();
  asm volatile("v_mfma_f32_16x16x32_bf16 %0, %1, a[116:119], %0" : "+v"(acc[7][1]) : "v"(wf[0]));
  SCHED_FENCE();
  asm volatile("v_mfma_f32_16x16x32_bf16 %0, %1, a[112:115], %0" : "+v"(acc[8][0]) : "v"(wf[1]));
  SCHED_FENCE();
  wf[0] = lds_frag(lb[2] + 17408);
  SCHED_FENCE();
  asm volatile("v_mfma_f32_16x16x32_bf16 %0, %1, a[116:119], %0" : "+v"(acc[8][1]) : "v"(wf[1]));
  SCHED_FENCE();
  asm volatile("v_mfma_f32_16x16x32_bf16 %0, %1, a[120:123], %0" : "+v"(acc[7][0]) : "v"(wf[2]));
  SCHED_FENCE();
  wf[1] = lds_frag(lb[2] + 14336);
  SCHED_FENCE();
  asm volatile("v_mfma_f32_16x16x32_bf16 %0, %1, a[124:127], %0" : "+v"(acc[7][1]) : "v"(wf[2]));
  SCHED_FENCE();
  asm volatile("v_mfma_f32_16x16x32_bf16 %0, %1, a[120:123], %0" : "+v"(acc[8][0]) : "v"(wf[3]));
  SCHED_FENCE();
  wf[2] = lds_frag(lb[2] + 18432);
  SCHED_FENCE();
  asm volatile("v_mfma_f32_16x16x32_bf16 %0, %1, a[124:127], %0" : "+v"(acc[8][1]) : "v"(wf[3]));
  SCHED_FENCE();
  asm volatile("v_mfma_f32_16x16x32_bf16 %0, %1, a[128:131], %0" : "+v"(acc[7][0]) : "v"(wf[4]));
  SCHED_FENCE();
  wf[3] = lds_frag(lb[2] + 15360);
  SCHED_FENCE();
  asm volatile("v_mfma_f32_16x16x32_bf16 %0, %1, a[132:135], %0" : "+v"(acc[7][1]) : "v"(wf[4]));
  SCHED_FENCE();
  asm volatile("v_mfma_f32_16x16x32_bf16 %0, %1, a[128:131], %0" : "+v"(acc[8][0]) : "v"(wf[5]));
  SCHED_FENCE();
  wf[4] = lds_frag(lb[2] + 19456);
  SCHED_FENCE();
  asm volatile("v_mfma_f32_16x16x32_bf16 %0, %1, a[132:135], %0" : "+v"(acc[8][1]) : "v"(wf[5]));
  SCHED_FENCE();
  asm volatile("v_mfma_f32_16x16x32_bf16 %0, %1, a[136:139], %0" : "+v"(acc[7][0]) : "v"(wf[6]));
  SCHED_FENCE();
  wf[5] = lds_frag(lb[2] + 20480);
  SCHED_FENCE();
  asm volatile("v_mfma_f32_16x16x32_bf16 %0, %1, a[140:143], %0" : "+v"(acc[7][1]) : "v"(wf[6]));
  SCHED_FENCE();
  asm volatile("v_mfma_f32_16x16x32_bf16 %0, %1, a[136:139], %0" : "+v"(acc[8][0]) : "v"(wf[0]));
  SCHED_FENCE();
  wf[6] = lds_frag(lb[2] + 24576);
  SCHED_FENCE();
  asm volatile("v_mfma_f32_16x16x32_bf16 %0, %1, a[140:143], %0" : "+v"(acc[8][1]) : "v"(wf[0]));
  SCHED_FENCE();
  asm volatile("v_mfma_f32_16x16x32_bf16 %0, %1, a[144:147], %0" : "+v"(acc[7][0]) : "v"(wf[1]));
  SCHED_FENCE();
  wf[0] = lds_frag(lb[2] + 21504);
  SCHED_FENCE();
  asm volatile("v_mfma_f32_16x16x32_bf16 %0, %1, a[148:151], %0" : "+v"(acc[7][1]) : "v"(wf[1]));
  SCHED_FENCE();
  asm volatile("v_mfma_f32_16x16x32_bf16 %0, %1, a[144:147], %0" : "+v"(acc[8][0]) : "v"(wf[2]));
  SCHED_FENCE();
  wf[1] = lds_frag(lb[2] + 25600);
  SCHED_FENCE();
  asm volatile("v_mfma_f32_16x16x32_bf16 %0, %1, a[148:151], %0" : "+v"(acc[8][1]) : "v"(wf[2]));
  SCHED_FENCE();
  asm volatile("v_mfma_f32_16x16x32_bf16 %0, %1, a[152:155], %0" : "+v"(acc[7][0]) : "v"(wf[3]));
  SCHED_FENCE();
  wf[2] = lds_frag(lb[2] + 22528);
  SCHED_FENCE();
  asm volatile("v_mfma_f32_16x16x32_bf16 %0, %1, a[156:159], %0" : "+v"(acc[7][1]) : "v"(wf[3]));
  SCHED_FENCE();
  asm volatile("v_mfma_f32_16x16x32_bf16 %0, %1, a[152:155], %0" : "+v"(acc[8][0]) : "v"(wf[4]));
  SCHED_FENCE();
  wf[3] = lds_frag(lb[2] + 26624);
  SCHED_FENCE();
  asm volatile("v_mfma_f32_16x16x32_bf16 %0, %1, a[156:159], %0" : "+v"(acc[8][1]) : "v"(wf[4]));
  SCHED_FENCE();
  asm volatile("v_mfma_f32_16x16x32_bf16 %0, %1, a[160:163], %0" : "+v"(acc[7][0]) : "v"(wf[5]));
  SCHED_FENCE();
  wf[4] = lds_frag(lb[2] + 23552);
  SCHED_FENCE();
  asm volatile("v_mfma_f32_16x16x32_bf16 %0, %1, a[164:167], %0" : "+v"(acc[7][1]) : "v"(wf[5]));
  SCHED_FENCE();
  asm volatile("v_mfma_f32_16x16x32_bf16 %0, %1, a[160:163], %0" : "+v"(acc[8][0]) : "v"(wf[6]));
  SCHED_FENCE();
  wf[5] = lds_frag(lb[2] + 27648);
  SCHED_FENCE();
  asm volatile("v_mfma_f32_16x16x32_bf16 %0, %1, a[164:167], %0" : "+v"(acc[8][1]) : "v"(wf[6]));
  SCHED_FENCE();
  asm volatile("v_mfma_f32_16x16x32_bf16 %0, %1, a[168:171], %0" : "+v"(acc[7][0]) : "v"(wf[0]));
  SCHED_FENCE();
  wait_vm_barrier<15>();
  wf[6] = lds_frag(lb[0] + 0);
  SCHED_FENCE();
  asm volatile("v_mfma_f32_16x16x32_bf16 %0, %1, a[172:175], %0" : "+v"(acc[7][1]) : "v"(wf[0]));
  SCHED_FENCE();
  asm volatile("v_mfma_f32_16x16x32_bf16 %0, %1, a[168:171], %0" : "+v"(acc[8][0]) : "v"(wf[1]));
  SCHED_FENCE();
  wf[0] = lds_frag(lb[0] + 4096);
  SCHED_FENCE();
  asm volatile("v_mfma_f32_16x16x32_bf16 %0, %1, a[172:175], %0" : "+v"(acc[8][1]) : "v"(wf[1]));
  SCHED_FENCE();
  asm volatile("v_mfma_f32_16x16x32_bf16 %0, %1, a[176:179], %0" : "+v"(acc[7][0]) : "v"(wf[2]));
  SCHED_FENCE();
  wf[1] = lds_frag(lb[0] + 1024);
  SCHED_FENCE();
  asm volatile("v_mfma_f32_16x16x32_bf16 %0, %1, a[180:183], %0" : "+v"(acc[7][1]) : "v"(wf[2]));
  SCHED_FENCE();
  asm volatile("v_mfma_f32_16x16x32_bf16 %0, %1, a[176:179], %0" : "+v"(acc[8][0]) : "v"(wf[3]));
  SCHED_FENCE();
  wf[2] = lds_frag(lb[0] + 5120);
  SCHED_FENCE();
  asm volatile("v_mfma_f32_16x16x32_bf16 %0, %1, a[180:183], %0" : "+v"(acc[8][1]) : "v"(wf[3]));
  SCHED_FENCE();
  asm volatile("v_mfma_f32_16x16x32_bf16 %0, %1, a[184:187], %0" : "+v"(acc[7][0]) : "v"(wf[4]));
  SCHED_FENCE();
  wf[3] = lds_frag(lb[0] + 2048);
  SCHED_FENCE();
  asm volatile("v_mfma_f32_16x16x32_bf16 %0, %1, a[188:191], %0" : "+v"(acc[7][1]) : "v"(wf[4]));
  SCHED_FENCE();
  asm volatile("v_mfma_f32_16x16x32_bf16 %0, %1, a[184:187], %0" : "+v"(acc[8][0]) : "v"(wf[5]));
  SCHED_FENCE();
  wf[4] = lds_frag(lb[0] + 6144);
  SCHED_FENCE();
  asm volatile("v_mfma_f32_16x16x32_bf16 %0, %1, a[188:191], %0" : "+v"(acc[8][1]) : "v"(wf[5]));
  SCHED_FENCE();
  asm volatile("v_mfma_f32_16x16x32_bf16 %0, %1, a[192:195], %0" : "+v"(acc[7][0]) : "v"(wf[6]));
  SCHED_FENCE();
  wf[5] = lds_frag(lb[0] + 3072);
  SCHED_FENCE();
  asm volatile("v_mfma_f32_16x16x32_bf16 %0, %1, a[196:199], %0" : "+v"(acc[7][1]) : "v"(wf[6]));
  SCHED_FENCE();
  asm volatile("v_mfma_f32_16x16x32_bf16 %0, %1, a[192:195], %0" : "+v"(acc[8][0]) : "v"(wf[0]));
  SCHED_FENCE();
  wf[6] = lds_frag(lb[0] + 7168);
  SCHED_FENCE();
  asm volatile("v_mfma_f32_16x16x32_bf16 %0, %1, a[196:199], %0" : "+v"(acc[8][1]) : "v"(wf[0]));
  SCHED_FENCE();
  asm volatile("v_mfma_f32_16x16x32_bf16 %0, %1, a[200:203], %0" : "+v"(acc[7][0]) : "v"(wf[1]));
  SCHED_FENCE();
  wf[0] = lds_frag(lb[2] + 28672);
  SCHED_FENCE();
  asm volatile("v_mfma_f32_16x16x32_bf16 %0, %1, a[204:207], %0" : "+v"(acc[7][1]) : "v"(wf[1]));
  SCHED_FENCE();
  asm volatile("v_mfma_f32_16x16x32_bf16 %0, %1, a[200:203], %0" : "+v"(acc[8][0]) : "v"(wf[2]));
  SCHED_FENCE();
  wf[1] = lds_frag(lb[2] + 32768);
  SCHED_FENCE();
  asm volatile("v_mfma_f32_16x16x32_bf16 %0, %1, a[204:207], %0" : "+v"(acc[8][1]) : "v"(wf[2]));
  SCHED_FENCE();
  asm volatile("v_mfma_f32_16x16x32_bf16 %0, %1, a[208:211], %0" : "+v"(acc[7][0]) : "v"(wf[3]));
  SCHED_FENCE();
  wf[2] = lds_frag(lb[2] + 29696);
  SCHED_FENCE();
  asm volatile("v_mfma_f32_16x16x32_bf16 %0, %1, a[212:215], %0" : "+v"(acc[7][1]) : "v"(wf[3]));
  SCHED_FENCE();
  asm volatile("v_mfma_f32_16x16x32_bf16 %0, %1, a[208:211], %0" : "+v"(acc[8][0]) : "v"(wf[4]));
  SCHED_FENCE();
  wf[3] = lds_frag(lb[2] + 33792);
  SCHED_FENCE();
  asm volatile("v_mfma_f32_16x16x32_bf16 %0, %1, a[212:215], %0" : "+v"(acc[8][1]) : "v"(wf[4]));
  SCHED_FENCE();
  asm volatile("v_mfma_f32_16x16x32_bf16 %0, %1, a[216:219], %0" : "+v"(acc[7][0]) : "v"(wf[5]));
  SCHED_FENCE();
  wf[4] = lds_frag(lb[2] + 30720);
  SCHED_FENCE();
  asm volatile("v_mfma_f32_16x16x32_bf16 %0, %1, a[220:223], %0" : "+v"(acc[7][1]) : "v"(wf[5]));
  SCHED_FENCE();
  asm volatile("v_mfma_f32_16x16x32_bf16 %0, %1, a[216:219], %0" : "+v"(acc[8][0]) : "v"(wf[6]));
  SCHED_FENCE();
  wf[5] = lds_frag(lb[2] + 34816);
  SCHED_FENCE();
  asm volatile("v_mfma_f32_16x16x32_bf16 %0, %1, a[220:223], %0" : "+v"(acc[8][1]) : "v"(wf[6]));
  SCHED_FENCE();
  asm volatile("v_mfma_f32_16x16x32_bf16 %0, %1, a[224:227], %0" : "+v"(acc[7][0]) : "v"(wf[0]));
  SCHED_FENCE();
  wf[6] = lds_frag(lb[2] + 31744);
  SCHED_FENCE();
  asm volatile("v_mfma_f32_16x16x32_bf16 %0, %1, a[228:231], %0" : "+v"(acc[7][1]) : "v"(wf[0]));
  SCHED_FENCE();
  asm volatile("v_mfma_f32_16x16x32_bf16 %0, %1, a[224:227], %0" : "+v"(acc[8][0]) : "v"(wf[1]));
  SCHED_FENCE();
  wf[0] = lds_frag(lb[2] + 35840);
  SCHED_FENCE();
  asm volatile("v_mfma_f32_16x16x32_bf16 %0, %1, a[228:231], %0" : "+v"(acc[8][1]) : "v"(wf[1]));
  SCHED_FENCE();
  asm volatile("v_mfma_f32_16x16x32_bf16 %0, %1, a[232:235], %0" : "+v"(acc[7][0]) : "v"(wf[2]));
  SCHED_FENCE();
  wf[1] = lds_frag(lb[2] + 36864);
  SCHED_FENCE();
  asm volatile("v_mfma_f32_16x16x32_bf16 %0, %1, a[236:239], %0" : "+v"(acc[7][1]) : "v"(wf[2]));
  SCHED_FENCE();
  asm volatile("v_mfma_f32_16x16x32_bf16 %0, %1, a[232:235], %0" : "+v"(acc[8][0]) : "v"(wf[3]));
  SCHED_FENCE();
  wf[2] = lds_frag(lb[2] + 40960);
  SCHED_FENCE();
  asm volatile("v_mfma_f32_16x16x32_bf16 %0, %1, a[236:239], %0" : "+v"(acc[8][1]) : "v"(wf[3]));
  SCHED_FENCE();
  asm volatile("v_mfma_f32_16x16x32_bf16 %0, %1, a[240:243], %0" : "+v"(acc[7][0]) : "v"(wf[4]));
  SCHED_FENCE();
  wf[3] = lds_frag(lb[2] + 37888);
  SCHED_FENCE();
  asm volatile("v_mfma_f32_16x16x32_bf16 %0, %1, a[244:247], %0" : "+v"(acc[7][1]) : "v"(wf[4]));
  SCHED_FENCE();
  asm volatile("v_mfma_f32_16x16x32_bf16 %0, %1, a[240:243], %0" : "+v"(acc[8][0]) : "v"(wf[5]));
  SCHED_FENCE();
  wf[4] = lds_frag(lb[2] + 41984);
  SCHED_FENCE();
  asm volatile("v_mfma_f32_16x16x32_bf16 %0, %1, a[244:247], %0" : "+v"(acc[8][1]) : "v"(wf[5]));
  SCHED_FENCE();
  asm volatile("v_mfma_f32_16x16x32_bf16 %0, %1, a[248:251], %0" : "+v"(acc[7][0]) : "v"(wf[6]));
  SCHED_FENCE();
  wf[5] = lds_frag(lb[2] + 38912);
  G2_LOADP(0, 0);
  SCHED_FENCE();
  asm volatile("v_mfma_f32_16x16x32_bf16 %0, %1, a[252:255], %0" : "+v"(acc[7][1]) : "v"(wf[6]));
  SCHED_FENCE();
  asm volatile("v_mfma_f32_16x16x32_bf16 %0, %1, a[248:251], %0" : "+v"(acc[8][0]) : "v"(wf[0]));
  SCHED_FENCE();
  wf[6] = lds_frag(lb[2] + 43008);
  G2_LOADP(0, 1);
  SCHED_FENCE();
  asm volatile("v_mfma_f32_16x16x32_bf16 %0, %1, a[252:255], %0" : "+v"(acc[8][1]) : "v"(wf[0]));
  SCHED_FENCE();
  asm volatile("v_mfma_f32_16x16x32_bf16 %0, %1, %2, %0" : "+v"(acc[7][0]) : "v"(wf[1]), "v"(tv[0][0]));
  SCHED_FENCE();
  wf[0] = lds_frag(lb[2] + 39936);
  G2_LOADP(0, 2);
  SCHED_FENCE();
  asm volatile("v_mfma_f32_16x16x32_bf16 %0, %1, %2, %0" : "+v"(acc[7][1]) : "v"(wf[1]), "v"(tv[1][0]));
  SCHED_FENCE();
  asm volatile("v_mfma_f32_16x16x32_bf16 %0, %1, %2, %0" : "+v"(acc[8][0]) : "v"(wf[2]), "v"(tv[0][0]));
  SCHED_FENCE();
  wf[1] = lds_frag(lb[2] + 44032);
  G2_LOADP(1, 0);
  SCHED_FENCE();
  asm volatile("v_mfma_f32_16x16x32_bf16 %0, %1, %2, %0" : "+v"(acc[8][1]) : "v"(wf[2]), "v"(tv[1][0]));
  SCHED_FENCE();
  asm volatile("v_mfma_f32_16x16x32_bf16 %0, %1, %2, %0" : "+v"(acc[7][0]) : "v"(wf[3]), "v"(tv[0][1]));
  SCHED_FENCE();
  G2_LOADP(1, 1);
  SCHED_FENCE();
  asm volatile("v_mfma_f32_16x16x32_bf16 %0, %1, %2, %0" : "+v"(acc[7][1]) : "v"(wf[3]), "v"(tv[1][1]));
  SCHED_FENCE();
  asm volatile("v_mfma_f32_16x16x32_bf16 %0, %1, %2, %0" : "+v"(acc[8][0]) : "v"(wf[4]), "v"(tv[0][1]));
  SCHED_FENCE();
  G2_LOADP(1, 2);
  SCHED_FENCE();
  asm volatile("v_mfma_f32_16x16x32_bf16 %0, %1, %2, %0" : "+v"(acc[8][1]) : "v"(wf[4]), "v"(tv[1][1]));
  SCHED_FENCE();
  asm volatile("v_mfma_f32_16x16x32_bf16 %0, %1, %2, %0" : "+v"(acc[7][0]) : "v"(wf[5]), "v"(tv[0][2]));
  SCHED_FENCE();
  G2_LOADP(2, 0);
  SCHED_FENCE();
  asm volatile("v_mfma_f32_16x16x32_bf16 %0, %1, %2, %0" : "+v"(acc[7][1]) : "v"(wf[5]), "v"(tv[1][2]));
  SCHED_FENCE();
  asm volatile("v_mfma_f32_16x16x32_bf16 %0, %1, %2, %0" : "+v"(acc[8][0]) : "v"(wf[6]), "v"(tv[0][2]));
  SCHED_FENCE();
  G2_LOADP(2, 1);
  SCHED_FENCE();
  asm volatile("v_mfma_f32_16x16x32_bf16 %0, %1, %2, %0" : "+v"(acc[8][1]) : "v"(wf[6]), "v"(tv[1][2]));
  SCHED_FENCE();
  asm volatile("v_mfma_f32_16x16x32_bf16 %0, %1, %2, %0" : "+v"(acc[7][0]) : "v"(wf[0]), "v"(tv[0][3]));
  SCHED_FENCE();
  G2_LOADP(2, 2);
  SCHED_FENCE();
  asm volatile("v_mfma_f32_16x16x32_bf16 %0, %1, %2, %0" : "+v"(acc[7][1]) : "v"(wf[0]), "v"(tv[1][3]));
  SCHED_FENCE();
  asm volatile("v_mfma_f32_16x16x32_bf16 %0, %1, %2, %0" : "+v"(acc[8][0]) : "v"(wf[1]), "v"(tv[0][3]));
  SCHED_FENCE();
  asm volatile("v_mfma_f32_16x16x32_bf16 %0, %1, %2, %0" : "+v"(acc[8][1]) : "v"(wf[1]), "v"(tv[1][3]));
  SCHED_FENCE();
  const unsigned vo = t16 * (D_ * 2) + g * 8;
  const unsigned so0 = (mt2 * 128 + w * 32) * (D_ * 2) + nq * 288;
  unsigned so[3] = {so0 + ksl * 64, so0 + ksl * 64 + 32, (w == ksl) ? so0 + 256 : 0x40000000u};
  for (unsigned spins = 0; spins < (1u << 16); ++spins) {
    unsigned m = 0;
#pragma unroll
    for (int a = 0; a < 3; ++a)
#pragma unroll
      for (int i = 0; i < 3; ++i) m = max(m, max(max(rv[a][i][0], rv[a][i][1]), max(rv[a][i][2], rv[a][i][3])));
    if (!__any(m == ~0u)) break;
    asm volatile("" ::: "memory");
#pragma unroll
    for (int a = 0; a < 3; ++a)
#pragma unroll
      for (int i = 0; i < 3; ++i) G2_LOADP(a, i);
  }
#pragma unroll
  for (int a = 0; a < 3; ++a)
#pragma unroll
    for (int b = 0; b < 2; ++b) {
      f32x4 s = acc[a < 2 ? 7 + a : 6][b];
#pragma unroll
      for (int i = 0; i < 3; ++i) {
        s[0] += bf_lo(rv[a][i][2 * b]); s[1] += bf_hi(rv[a][i][2 * b]);
        s[2] += bf_lo(rv[a][i][2 * b + 1]); s[3] += bf_hi(rv[a][i][2 * b + 1]);
      }
      const float mu = __uint_as_float(mv[b]);
      u32x2 o;
      o[0] = pack_bf16((bf_lo(hv[a][b][0]) + mu) + s[0], (bf_hi(hv[a][b][0]) + mu) + s[1]);
      o[1] = pack_bf16((bf_lo(hv[a][b][1]) + mu) + s[2], (bf_hi(hv[a][b][1]) + mu) + s[3]);
      __builtin_amdgcn_raw_buffer_store_b64(o, ro, vo, so[a] + b * 16 * (D_ * 2), 0);
    }
  }
  asm volatile("ffn_code_end_lbl:" ::: "memory");
}

extern "C" void kernel_launch(void* const* d_in, const int* in_sizes, int n_in, void* d_out, int out_size, void* d_ws,
                              size_t ws_size, hipStream_t stream) {
  const bf16_t* x = (const bf16_t*)d_in[0];
  const bf16_t* wqkv = (const bf16_t*)d_in[1];
  const bf16_t* wo = (const bf16_t*)d_in[2];
  const bf16_t* wg = (const bf16_t*)d_in[4];
  const bf16_t* wu = (const bf16_t*)d_in[5];
  const bf16_t* wd = (const bf16_t*)d_in[6];
  char* ws = (char*)d_ws;
  bf16_t* o_frag = (bf16_t*)(ws + WS_O);
  float* hmean = (float*)(ws + WS_H);
  bf16_t* hc = (bf16_t*)(ws + WS_HC);
  bf16_t* act = (bf16_t*)(ws + WS_ACT);
  float* part = (float*)(ws + WS_PART);
  float* meanx = (float*)(ws + WS_MEANX);
  bf16_t* wbar = (bf16_t*)(ws + WS_WBAR);
  float* ssq = (float*)(ws + WS_SSQ);
  char* wf = ws + WS_WF;
  unsigned* cnt = (unsigned*)(ws + WS_CNT);
  k_qkv_attn<<<256, 768, 0, stream>>>(x, wqkv, wo, wg, wu, part, o_frag, meanx, (bf16_t*)ssq, (bf16_t*)wf, cnt, wd);
  k_ffn<<<256, 256, 0, stream>>>(x, (const bf16_t*)(wf + WFO_OFF), o_frag, meanx, wbar, hc, ssq, hmean, wg, wu, wd,
                                 (bf16_t*)(wf + WF2_OFF), act, part, cnt, (bf16_t*)d_out);
}
```
